```python
import torch

def _softmax(x, axis=-1):
    return torch.softmax(x, dim=axis)

def _gelu(x):
    return torch.nn.functional.gelu(x, approximate='tanh')

def _silu(x):
    return torch.nn.functional.silu(x)

def _sigmoid(x):
    return torch.sigmoid(x)

def _layernorm(x, axis=-1, eps=1e-05):
    m = x.mean(dim=axis, keepdim=True)
    v = x.var(dim=axis, keepdim=True, unbiased=False)
    return (x - m) / torch.sqrt(v + eps)

def _scatter_add(tgt, idx, src, axis=0):
    return tgt.clone().index_add_(axis, idx, src)

def _segment_sum(src, seg, n):
    out = torch.zeros((n,) + src.shape[1:], dtype=src.dtype)
    return out.index_add_(0, seg, src)

def _pad(x, pw):
    flat = tuple((v for pair in reversed(pw) for v in pair))
    return torch.nn.functional.pad(x, flat)
kernel_name = 'interleave_layers__memory_aug_recurrent_mem_tr__dense_activation_relu__prompt'

def setup_inputs():

    _w = lambda a: (lambda t: t.to(torch.bfloat16) if t.is_floating_point() else t)(torch.as_tensor(a))

    def _A_setup():
        B, S, Dm = (1, 2048, 640)
        H, Dh = (32, 128)
        M, seg = (8, 32)
        seg = min(seg, S)
        S = S // seg * seg
        return {'x': _w((lambda *s: torch.randn(*s, dtype=torch.bfloat16) * 0.05)(B, S, Dm)), 'mem0': _w((lambda *s: torch.randn(*s, dtype=torch.bfloat16) * 0.8)(B, M, Dm)), 'Wq': _w((lambda *s: torch.randn(*s, dtype=torch.bfloat16) * 0.8)(Dm, H, Dh) * Dm ** (-0.5)), 'Wk': _w((lambda *s: torch.randn(*s, dtype=torch.bfloat16) * 0.8)(Dm, H, Dh) * Dm ** (-0.5)), 'Wv': _w((lambda *s: torch.randn(*s, dtype=torch.bfloat16) * 0.8)(Dm, H, Dh) * Dm ** (-0.5)), 'Wo': _w((lambda *s: torch.randn(*s, dtype=torch.bfloat16) * 0.8)(H, Dh, Dm) * (H * Dh) ** (-0.5)), 'seg_len': seg}
    _A_in = _A_setup()

    def _B_setup():
        B, S, D, Dff = (1, 2048, 640, 1728)
        return {'x': _w((lambda *s: torch.randn(*s, dtype=torch.bfloat16) * 0.05)(B, S, D)), 'w1': _w((lambda *s: torch.randn(*s, dtype=torch.bfloat16) * 0.05)(D, Dff)), 'w2': _w((lambda *s: torch.randn(*s, dtype=torch.bfloat16) * 0.05)(Dff, D))}
    _B_in = _B_setup()
    return {'A_x': _A_in['x'], 'A_mem0': _A_in['mem0'], 'A_Wq': _A_in['Wq'], 'A_Wk': _A_in['Wk'], 'A_Wv': _A_in['Wv'], 'A_Wo': _A_in['Wo'], 'A_seg_len': _A_in['seg_len'], 'B_x': _B_in['x'], 'B_w1': _B_in['w1'], 'B_w2': _B_in['w2']}

def reference(A_x, A_mem0, A_Wq, A_Wk, A_Wv, A_Wo, A_seg_len, B_x, B_w1, B_w2):

    def _A_ref(x, mem0, Wq, Wk, Wv, Wo, seg_len):
        B, S, Dm = x.shape
        H, Dh = (Wq.shape[1], Wq.shape[2])
        M = mem0.shape[1]
        scale = Dh ** (-0.5)
        n_seg = S // seg_len
        L = M + seg_len
        idx = torch.arange(L)
        mask = (idx[:, None] < M) | (idx[:, None] >= idx[None, :])
        mem = mem0
        outs = []
        for s in range(n_seg):
            xs = x[:, s * seg_len:(s + 1) * seg_len, :].clone()
            h = torch.concatenate([mem, xs], axis=1)
            q = torch.einsum('btd,dhe->bhte', h, Wq)
            k = torch.einsum('btd,dhe->bhte', h, Wk)
            v = torch.einsum('btd,dhe->bhte', h, Wv)
            scores = torch.einsum('bhqe,bhke->bhqk', q, k) * scale
            scores = torch.where(mask[None, None], scores, -1000000000.0)
            p = _softmax(scores, axis=-1)
            o = torch.einsum('bhqk,bhke->bhqe', p, v)
            o = torch.einsum('bhte,hed->btd', o, Wo)
            mem = o[:, :M, :].clone()
            outs.append(o[:, M:, :])
        return torch.concatenate(outs, axis=1)

    def _B_ref(x, w1, w2):
        h = torch.einsum('bsd,df->bsf', x, w1)
        zero = torch.zeros_like(h)
        h = torch.maximum(h, zero)
        return torch.einsum('bsf,fd->bsd', h, w2)
    x = A_x
    x = x + _A_ref(_layernorm(x, axis=-1), A_mem0, A_Wq, A_Wk, A_Wv, A_Wo, A_seg_len)
    x = x + _B_ref(_layernorm(x, axis=-1), B_w1, B_w2)
    x = x + _A_ref(_layernorm(x, axis=-1), A_mem0, A_Wq, A_Wk, A_Wv, A_Wo, A_seg_len)
    x = x + _B_ref(_layernorm(x, axis=-1), B_w1, B_w2)
    return x
```

```cpp
#include <hip/hip_runtime.h>
#include <hip/hip_bf16.h>

#define GAS __attribute__((address_space(1)))
#define LAS __attribute__((address_space(3)))
typedef short bf16x8 __attribute__((ext_vector_type(8)));
typedef short s16x4 __attribute__((ext_vector_type(4)));
typedef float f32x4 __attribute__((ext_vector_type(4)));
typedef float f32x2 __attribute__((ext_vector_type(2)));
typedef __bf16 bf2 __attribute__((ext_vector_type(2)));
typedef unsigned v4u __attribute__((ext_vector_type(4)));
typedef unsigned v2u __attribute__((ext_vector_type(2)));
typedef unsigned char u8;
typedef LAS u8* ldsp;
typedef __amdgpu_buffer_rsrc_t rsrc_t;

#define LAG 8
#define PRE1 8
#define PRE2 6
#define PRE2X 4
#define M1 24
#define D2 88
#define PSL 2
#define DLY 30
#define LNMASK 0x800000080008088Bull
constexpr int D = 640, H = 32, E = 128, NSEG = 64, DFF = 1728;
constexpr int NSTEP = NSEG + LAG;
constexpr unsigned SENT = 0xFFFFFFFFu;
constexpr int SC1 = 16;
constexpr int CODE_BYTES = 60 * 1024;
constexpr int SPINCAP = 1 << 17;

constexpr int IMG = 20480;
constexpr size_t OFF_CTL = 0;
constexpr size_t OFF_PROG = 16384;
constexpr size_t OFF_MI = 458752;
constexpr size_t OFF_CLAIM = 768 << 10;
constexpr size_t OFF_RDY = OFF_CLAIM + 256 * 64;
constexpr size_t OFF_PR = 2 << 20;
constexpr size_t OFF_KM = OFF_PR + (8 << 20);
#define FW 9
constexpr int RK = 4, RS = 16, NPF = FW + 1;
static_assert(LAG < RS, "ring");
constexpr size_t OFF_KX = 120 << 20;
constexpr int CELL = 16384, CO = 1536;
constexpr size_t OFF_TP = 16 << 20;
constexpr size_t OFF_O = 80 << 20;
constexpr size_t OFF_XF = 12 << 20;
constexpr size_t OFF_XN = 96 << 20;
constexpr size_t OFF_XR = 108 << 20;
constexpr int NPAGE = 64;
constexpr size_t OFF_END = 128 << 20;
constexpr unsigned WSB = 128u << 20;
static_assert(OFF_END <= (254u << 20), "workspace");
constexpr int PG_HEAD = 64;
constexpr int PG_HINT = 192;
constexpr int PG_LN0 = 360;
constexpr int PG_O = 512;
constexpr int PG_WO = 1024;
constexpr int PG_FFN = 1152;
constexpr int PG_X1 = 1280;
constexpr int PG_XN = 1408;
constexpr int PG_FIN = 1472;
constexpr int PG_N = 1504;
__device__ __forceinline__ int pg(int i) { return (i >> 6) * 16384 + (i & 63) * 128; }
static_assert(OFF_PROG + (PG_N + 63) / 64 * 16384 <= OFF_MI, "control block");
__device__ __forceinline__ int pol_word(int lane) { return (int)(OFF_RDY - OFF_PROG) + lane * 128; }
constexpr unsigned PGB = OFF_RDY + 8192 - OFF_PROG;

struct Params {
  const u8 *x, *mem0, *Wq, *Wk, *Wv, *Wo, *w1, *w2;
  u8* out;
  u8* ws;
  unsigned long long* dbg;
  unsigned long long tag;
  int mask, role;
};

constexpr int OOB = 0x7FFFFFF0;
__device__ __forceinline__ rsrc_t mkrsrc(const void* p, int n = 0x7FFFFF00) { return __builtin_amdgcn_make_buffer_rsrc((void*)p, 0, n, 0x00020000); }
__device__ __forceinline__ v4u ld16(rsrc_t r, int voff, int soff) { return __builtin_amdgcn_raw_buffer_load_b128(r, voff, soff, SC1); }
__device__ __forceinline__ v2u ld8(rsrc_t r, int voff, int soff) { return __builtin_amdgcn_raw_buffer_load_b64(r, voff, soff, SC1); }
__device__ __forceinline__ unsigned ld4(rsrc_t r, int voff, int soff) { return __builtin_amdgcn_raw_buffer_load_b32(r, voff, soff, SC1); }
__device__ __forceinline__ void st16(v4u v, rsrc_t r, int voff, int soff) { __builtin_amdgcn_raw_buffer_store_b128(v, r, voff + soff, 0, SC1); }
__device__ __forceinline__ void st8(v2u v, rsrc_t r, int voff, int soff) { __builtin_amdgcn_raw_buffer_store_b64(v, r, voff, soff, SC1); }
__device__ __forceinline__ void st4(unsigned v, rsrc_t r, int voff, int soff) { __builtin_amdgcn_raw_buffer_store_b32(v, r, voff, soff, SC1); }
__device__ __forceinline__ unsigned pk(float a, float b) { f32x2 f = {a, b}; return __builtin_bit_cast(unsigned, __builtin_convertvector(f, bf2)); }
__device__ __forceinline__ unsigned san(unsigned x) { return x < 0xFFFFFFFEu ? x : 0xFFFFFFFEu; }
__device__ __forceinline__ v2u pk4(f32x4 a) { return v2u{pk(a[0], a[1]), pk(a[2], a[3])}; }
__device__ __forceinline__ v4u pk8(f32x4 a, f32x4 b) { return v4u{pk(a[0], a[1]), pk(a[2], a[3]), pk(b[0], b[1]), pk(b[2], b[3])}; }
__device__ __forceinline__ v4u san4(v4u v) { return v4u{san(v.x), san(v.y), san(v.z), san(v.w)}; }
__device__ __forceinline__ v2u san2(v2u v) { return v2u{san(v.x), san(v.y)}; }
__device__ __forceinline__ bool ok4(v4u v) { return ((v.x != SENT) & (v.y != SENT)) & ((v.z != SENT) & (v.w != SENT)); }
__device__ __forceinline__ bool ok2(v2u v) { return (v.x != SENT) & (v.y != SENT); }
__device__ __forceinline__ bf16x8 asf(v4u v) { return __builtin_bit_cast(bf16x8, v); }
__device__ __forceinline__ f32x4 mfma32(bf16x8 a, bf16x8 b, f32x4 c) { return __builtin_amdgcn_mfma_f32_16x16x32_bf16(a, b, c, 0, 0, 0); }
__device__ __forceinline__ f32x4 mfma16(v2u a, v2u b, f32x4 c) { return __builtin_amdgcn_mfma_f32_16x16x16bf16_1k(__builtin_bit_cast(s16x4, a), __builtin_bit_cast(s16x4, b), c, 0, 0, 0); }
__device__ __forceinline__ s16x4 trd(ldsp p) { return __builtin_amdgcn_ds_read_tr16_b64_v4i16((LAS s16x4*)p); }
__device__ __forceinline__ bf16x8 trfrag(ldsp p, int hi) { s16x4 a = trd(p), b = trd(p + hi); return __builtin_shufflevector(a, b, 0, 1, 2, 3, 4, 5, 6, 7); }
__device__ __forceinline__ void dma16(const u8* g, ldsp l) { __builtin_amdgcn_global_load_lds((const GAS unsigned*)g, (LAS unsigned*)l, 16, 0, 0); }
__device__ __forceinline__ float xg_max(float x) {
  auto s = __builtin_amdgcn_permlane16_swap(__float_as_uint(x), __float_as_uint(x), false, false);
  x = fmaxf(__uint_as_float(s[0]), __uint_as_float(s[1]));
  auto t = __builtin_amdgcn_permlane32_swap(__float_as_uint(x), __float_as_uint(x), false, false);
  return fmaxf(__uint_as_float(t[0]), __uint_as_float(t[1]));
}
__device__ __forceinline__ float xg_sum(float x) {
  auto s = __builtin_amdgcn_permlane16_swap(__float_as_uint(x), __float_as_uint(x), false, false);
  x = __uint_as_float(s[0]) + __uint_as_float(s[1]);
  auto t = __builtin_amdgcn_permlane32_swap(__float_as_uint(x), __float_as_uint(x), false, false);
  return __uint_as_float(t[0]) + __uint_as_float(t[1]);
}
#define VM0() asm volatile("s_waitcnt vmcnt(0)" ::: "memory")
#define LGKM0() asm volatile("s_waitcnt lgkmcnt(0)" ::: "memory")
#define BAR() do { asm volatile("s_waitcnt lgkmcnt(0)" ::: "memory"); __builtin_amdgcn_s_barrier(); asm volatile("" ::: "memory"); } while (0)
#define SB() __builtin_amdgcn_sched_barrier(0)
#define PIN_A(x) asm volatile("" : "+a"(x))
#define PIN_V(x) asm volatile("" : "+v"(x))
#define VMC(n) asm volatile("s_waitcnt vmcnt(" #n ")" ::: "memory")
#define LGC(n) asm volatile("s_waitcnt lgkmcnt(" #n ")" ::: "memory")
#define SBAR() do { asm volatile("" ::: "memory"); __builtin_amdgcn_s_barrier(); asm volatile("" ::: "memory"); } while (0)
#define MF0(d, ca, a, cb, b) asm volatile("v_mfma_f32_16x16x32_bf16 %0, %1, %2, 0" : "=&v"(d) : ca(a), cb(b))
#define MFA(d, ca, a, cb, b) asm volatile("v_mfma_f32_16x16x32_bf16 %0, %1, %2, %0" : "+v"(d) : ca(a), cb(b))
#define MFH(d, a, b) asm volatile("v_mfma_f32_16x16x16_bf16 %0, %1, %2, %0" : "+v"(d) : "v"(a), "v"(b))
#define NOP1(n, a) asm volatile("s_nop " #n : "+v"(a))
#define NOP2(n, a, b) asm volatile("s_nop " #n : "+v"(a), "+v"(b))
#define NOP3(n, a, b, c) asm volatile("s_nop " #n : "+v"(a), "+v"(b), "+v"(c))
__device__ __forceinline__ void dma_sc1(rsrc_t r, int voff, int soff, unsigned ldsaddr) {
  asm volatile("s_mov_b32 m0, %0\n\ts_nop 0\n\tbuffer_load_dwordx4 %1, %2, %3 offen sc1 lds" :: "s"(ldsaddr), "v"(voff), "s"(r), "s"(soff) : "m0", "memory");
}
__device__ __forceinline__ unsigned umax3(unsigned a, unsigned b, unsigned c) { return max(max(a, b), c); }
__device__ __forceinline__ unsigned chk4(unsigned m, v4u v) { return umax3(umax3(m, v.x, v.y), v.z, v.w); }
__device__ __forceinline__ float vmax(float a, float b) { float d; asm("v_max_f32 %0, %1, %2" : "=v"(d) : "v"(a), "v"(b)); return d; }
__device__ __forceinline__ float vmax3(float a, float b, float c) { float d; asm("v_max3_f32 %0, %1, %2, %3" : "=v"(d) : "v"(a), "v"(b), "v"(c)); return d; }
__device__ __forceinline__ float xg_max2(float x) {
  float y;
  asm("v_mov_b32 %1, %0\n\ts_nop 1\n\tv_permlane16_swap_b32 %0, %1\n\ts_nop 0\n\tv_max_f32 %0, %0, %1\n\tv_mov_b32 %1, %0\n\ts_nop 1\n\tv_permlane32_swap_b32 %0, %1\n\ts_nop 0\n\tv_max_f32 %0, %0, %1"
      : "+v"(x), "=&v"(y));
  return x;
}
__device__ __forceinline__ unsigned nonan(unsigned x) {
  typedef unsigned short u16x2 __attribute__((ext_vector_type(2)));
  const u16x2 lim = {0xFFFE, 0xFFFE};
  return __builtin_bit_cast(unsigned, __builtin_elementwise_min(__builtin_bit_cast(u16x2, x), lim));
}
__device__ __forceinline__ unsigned rtc() { unsigned long long t; asm volatile("s_memrealtime %0\n\ts_waitcnt lgkmcnt(0)" : "=s"(t) :: "memory"); return (unsigned)t; }
__device__ __forceinline__ void wait_until(unsigned t) { for (int i = 0; i < 4096 && (int)(rtc() - t) < 0; ++i) {} }
__device__ __forceinline__ int wave_max(int x) {
  x = max(x, __builtin_amdgcn_update_dpp(x, x, 0xB1, 0xf, 0xf, false));
  x = max(x, __builtin_amdgcn_update_dpp(x, x, 0x4E, 0xf, 0xf, false));
  x = max(x, __builtin_amdgcn_update_dpp(x, x, 0x141, 0xf, 0xf, false));
  x = max(x, __builtin_amdgcn_update_dpp(x, x, 0x140, 0xf, 0xf, false));
  return max(max(__builtin_amdgcn_readlane(x, 0), __builtin_amdgcn_readlane(x, 16)), max(__builtin_amdgcn_readlane(x, 32), __builtin_amdgcn_readlane(x, 48)));
}
__device__ __forceinline__ int lane_id() {
  int l; asm volatile("v_mbcnt_lo_u32_b32 %0, -1, 0\n\tv_mbcnt_hi_u32_b32 %0, -1, %0" : "=v"(l)); __builtin_assume(l >= 0 && l < 64); return l;
}
__device__ __forceinline__ bool give_up(int& sp, bool& dead) {
  if (++sp >= SPINCAP) dead = true;
  return dead;
}
#define STAMP(i)
#define TR(i)

template <int RB>
__device__ __forceinline__ void stage32k(const u8* src, int rstride, ldsp dst, int w, int lane) {
  constexpr int LPR = RB / 16, RPI = 64 / LPR;
#pragma unroll
  for (int i = 0; i < 8; ++i) {
    const int row = (w * 8 + i) * RPI + lane / LPR;
    const int sw = (RB == 256) ? ((row & 7) << 1) : (((row >> 1) & 3) << 1);
    dma16(src + (size_t)row * rstride + (((lane % LPR) ^ sw) << 4), dst + (w * 8 + i) * 1024);
  }
}
template <int RB>
__device__ __forceinline__ int trbase(int ct, int lane) {
  const int g = lane >> 4, q = (lane >> 2) & 3, p = lane & 3, row = 4 * g + q;
  const int sw = (RB == 256) ? ((row & 7) << 1) : (((row >> 1) & 3) << 1);
  return row * RB + (((2 * ct + (p >> 1)) ^ sw) << 4) + 8 * (p & 1);
}

constexpr int CL_MEM = 0, CL_Q = CL_MEM + IMG, CL_SX = CL_Q + 4096, CL_SM = CL_SX + 4096, CL_NINF = CL_SM + 4096, CL_O = CL_NINF + 1024,
              CL_RED = CL_O + 2048, CL_DUMMY = CL_RED + 2560, CL_WV = 40960, CL_WO = 65536, CL_END = CL_WO + 81920;
static_assert(CL_DUMMY + 1024 <= CL_WV && CL_RED == 35840 && CL_O == 33792, "lds");
constexpr int CL_STG = 0;
constexpr int LDS_BYTES = 160 * 1024;
static_assert(CL_END <= LDS_BYTES && CL_STG + 65536 <= LDS_BYTES, "lds");

typedef int i32x16 __attribute__((ext_vector_type(16)));
__device__ __forceinline__ v4u rawrsrc(const void* p, unsigned n) {
  const unsigned long long a = (unsigned long long)p;
  return v4u{(unsigned)__builtin_amdgcn_readfirstlane((unsigned)a), (unsigned)__builtin_amdgcn_readfirstlane((unsigned)(a >> 32) & 0xFFFFu), (unsigned)__builtin_amdgcn_readfirstlane(n), 0x00020000u};
}
__device__ __forceinline__ int uni(int x) { return __builtin_amdgcn_readfirstlane(x); }
__device__ __forceinline__ unsigned long long uni64(unsigned long long a) {
  return (unsigned long long)(unsigned)__builtin_amdgcn_readfirstlane((unsigned)a) | ((unsigned long long)(unsigned)__builtin_amdgcn_readfirstlane((unsigned)(a >> 32)) << 32);
}
#define R10(p, n) p #n "0", p #n "1", p #n "2", p #n "3", p #n "4", p #n "5", p #n "6", p #n "7", p #n "8", p #n "9"
#define R0_199(p) p "0", p "1", p "2", p "3", p "4", p "5", p "6", p "7", p "8", p "9", R10(p, 1), R10(p, 2), R10(p, 3), R10(p, 4), R10(p, 5), R10(p, 6), R10(p, 7), R10(p, 8), R10(p, 9), \
                  R10(p, 10), R10(p, 11), R10(p, 12), R10(p, 13), R10(p, 14), R10(p, 15), R10(p, 16), R10(p, 17), R10(p, 18), R10(p, 19)
#define R0_179(p) p "0", p "1", p "2", p "3", p "4", p "5", p "6", p "7", p "8", p "9", R10(p, 1), R10(p, 2), R10(p, 3), R10(p, 4), R10(p, 5), R10(p, 6), R10(p, 7), R10(p, 8), R10(p, 9), \
                  R10(p, 10), R10(p, 11), R10(p, 12), R10(p, 13), R10(p, 14), R10(p, 15), R10(p, 16), R10(p, 17)
#define A_ALL R0_199("a"), R10("a", 20), R10("a", 21), R10("a", 22), R10("a", 23), R10("a", 24), "a250", "a251", "a252", "a253", "a254", "a255"

__device__ __forceinline__ void chain_role(const Params& p, int w, int c) {
  const int lane = lane_id(), tid = w * 64 + lane;
  const int h = c >> 1, half = c & 1, g = lane >> 4, r = lane & 15;
  u8* ws = p.ws;
  v2u m0 = {0u, 0u};
  const int u_red = c * 20 + (tid >> 1), r_red = u_red & 15;
  if (tid < 40) {
    const int T = u_red >> 6, gg = (u_red >> 4) & 3;
    m0 = san2(*(const GAS v2u*)(p.mem0 + (r_red & 7) * 1280 + (32 * T + 16 * (tid & 1) + 4 * gg) * 2));
  }
  i32x16 mi;
  v4u mj;
#pragma unroll
  for (int i = 0; i < 5; ++i) {
    const u8* q = p.mem0 + (r & 7) * 1280 + (32 * (w + 4 * i) + 4 * g) * 2;
    const v2u a = *(const GAS v2u*)q, b = *(const GAS v2u*)(q + 32);
    if (i < 4) { mi[4 * i] = a.x; mi[4 * i + 1] = a.y; mi[4 * i + 2] = b.x; mi[4 * i + 3] = b.y; } else mj = v4u{a.x, a.y, b.x, b.y};
  }
  const bool lo = r < 8;
  const int l16 = lane * 16;
  const int rot = c >> 4;
  const int slot = max((c * 20 / 256 + 5 - w) % 5, ((c * 20 + 19) / 256 + 5 - w) % 5);
  i32x16 c0, c1, c2;
  c0[0] = tid * 16; c0[1] = l16; c0[2] = l16 + CL_SX + (lo ? 0 : 2048); c0[3] = l16 + (((g >> 1) == (r >> 3)) ? CL_SM : CL_NINF);
  c0[4] = CL_WO + w * 20480 + l16; c0[5] = CL_O + (w >> 1) * 1024 + l16 + (w & 1) * 8;
#pragma unroll
  for (int i = 0; i < 5; ++i) { const int u = (4 * ((i + rot) % 5) + w) * 64 + lane; c0[6 + i] = (u / 20) * 131072 + (c >> 4) * 16384 + 1536 + (c & 15) * 320 + (u % 20) * 16; }
  c0[11] = c * 131072 + w * 16384 + 1536 + g * 320 + r * 16;
  c0[12] = c * 131072 + w * 16384 + 1536 + (4 * (r & 3) + g) * 320 + 256 + (r >> 2) * 16;
  c0[13] = ((w >> 1) == half ? 0 : 0x20000000) + w * 512 + (g * 8 + (r & 7)) * 16;
  c0[14] = 2048 + (4 * half + w) * 256 + ((g & 1) * 16 + r) * 8;
  c0[15] = lane < 32 ? CL_RED + w * 640 + (r * 2 + g) * 16 : CL_DUMMY + l16;
  c1[0] = lo ? CL_RED + w * 640 + 512 + (g * 8 + r) * 4 : CL_DUMMY + l16;
  c1[1] = pg(PG_HEAD + h * 4 + (lane & 1) * 2 + ((lane & 1) ? (LAG & 1) : 0));
  c1[2] = m0.x; c1[3] = m0.y;
#pragma unroll
  for (int j = 0; j < 4; ++j) c1[4 + j] = (r == 2 * j) ? 0x3F80 : ((r == 2 * j + 1) ? 0x3F800000 : 0);
  c1[8] = CL_WV + w * 5120 + l16;
  c1[9] = w > 1 ? OOB : lane < 40 ? ((c * 320 + lane * 8) >> 13) * 16384 + ((c * 320 + lane * 8) & 8191) : (tid == 104 ? 36864 + c * 32 : OOB);
  c1[10] = tid == 0 ? c * 32 : OOB;
  c1[11] = lane * 32;
  c1[12] = c1[13] = c1[14] = c1[15] = 0;
  c2[0] = (16 * w + g) * 8192 + ((r ^ (g << 1)) << 4); c2[1] = (16 * w + 4 + g) * 8192 + ((r ^ ((4 + g) << 1)) << 4);
  c2[2] = (32 * w + (lane >> 3)) * 8192 + (((lane & 7) ^ (((lane >> 4) & 3) << 1)) << 4);
  c2[3] = (16 * w + g) * 1280 + ((r ^ (g << 1)) << 4); c2[4] = (16 * w + 4 + g) * 1280 + ((r ^ ((4 + g) << 1)) << 4);
  c2[5] = trbase<256>(2 * w, lane); c2[6] = trbase<256>(2 * w + 1, lane); c2[7] = trbase<128>(w, lane);
  c2[8] = CL_WO + w * 4096 + l16;
#pragma unroll
  for (int j = 9; j < 16; ++j) c2[j] = 0;
  c2[12] = pol_word(lane);
  c1[12] = tid < 2 ? pg(PG_HINT + c * 2 + tid) : OOB;
  v4u sMI = rawrsrc(ws + OFF_MI, 0x7FFFFF00u), sPR = rawrsrc(ws + OFF_PR, 0x7FFFFF00u), sKM = rawrsrc(ws + OFF_KM, (2 << 20) - 8192), sPG = rawrsrc(ws + OFF_PROG, PGB);
  v4u sQ = rawrsrc(p.Wq + h * 256, 640 * 8192 - h * 256), sK = rawrsrc(p.Wk + h * 256, 640 * 8192 - h * 256), sV = rawrsrc(p.Wv + h * 256 + half * 128, 640 * 8192 - h * 256 - half * 128);
  v4u sO = rawrsrc(p.Wo + (size_t)(h * 128 + half * 64) * 1280, 64 * 1280);
  int sci = c * 131072 + w * 16384 + 1536;
  unsigned long long kxb = uni64((unsigned long long)(ws + OFF_KX + (size_t)h * 2 * RK * 32768));
  const unsigned long long mlo = __ballot(lo), mgh = __ballot(g >= 2), mred = __ballot(r_red >= 8);
  const unsigned long long dbg = uni64((unsigned long long)p.dbg + (c * 4 + w) * 32 - 8192);
  asm volatile(
" s_nop 4\n"
" s_mov_b32 s90, 0\n"
" s_lshl_b32 s77, s69, 12\n"
" s_add_i32 m0, s77, 65536\n"
" s_mov_b32 s76, 0\n"
" buffer_load_dwordx4 v208, s[56:59], s76 offen lds\n"
" s_add_i32 m0, s77, 66560\n"
" s_mov_b32 s76, 0\n"
" buffer_load_dwordx4 v209, s[56:59], s76 offen lds\n"
" s_add_i32 m0, s77, 67584\n"
" s_mov_b32 s76, 65536\n"
" buffer_load_dwordx4 v208, s[56:59], s76 offen lds\n"
" s_add_i32 m0, s77, 68608\n"
" s_mov_b32 s76, 65536\n"
" buffer_load_dwordx4 v209, s[56:59], s76 offen lds\n"
" s_add_i32 m0, s77, 81920\n"
" s_mov_b32 s76, 524288\n"
" buffer_load_dwordx4 v208, s[56:59], s76 offen lds\n"
" s_add_i32 m0, s77, 82944\n"
" s_mov_b32 s76, 524288\n"
" buffer_load_dwordx4 v209, s[56:59], s76 offen lds\n"
" s_add_i32 m0, s77, 83968\n"
" s_mov_b32 s76, 589824\n"
" buffer_load_dwordx4 v208, s[56:59], s76 offen lds\n"
" s_add_i32 m0, s77, 84992\n"
" s_mov_b32 s76, 589824\n"
" buffer_load_dwordx4 v209, s[56:59], s76 offen lds\n"
" s_add_i32 m0, s77, 98304\n"
" s_mov_b32 s76, 1048576\n"
" buffer_load_dwordx4 v208, s[56:59], s76 offen lds\n"
" s_add_i32 m0, s77, 99328\n"
" s_mov_b32 s76, 1048576\n"
" buffer_load_dwordx4 v209, s[56:59], s76 offen lds\n"
" s_add_i32 m0, s77, 100352\n"
" s_mov_b32 s76, 1114112\n"
" buffer_load_dwordx4 v208, s[56:59], s76 offen lds\n"
" s_add_i32 m0, s77, 101376\n"
" s_mov_b32 s76, 1114112\n"
" buffer_load_dwordx4 v209, s[56:59], s76 offen lds\n"
" s_add_i32 m0, s77, 114688\n"
" s_mov_b32 s76, 1572864\n"
" buffer_load_dwordx4 v208, s[56:59], s76 offen lds\n"
" s_add_i32 m0, s77, 115712\n"
" s_mov_b32 s76, 1572864\n"
" buffer_load_dwordx4 v209, s[56:59], s76 offen lds\n"
" s_add_i32 m0, s77, 116736\n"
" s_mov_b32 s76, 1638400\n"
" buffer_load_dwordx4 v208, s[56:59], s76 offen lds\n"
" s_add_i32 m0, s77, 117760\n"
" s_mov_b32 s76, 1638400\n"
" buffer_load_dwordx4 v209, s[56:59], s76 offen lds\n"
" s_add_i32 m0, s77, 131072\n"
" s_mov_b32 s76, 2097152\n"
" buffer_load_dwordx4 v208, s[56:59], s76 offen lds\n"
" s_add_i32 m0, s77, 132096\n"
" s_mov_b32 s76, 2097152\n"
" buffer_load_dwordx4 v209, s[56:59], s76 offen lds\n"
" s_add_i32 m0, s77, 133120\n"
" s_mov_b32 s76, 2162688\n"
" buffer_load_dwordx4 v208, s[56:59], s76 offen lds\n"
" s_add_i32 m0, s77, 134144\n"
" s_mov_b32 s76, 2162688\n"
" buffer_load_dwordx4 v209, s[56:59], s76 offen lds\n"
" s_add_i32 m0, s77, 0\n"
" s_mov_b32 s76, 2621440\n"
" buffer_load_dwordx4 v208, s[56:59], s76 offen lds\n"
" s_add_i32 m0, s77, 1024\n"
" s_mov_b32 s76, 2621440\n"
" buffer_load_dwordx4 v209, s[56:59], s76 offen lds\n"
" s_add_i32 m0, s77, 2048\n"
" s_mov_b32 s76, 2686976\n"
" buffer_load_dwordx4 v208, s[56:59], s76 offen lds\n"
" s_add_i32 m0, s77, 3072\n"
" s_mov_b32 s76, 2686976\n"
" buffer_load_dwordx4 v209, s[56:59], s76 offen lds\n"
" s_add_i32 m0, s77, 16384\n"
" s_mov_b32 s76, 3145728\n"
" buffer_load_dwordx4 v208, s[56:59], s76 offen lds\n"
" s_add_i32 m0, s77, 17408\n"
" s_mov_b32 s76, 3145728\n"
" buffer_load_dwordx4 v209, s[56:59], s76 offen lds\n"
" s_add_i32 m0, s77, 18432\n"
" s_mov_b32 s76, 3211264\n"
" buffer_load_dwordx4 v208, s[56:59], s76 offen lds\n"
" s_add_i32 m0, s77, 19456\n"
" s_mov_b32 s76, 3211264\n"
" buffer_load_dwordx4 v209, s[56:59], s76 offen lds\n"
" s_add_i32 m0, s77, 32768\n"
" s_mov_b32 s76, 3670016\n"
" buffer_load_dwordx4 v208, s[56:59], s76 offen lds\n"
" s_add_i32 m0, s77, 33792\n"
" s_mov_b32 s76, 3670016\n"
" buffer_load_dwordx4 v209, s[56:59], s76 offen lds\n"
" s_add_i32 m0, s77, 34816\n"
" s_mov_b32 s76, 3735552\n"
" buffer_load_dwordx4 v208, s[56:59], s76 offen lds\n"
" s_add_i32 m0, s77, 35840\n"
" s_mov_b32 s76, 3735552\n"
" buffer_load_dwordx4 v209, s[56:59], s76 offen lds\n"
" s_add_i32 m0, s77, 49152\n"
" s_mov_b32 s76, 4194304\n"
" buffer_load_dwordx4 v208, s[56:59], s76 offen lds\n"
" s_add_i32 m0, s77, 50176\n"
" s_mov_b32 s76, 4194304\n"
" buffer_load_dwordx4 v209, s[56:59], s76 offen lds\n"
" s_add_i32 m0, s77, 51200\n"
" s_mov_b32 s76, 4259840\n"
" buffer_load_dwordx4 v208, s[56:59], s76 offen lds\n"
" s_add_i32 m0, s77, 52224\n"
" s_mov_b32 s76, 4259840\n"
" buffer_load_dwordx4 v209, s[56:59], s76 offen lds\n"
" s_waitcnt lgkmcnt(0)\n"
" s_waitcnt vmcnt(32)\n"
" s_barrier\n"
" s_add_i32 m0, s77, 147456\n"
" s_mov_b32 s76, 4718592\n"
" buffer_load_dwordx4 v208, s[56:59], s76 offen lds\n"
" s_add_i32 m0, s77, 148480\n"
" s_mov_b32 s76, 4718592\n"
" buffer_load_dwordx4 v209, s[56:59], s76 offen lds\n"
" s_add_i32 m0, s77, 149504\n"
" s_mov_b32 s76, 4784128\n"
" buffer_load_dwordx4 v208, s[56:59], s76 offen lds\n"
" s_add_i32 m0, s77, 150528\n"
" s_mov_b32 s76, 4784128\n"
" buffer_load_dwordx4 v209, s[56:59], s76 offen lds\n"
" v_add_u32 v217, 65536, v213\n"
" v_add_u32 v218, 65536, v214\n"
" v_add_u32 v219, 65536, v215\n"
" ds_read_b64_tr_b16 a[160:161], v217 offset:0\n"
" ds_read_b64_tr_b16 a[162:163], v217 offset:4096\n"
" ds_read_b64_tr_b16 a[240:241], v218 offset:0\n"
" ds_read_b64_tr_b16 a[242:243], v218 offset:4096\n"
" ds_read_b64_tr_b16 a[164:165], v217 offset:8192\n"
" ds_read_b64_tr_b16 a[166:167], v217 offset:12288\n"
" ds_read_b64_tr_b16 a[244:245], v218 offset:8192\n"
" ds_read_b64_tr_b16 a[246:247], v218 offset:12288\n"
" s_waitcnt lgkmcnt(0)\n"
" s_waitcnt vmcnt(32)\n"
" s_barrier\n"
" s_add_i32 m0, s77, 65536\n"
" s_mov_b32 s76, 0\n"
" buffer_load_dwordx4 v210, s[60:63], s76 offen lds\n"
" s_add_i32 m0, s77, 66560\n"
" s_mov_b32 s76, 65536\n"
" buffer_load_dwordx4 v210, s[60:63], s76 offen lds\n"
" s_add_i32 m0, s77, 67584\n"
" s_mov_b32 s76, 131072\n"
" buffer_load_dwordx4 v210, s[60:63], s76 offen lds\n"
" s_add_i32 m0, s77, 68608\n"
" s_mov_b32 s76, 196608\n"
" buffer_load_dwordx4 v210, s[60:63], s76 offen lds\n"
" v_add_u32 v217, 81920, v213\n"
" v_add_u32 v218, 81920, v214\n"
" v_add_u32 v219, 81920, v215\n"
" ds_read_b64_tr_b16 a[168:169], v217 offset:0\n"
" ds_read_b64_tr_b16 a[170:171], v217 offset:4096\n"
" ds_read_b64_tr_b16 a[248:249], v218 offset:0\n"
" ds_read_b64_tr_b16 a[250:251], v218 offset:4096\n"
" ds_read_b64_tr_b16 a[172:173], v217 offset:8192\n"
" ds_read_b64_tr_b16 a[174:175], v217 offset:12288\n"
" ds_read_b64_tr_b16 a[252:253], v218 offset:8192\n"
" ds_read_b64_tr_b16 a[254:255], v218 offset:12288\n"
" s_waitcnt lgkmcnt(0)\n"
" s_waitcnt vmcnt(32)\n"
" s_barrier\n"
" s_add_i32 m0, s77, 81920\n"
" s_mov_b32 s76, 1048576\n"
" buffer_load_dwordx4 v210, s[60:63], s76 offen lds\n"
" s_add_i32 m0, s77, 82944\n"
" s_mov_b32 s76, 1114112\n"
" buffer_load_dwordx4 v210, s[60:63], s76 offen lds\n"
" s_add_i32 m0, s77, 83968\n"
" s_mov_b32 s76, 1179648\n"
" buffer_load_dwordx4 v210, s[60:63], s76 offen lds\n"
" s_add_i32 m0, s77, 84992\n"
" s_mov_b32 s76, 1245184\n"
" buffer_load_dwordx4 v210, s[60:63], s76 offen lds\n"
" v_add_u32 v217, 98304, v213\n"
" v_add_u32 v218, 98304, v214\n"
" v_add_u32 v219, 98304, v215\n"
" ds_read_b64_tr_b16 a[176:177], v217 offset:0\n"
" ds_read_b64_tr_b16 a[178:179], v217 offset:4096\n"
" ds_read_b64_tr_b16 v[0:1], v218 offset:0\n"
" ds_read_b64_tr_b16 v[2:3], v218 offset:4096\n"
" ds_read_b64_tr_b16 a[180:181], v217 offset:8192\n"
" ds_read_b64_tr_b16 a[182:183], v217 offset:12288\n"
" ds_read_b64_tr_b16 v[4:5], v218 offset:8192\n"
" ds_read_b64_tr_b16 v[6:7], v218 offset:12288\n"
" s_waitcnt lgkmcnt(0)\n"
" s_waitcnt vmcnt(32)\n"
" s_barrier\n"
" s_add_i32 m0, s77, 98304\n"
" s_mov_b32 s76, 2097152\n"
" buffer_load_dwordx4 v210, s[60:63], s76 offen lds\n"
" s_add_i32 m0, s77, 99328\n"
" s_mov_b32 s76, 2162688\n"
" buffer_load_dwordx4 v210, s[60:63], s76 offen lds\n"
" s_add_i32 m0, s77, 100352\n"
" s_mov_b32 s76, 2228224\n"
" buffer_load_dwordx4 v210, s[60:63], s76 offen lds\n"
" s_add_i32 m0, s77, 101376\n"
" s_mov_b32 s76, 2293760\n"
" buffer_load_dwordx4 v210, s[60:63], s76 offen lds\n"
" v_add_u32 v217, 114688, v213\n"
" v_add_u32 v218, 114688, v214\n"
" v_add_u32 v219, 114688, v215\n"
" ds_read_b64_tr_b16 a[184:185], v217 offset:0\n"
" ds_read_b64_tr_b16 a[186:187], v217 offset:4096\n"
" ds_read_b64_tr_b16 v[8:9], v218 offset:0\n"
" ds_read_b64_tr_b16 v[10:11], v218 offset:4096\n"
" ds_read_b64_tr_b16 a[188:189], v217 offset:8192\n"
" ds_read_b64_tr_b16 a[190:191], v217 offset:12288\n"
" ds_read_b64_tr_b16 v[12:13], v218 offset:8192\n"
" ds_read_b64_tr_b16 v[14:15], v218 offset:12288\n"
" s_waitcnt lgkmcnt(0)\n"
" s_waitcnt vmcnt(32)\n"
" s_barrier\n"
" s_add_i32 m0, s77, 114688\n"
" s_mov_b32 s76, 3145728\n"
" buffer_load_dwordx4 v210, s[60:63], s76 offen lds\n"
" s_add_i32 m0, s77, 115712\n"
" s_mov_b32 s76, 3211264\n"
" buffer_load_dwordx4 v210, s[60:63], s76 offen lds\n"
" s_add_i32 m0, s77, 116736\n"
" s_mov_b32 s76, 3276800\n"
" buffer_load_dwordx4 v210, s[60:63], s76 offen lds\n"
" s_add_i32 m0, s77, 117760\n"
" s_mov_b32 s76, 3342336\n"
" buffer_load_dwordx4 v210, s[60:63], s76 offen lds\n"
" v_add_u32 v217, 131072, v213\n"
" v_add_u32 v218, 131072, v214\n"
" v_add_u32 v219, 131072, v215\n"
" ds_read_b64_tr_b16 a[192:193], v217 offset:0\n"
" ds_read_b64_tr_b16 a[194:195], v217 offset:4096\n"
" ds_read_b64_tr_b16 v[16:17], v218 offset:0\n"
" ds_read_b64_tr_b16 v[18:19], v218 offset:4096\n"
" ds_read_b64_tr_b16 a[196:197], v217 offset:8192\n"
" ds_read_b64_tr_b16 a[198:199], v217 offset:12288\n"
" ds_read_b64_tr_b16 v[20:21], v218 offset:8192\n"
" ds_read_b64_tr_b16 v[22:23], v218 offset:12288\n"
" s_waitcnt lgkmcnt(0)\n"
" s_waitcnt vmcnt(32)\n"
" s_barrier\n"
" s_add_i32 m0, s77, 131072\n"
" s_mov_b32 s76, 4194304\n"
" buffer_load_dwordx4 v210, s[60:63], s76 offen lds\n"
" s_add_i32 m0, s77, 132096\n"
" s_mov_b32 s76, 4259840\n"
" buffer_load_dwordx4 v210, s[60:63], s76 offen lds\n"
" s_add_i32 m0, s77, 133120\n"
" s_mov_b32 s76, 4325376\n"
" buffer_load_dwordx4 v210, s[60:63], s76 offen lds\n"
" s_add_i32 m0, s77, 134144\n"
" s_mov_b32 s76, 4390912\n"
" buffer_load_dwordx4 v210, s[60:63], s76 offen lds\n"
" ds_read_b64_tr_b16 a[200:201], v213 offset:0\n"
" ds_read_b64_tr_b16 a[202:203], v213 offset:4096\n"
" ds_read_b64_tr_b16 v[24:25], v214 offset:0\n"
" ds_read_b64_tr_b16 v[26:27], v214 offset:4096\n"
" ds_read_b64_tr_b16 a[204:205], v213 offset:8192\n"
" ds_read_b64_tr_b16 a[206:207], v213 offset:12288\n"
" ds_read_b64_tr_b16 v[28:29], v214 offset:8192\n"
" ds_read_b64_tr_b16 v[30:31], v214 offset:12288\n"
" s_waitcnt lgkmcnt(0)\n"
" s_waitcnt vmcnt(32)\n"
" s_barrier\n"
" s_add_i32 m0, s77, 0\n"
" s_mov_b32 s76, 0\n"
" buffer_load_dwordx4 v208, s[52:55], s76 offen lds\n"
" s_add_i32 m0, s77, 1024\n"
" s_mov_b32 s76, 0\n"
" buffer_load_dwordx4 v209, s[52:55], s76 offen lds\n"
" s_add_i32 m0, s77, 2048\n"
" s_mov_b32 s76, 65536\n"
" buffer_load_dwordx4 v208, s[52:55], s76 offen lds\n"
" s_add_i32 m0, s77, 3072\n"
" s_mov_b32 s76, 65536\n"
" buffer_load_dwordx4 v209, s[52:55], s76 offen lds\n"
" ds_read_b64_tr_b16 a[208:209], v213 offset:16384\n"
" ds_read_b64_tr_b16 a[210:211], v213 offset:20480\n"
" ds_read_b64_tr_b16 v[32:33], v214 offset:16384\n"
" ds_read_b64_tr_b16 v[34:35], v214 offset:20480\n"
" ds_read_b64_tr_b16 a[212:213], v213 offset:24576\n"
" ds_read_b64_tr_b16 a[214:215], v213 offset:28672\n"
" ds_read_b64_tr_b16 v[36:37], v214 offset:24576\n"
" ds_read_b64_tr_b16 v[38:39], v214 offset:28672\n"
" s_waitcnt lgkmcnt(0)\n"
" s_waitcnt vmcnt(32)\n"
" s_barrier\n"
" s_add_i32 m0, s77, 16384\n"
" s_mov_b32 s76, 524288\n"
" buffer_load_dwordx4 v208, s[52:55], s76 offen lds\n"
" s_add_i32 m0, s77, 17408\n"
" s_mov_b32 s76, 524288\n"
" buffer_load_dwordx4 v209, s[52:55], s76 offen lds\n"
" s_add_i32 m0, s77, 18432\n"
" s_mov_b32 s76, 589824\n"
" buffer_load_dwordx4 v208, s[52:55], s76 offen lds\n"
" s_add_i32 m0, s77, 19456\n"
" s_mov_b32 s76, 589824\n"
" buffer_load_dwordx4 v209, s[52:55], s76 offen lds\n"
" ds_read_b64_tr_b16 a[216:217], v213 offset:32768\n"
" ds_read_b64_tr_b16 a[218:219], v213 offset:36864\n"
" ds_read_b64_tr_b16 v[40:41], v214 offset:32768\n"
" ds_read_b64_tr_b16 v[42:43], v214 offset:36864\n"
" ds_read_b64_tr_b16 a[220:221], v213 offset:40960\n"
" ds_read_b64_tr_b16 a[222:223], v213 offset:45056\n"
" ds_read_b64_tr_b16 v[44:45], v214 offset:40960\n"
" ds_read_b64_tr_b16 v[46:47], v214 offset:45056\n"
" s_waitcnt lgkmcnt(0)\n"
" s_waitcnt vmcnt(32)\n"
" s_barrier\n"
" s_add_i32 m0, s77, 32768\n"
" s_mov_b32 s76, 1048576\n"
" buffer_load_dwordx4 v208, s[52:55], s76 offen lds\n"
" s_add_i32 m0, s77, 33792\n"
" s_mov_b32 s76, 1048576\n"
" buffer_load_dwordx4 v209, s[52:55], s76 offen lds\n"
" s_add_i32 m0, s77, 34816\n"
" s_mov_b32 s76, 1114112\n"
" buffer_load_dwordx4 v208, s[52:55], s76 offen lds\n"
" s_add_i32 m0, s77, 35840\n"
" s_mov_b32 s76, 1114112\n"
" buffer_load_dwordx4 v209, s[52:55], s76 offen lds\n"
" ds_read_b64_tr_b16 a[224:225], v213 offset:49152\n"
" ds_read_b64_tr_b16 a[226:227], v213 offset:53248\n"
" ds_read_b64_tr_b16 v[48:49], v214 offset:49152\n"
" ds_read_b64_tr_b16 v[50:51], v214 offset:53248\n"
" ds_read_b64_tr_b16 a[228:229], v213 offset:57344\n"
" ds_read_b64_tr_b16 a[230:231], v213 offset:61440\n"
" ds_read_b64_tr_b16 v[52:53], v214 offset:57344\n"
" ds_read_b64_tr_b16 v[54:55], v214 offset:61440\n"
" s_waitcnt lgkmcnt(0)\n"
" s_waitcnt vmcnt(32)\n"
" s_barrier\n"
" s_add_i32 m0, s77, 49152\n"
" s_mov_b32 s76, 1572864\n"
" buffer_load_dwordx4 v208, s[52:55], s76 offen lds\n"
" s_add_i32 m0, s77, 50176\n"
" s_mov_b32 s76, 1572864\n"
" buffer_load_dwordx4 v209, s[52:55], s76 offen lds\n"
" s_add_i32 m0, s77, 51200\n"
" s_mov_b32 s76, 1638400\n"
" buffer_load_dwordx4 v208, s[52:55], s76 offen lds\n"
" s_add_i32 m0, s77, 52224\n"
" s_mov_b32 s76, 1638400\n"
" buffer_load_dwordx4 v209, s[52:55], s76 offen lds\n"
" v_add_u32 v217, 147456, v213\n"
" v_add_u32 v218, 147456, v214\n"
" v_add_u32 v219, 147456, v215\n"
" ds_read_b64_tr_b16 a[232:233], v217 offset:0\n"
" ds_read_b64_tr_b16 a[234:235], v217 offset:4096\n"
" ds_read_b64_tr_b16 v[56:57], v218 offset:0\n"
" ds_read_b64_tr_b16 v[58:59], v218 offset:4096\n"
" ds_read_b64_tr_b16 a[236:237], v217 offset:8192\n"
" ds_read_b64_tr_b16 a[238:239], v217 offset:12288\n"
" ds_read_b64_tr_b16 v[60:61], v218 offset:8192\n"
" ds_read_b64_tr_b16 v[62:63], v218 offset:12288\n"
" s_waitcnt lgkmcnt(0)\n"
" s_waitcnt vmcnt(32)\n"
" s_barrier\n"
" s_add_i32 m0, s77, 147456\n"
" s_mov_b32 s76, 2097152\n"
" buffer_load_dwordx4 v208, s[52:55], s76 offen lds\n"
" s_add_i32 m0, s77, 148480\n"
" s_mov_b32 s76, 2097152\n"
" buffer_load_dwordx4 v209, s[52:55], s76 offen lds\n"
" s_add_i32 m0, s77, 149504\n"
" s_mov_b32 s76, 2162688\n"
" buffer_load_dwordx4 v208, s[52:55], s76 offen lds\n"
" s_add_i32 m0, s77, 150528\n"
" s_mov_b32 s76, 2162688\n"
" buffer_load_dwordx4 v209, s[52:55], s76 offen lds\n"
" v_add_u32 v217, 65536, v213\n"
" v_add_u32 v218, 65536, v214\n"
" v_add_u32 v219, 65536, v215\n"
" ds_read_b64_tr_b16 v[64:65], v219 offset:0\n"
" ds_read_b64_tr_b16 v[66:67], v219 offset:2048\n"
" ds_read_b64_tr_b16 v[68:69], v219 offset:4096\n"
" ds_read_b64_tr_b16 v[70:71], v219 offset:6144\n"
" ds_read_b64_tr_b16 v[72:73], v219 offset:8192\n"
" ds_read_b64_tr_b16 v[74:75], v219 offset:10240\n"
" ds_read_b64_tr_b16 v[76:77], v219 offset:12288\n"
" ds_read_b64_tr_b16 v[78:79], v219 offset:14336\n"
" s_waitcnt lgkmcnt(0)\n"
" s_waitcnt vmcnt(32)\n"
" s_barrier\n"
" s_add_i32 m0, s77, 65536\n"
" s_mov_b32 s76, 2621440\n"
" buffer_load_dwordx4 v208, s[52:55], s76 offen lds\n"
" s_add_i32 m0, s77, 66560\n"
" s_mov_b32 s76, 2621440\n"
" buffer_load_dwordx4 v209, s[52:55], s76 offen lds\n"
" s_add_i32 m0, s77, 67584\n"
" s_mov_b32 s76, 2686976\n"
" buffer_load_dwordx4 v208, s[52:55], s76 offen lds\n"
" s_add_i32 m0, s77, 68608\n"
" s_mov_b32 s76, 2686976\n"
" buffer_load_dwordx4 v209, s[52:55], s76 offen lds\n"
" v_add_u32 v217, 81920, v213\n"
" v_add_u32 v218, 81920, v214\n"
" v_add_u32 v219, 81920, v215\n"
" ds_read_b64_tr_b16 v[80:81], v219 offset:0\n"
" ds_read_b64_tr_b16 v[82:83], v219 offset:2048\n"
" ds_read_b64_tr_b16 v[84:85], v219 offset:4096\n"
" ds_read_b64_tr_b16 v[86:87], v219 offset:6144\n"
" ds_read_b64_tr_b16 v[88:89], v219 offset:8192\n"
" ds_read_b64_tr_b16 v[90:91], v219 offset:10240\n"
" ds_read_b64_tr_b16 v[92:93], v219 offset:12288\n"
" ds_read_b64_tr_b16 v[94:95], v219 offset:14336\n"
" s_waitcnt lgkmcnt(0)\n"
" s_waitcnt vmcnt(32)\n"
" s_barrier\n"
" s_add_i32 m0, s77, 81920\n"
" s_mov_b32 s76, 3145728\n"
" buffer_load_dwordx4 v208, s[52:55], s76 offen lds\n"
" s_add_i32 m0, s77, 82944\n"
" s_mov_b32 s76, 3145728\n"
" buffer_load_dwordx4 v209, s[52:55], s76 offen lds\n"
" s_add_i32 m0, s77, 83968\n"
" s_mov_b32 s76, 3211264\n"
" buffer_load_dwordx4 v208, s[52:55], s76 offen lds\n"
" s_add_i32 m0, s77, 84992\n"
" s_mov_b32 s76, 3211264\n"
" buffer_load_dwordx4 v209, s[52:55], s76 offen lds\n"
" v_add_u32 v217, 98304, v213\n"
" v_add_u32 v218, 98304, v214\n"
" v_add_u32 v219, 98304, v215\n"
" ds_read_b64_tr_b16 v[96:97], v219 offset:0\n"
" ds_read_b64_tr_b16 v[98:99], v219 offset:2048\n"
" ds_read_b64_tr_b16 v[100:101], v219 offset:4096\n"
" ds_read_b64_tr_b16 v[102:103], v219 offset:6144\n"
" ds_read_b64_tr_b16 v[104:105], v219 offset:8192\n"
" ds_read_b64_tr_b16 v[106:107], v219 offset:10240\n"
" ds_read_b64_tr_b16 v[108:109], v219 offset:12288\n"
" ds_read_b64_tr_b16 v[110:111], v219 offset:14336\n"
" s_waitcnt lgkmcnt(0)\n"
" s_waitcnt vmcnt(32)\n"
" s_barrier\n"
" s_add_i32 m0, s77, 98304\n"
" s_mov_b32 s76, 3670016\n"
" buffer_load_dwordx4 v208, s[52:55], s76 offen lds\n"
" s_add_i32 m0, s77, 99328\n"
" s_mov_b32 s76, 3670016\n"
" buffer_load_dwordx4 v209, s[52:55], s76 offen lds\n"
" s_add_i32 m0, s77, 100352\n"
" s_mov_b32 s76, 3735552\n"
" buffer_load_dwordx4 v208, s[52:55], s76 offen lds\n"
" s_add_i32 m0, s77, 101376\n"
" s_mov_b32 s76, 3735552\n"
" buffer_load_dwordx4 v209, s[52:55], s76 offen lds\n"
" v_add_u32 v217, 114688, v213\n"
" v_add_u32 v218, 114688, v214\n"
" v_add_u32 v219, 114688, v215\n"
" ds_read_b64_tr_b16 v[128:129], v219 offset:0\n"
" ds_read_b64_tr_b16 v[130:131], v219 offset:2048\n"
" ds_read_b64_tr_b16 v[132:133], v219 offset:4096\n"
" ds_read_b64_tr_b16 v[134:135], v219 offset:6144\n"
" ds_read_b64_tr_b16 v[136:137], v219 offset:8192\n"
" ds_read_b64_tr_b16 v[138:139], v219 offset:10240\n"
" ds_read_b64_tr_b16 v[140:141], v219 offset:12288\n"
" ds_read_b64_tr_b16 v[142:143], v219 offset:14336\n"
" s_waitcnt lgkmcnt(0)\n"
" s_waitcnt vmcnt(32)\n"
" s_barrier\n"
" s_add_i32 m0, s77, 114688\n"
" s_mov_b32 s76, 4194304\n"
" buffer_load_dwordx4 v208, s[52:55], s76 offen lds\n"
" s_add_i32 m0, s77, 115712\n"
" s_mov_b32 s76, 4194304\n"
" buffer_load_dwordx4 v209, s[52:55], s76 offen lds\n"
" s_add_i32 m0, s77, 116736\n"
" s_mov_b32 s76, 4259840\n"
" buffer_load_dwordx4 v208, s[52:55], s76 offen lds\n"
" s_add_i32 m0, s77, 117760\n"
" s_mov_b32 s76, 4259840\n"
" buffer_load_dwordx4 v209, s[52:55], s76 offen lds\n"
" v_add_u32 v217, 131072, v213\n"
" v_add_u32 v218, 131072, v214\n"
" v_add_u32 v219, 131072, v215\n"
" ds_read_b64_tr_b16 v[144:145], v219 offset:0\n"
" ds_read_b64_tr_b16 v[146:147], v219 offset:2048\n"
" ds_read_b64_tr_b16 v[112:113], v219 offset:4096\n"
" ds_read_b64_tr_b16 v[114:115], v219 offset:6144\n"
" ds_read_b64_tr_b16 v[116:117], v219 offset:8192\n"
" ds_read_b64_tr_b16 v[118:119], v219 offset:10240\n"
" ds_read_b64_tr_b16 v[120:121], v219 offset:12288\n"
" ds_read_b64_tr_b16 v[122:123], v219 offset:14336\n"
" s_waitcnt lgkmcnt(0)\n"
" s_waitcnt vmcnt(32)\n"
" s_barrier\n"
" s_add_i32 m0, s77, 131072\n"
" s_mov_b32 s76, 4718592\n"
" buffer_load_dwordx4 v208, s[52:55], s76 offen lds\n"
" s_add_i32 m0, s77, 132096\n"
" s_mov_b32 s76, 4718592\n"
" buffer_load_dwordx4 v209, s[52:55], s76 offen lds\n"
" s_add_i32 m0, s77, 133120\n"
" s_mov_b32 s76, 4784128\n"
" buffer_load_dwordx4 v208, s[52:55], s76 offen lds\n"
" s_add_i32 m0, s77, 134144\n"
" s_mov_b32 s76, 4784128\n"
" buffer_load_dwordx4 v209, s[52:55], s76 offen lds\n"
" ds_read_b64_tr_b16 a[0:1], v213 offset:0\n"
" ds_read_b64_tr_b16 a[2:3], v213 offset:4096\n"
" ds_read_b64_tr_b16 a[4:5], v214 offset:0\n"
" ds_read_b64_tr_b16 a[6:7], v214 offset:4096\n"
" ds_read_b64_tr_b16 a[8:9], v213 offset:8192\n"
" ds_read_b64_tr_b16 a[10:11], v213 offset:12288\n"
" ds_read_b64_tr_b16 a[12:13], v214 offset:8192\n"
" ds_read_b64_tr_b16 a[14:15], v214 offset:12288\n"
" s_waitcnt lgkmcnt(0)\n"
" s_waitcnt vmcnt(32)\n"
" s_barrier\n"
" s_add_i32 m0, s77, 0\n"
" s_add_i32 s78, s32, 0\n"
" s_cmp_ge_u32 s78, 5\n"
" s_cselect_b32 s79, 5, 0\n"
" s_sub_i32 s78, s78, s79\n"
" s_lshl_b32 s78, s78, 8\n"
" s_add_i32 s76, s78, 0\n"
" buffer_load_dwordx4 v211, s[84:87], s76 offen lds\n"
" s_add_i32 m0, s77, 1024\n"
" s_add_i32 s76, s78, 0\n"
" buffer_load_dwordx4 v212, s[84:87], s76 offen lds\n"
" s_add_i32 m0, s77, 2048\n"
" s_add_i32 s76, s78, 10240\n"
" buffer_load_dwordx4 v211, s[84:87], s76 offen lds\n"
" s_add_i32 m0, s77, 3072\n"
" s_add_i32 s76, s78, 10240\n"
" buffer_load_dwordx4 v212, s[84:87], s76 offen lds\n"
" ds_read_b64_tr_b16 a[16:17], v213 offset:16384\n"
" ds_read_b64_tr_b16 a[18:19], v213 offset:20480\n"
" ds_read_b64_tr_b16 a[20:21], v214 offset:16384\n"
" ds_read_b64_tr_b16 a[22:23], v214 offset:20480\n"
" ds_read_b64_tr_b16 a[24:25], v213 offset:24576\n"
" ds_read_b64_tr_b16 a[26:27], v213 offset:28672\n"
" ds_read_b64_tr_b16 a[28:29], v214 offset:24576\n"
" ds_read_b64_tr_b16 a[30:31], v214 offset:28672\n"
" s_waitcnt lgkmcnt(0)\n"
" s_waitcnt vmcnt(32)\n"
" s_barrier\n"
" s_add_i32 m0, s77, 16384\n"
" s_add_i32 s78, s32, 1\n"
" s_cmp_ge_u32 s78, 5\n"
" s_cselect_b32 s79, 5, 0\n"
" s_sub_i32 s78, s78, s79\n"
" s_lshl_b32 s78, s78, 8\n"
" s_add_i32 s76, s78, 0\n"
" buffer_load_dwordx4 v211, s[84:87], s76 offen lds\n"
" s_add_i32 m0, s77, 17408\n"
" s_add_i32 s76, s78, 0\n"
" buffer_load_dwordx4 v212, s[84:87], s76 offen lds\n"
" s_add_i32 m0, s77, 18432\n"
" s_add_i32 s76, s78, 10240\n"
" buffer_load_dwordx4 v211, s[84:87], s76 offen lds\n"
" s_add_i32 m0, s77, 19456\n"
" s_add_i32 s76, s78, 10240\n"
" buffer_load_dwordx4 v212, s[84:87], s76 offen lds\n"
" ds_read_b64_tr_b16 a[32:33], v213 offset:32768\n"
" ds_read_b64_tr_b16 a[34:35], v213 offset:36864\n"
" ds_read_b64_tr_b16 a[36:37], v214 offset:32768\n"
" ds_read_b64_tr_b16 a[38:39], v214 offset:36864\n"
" ds_read_b64_tr_b16 a[40:41], v213 offset:40960\n"
" ds_read_b64_tr_b16 a[42:43], v213 offset:45056\n"
" ds_read_b64_tr_b16 a[44:45], v214 offset:40960\n"
" ds_read_b64_tr_b16 a[46:47], v214 offset:45056\n"
" s_waitcnt lgkmcnt(0)\n"
" s_waitcnt vmcnt(32)\n"
" s_barrier\n"
" s_add_i32 m0, s77, 32768\n"
" s_add_i32 s78, s32, 2\n"
" s_cmp_ge_u32 s78, 5\n"
" s_cselect_b32 s79, 5, 0\n"
" s_sub_i32 s78, s78, s79\n"
" s_lshl_b32 s78, s78, 8\n"
" s_add_i32 s76, s78, 0\n"
" buffer_load_dwordx4 v211, s[84:87], s76 offen lds\n"
" s_add_i32 m0, s77, 33792\n"
" s_add_i32 s76, s78, 0\n"
" buffer_load_dwordx4 v212, s[84:87], s76 offen lds\n"
" s_add_i32 m0, s77, 34816\n"
" s_add_i32 s76, s78, 10240\n"
" buffer_load_dwordx4 v211, s[84:87], s76 offen lds\n"
" s_add_i32 m0, s77, 35840\n"
" s_add_i32 s76, s78, 10240\n"
" buffer_load_dwordx4 v212, s[84:87], s76 offen lds\n"
" ds_read_b64_tr_b16 a[48:49], v213 offset:49152\n"
" ds_read_b64_tr_b16 a[50:51], v213 offset:53248\n"
" ds_read_b64_tr_b16 a[52:53], v214 offset:49152\n"
" ds_read_b64_tr_b16 a[54:55], v214 offset:53248\n"
" ds_read_b64_tr_b16 a[56:57], v213 offset:57344\n"
" ds_read_b64_tr_b16 a[58:59], v213 offset:61440\n"
" ds_read_b64_tr_b16 a[60:61], v214 offset:57344\n"
" ds_read_b64_tr_b16 a[62:63], v214 offset:61440\n"
" s_waitcnt lgkmcnt(0)\n"
" s_waitcnt vmcnt(32)\n"
" s_barrier\n"
" s_add_i32 m0, s77, 49152\n"
" s_add_i32 s78, s32, 3\n"
" s_cmp_ge_u32 s78, 5\n"
" s_cselect_b32 s79, 5, 0\n"
" s_sub_i32 s78, s78, s79\n"
" s_lshl_b32 s78, s78, 8\n"
" s_add_i32 s76, s78, 0\n"
" buffer_load_dwordx4 v211, s[84:87], s76 offen lds\n"
" s_add_i32 m0, s77, 50176\n"
" s_add_i32 s76, s78, 0\n"
" buffer_load_dwordx4 v212, s[84:87], s76 offen lds\n"
" s_add_i32 m0, s77, 51200\n"
" s_add_i32 s76, s78, 10240\n"
" buffer_load_dwordx4 v211, s[84:87], s76 offen lds\n"
" s_add_i32 m0, s77, 52224\n"
" s_add_i32 s76, s78, 10240\n"
" buffer_load_dwordx4 v212, s[84:87], s76 offen lds\n"
" v_add_u32 v217, 147456, v213\n"
" v_add_u32 v218, 147456, v214\n"
" v_add_u32 v219, 147456, v215\n"
" ds_read_b64_tr_b16 a[64:65], v217 offset:0\n"
" ds_read_b64_tr_b16 a[66:67], v217 offset:4096\n"
" ds_read_b64_tr_b16 a[68:69], v218 offset:0\n"
" ds_read_b64_tr_b16 a[70:71], v218 offset:4096\n"
" ds_read_b64_tr_b16 a[72:73], v217 offset:8192\n"
" ds_read_b64_tr_b16 a[74:75], v217 offset:12288\n"
" ds_read_b64_tr_b16 a[76:77], v218 offset:8192\n"
" ds_read_b64_tr_b16 a[78:79], v218 offset:12288\n"
" s_waitcnt lgkmcnt(0)\n"
" s_waitcnt vmcnt(32)\n"
" s_barrier\n"
" s_add_i32 m0, s77, 147456\n"
" s_add_i32 s78, s32, 4\n"
" s_cmp_ge_u32 s78, 5\n"
" s_cselect_b32 s79, 5, 0\n"
" s_sub_i32 s78, s78, s79\n"
" s_lshl_b32 s78, s78, 8\n"
" s_add_i32 s76, s78, 0\n"
" buffer_load_dwordx4 v211, s[84:87], s76 offen lds\n"
" s_add_i32 m0, s77, 148480\n"
" s_add_i32 s76, s78, 0\n"
" buffer_load_dwordx4 v212, s[84:87], s76 offen lds\n"
" s_add_i32 m0, s77, 149504\n"
" s_add_i32 s76, s78, 10240\n"
" buffer_load_dwordx4 v211, s[84:87], s76 offen lds\n"
" s_add_i32 m0, s77, 150528\n"
" s_add_i32 s76, s78, 10240\n"
" buffer_load_dwordx4 v212, s[84:87], s76 offen lds\n"
" v_add_u32 v217, 65536, v213\n"
" v_add_u32 v218, 65536, v214\n"
" v_add_u32 v219, 65536, v215\n"
" ds_read_b64_tr_b16 a[80:81], v217 offset:0\n"
" ds_read_b64_tr_b16 a[82:83], v217 offset:4096\n"
" ds_read_b64_tr_b16 a[84:85], v218 offset:0\n"
" ds_read_b64_tr_b16 a[86:87], v218 offset:4096\n"
" ds_read_b64_tr_b16 a[88:89], v217 offset:8192\n"
" ds_read_b64_tr_b16 a[90:91], v217 offset:12288\n"
" ds_read_b64_tr_b16 a[92:93], v218 offset:8192\n"
" ds_read_b64_tr_b16 a[94:95], v218 offset:12288\n"
" s_waitcnt lgkmcnt(0)\n"
" s_waitcnt vmcnt(32)\n"
" s_barrier\n"
" buffer_load_dwordx2 v[176:177], v220, s[48:51], 0 offen sc1\n"
" v_add_u32 v217, 81920, v213\n"
" v_add_u32 v218, 81920, v214\n"
" v_add_u32 v219, 81920, v215\n"
" ds_read_b64_tr_b16 a[96:97], v217 offset:0\n"
" ds_read_b64_tr_b16 a[98:99], v217 offset:4096\n"
" ds_read_b64_tr_b16 a[100:101], v218 offset:0\n"
" ds_read_b64_tr_b16 a[102:103], v218 offset:4096\n"
" ds_read_b64_tr_b16 a[104:105], v217 offset:8192\n"
" ds_read_b64_tr_b16 a[106:107], v217 offset:12288\n"
" ds_read_b64_tr_b16 a[108:109], v218 offset:8192\n"
" ds_read_b64_tr_b16 a[110:111], v218 offset:12288\n"
" s_waitcnt lgkmcnt(0)\n"
" s_waitcnt vmcnt(29)\n"
" s_barrier\n"
" v_add_u32 v217, 98304, v213\n"
" v_add_u32 v218, 98304, v214\n"
" v_add_u32 v219, 98304, v215\n"
" ds_read_b64_tr_b16 a[112:113], v217 offset:0\n"
" ds_read_b64_tr_b16 a[114:115], v217 offset:4096\n"
" ds_read_b64_tr_b16 a[116:117], v218 offset:0\n"
" ds_read_b64_tr_b16 a[118:119], v218 offset:4096\n"
" ds_read_b64_tr_b16 a[120:121], v217 offset:8192\n"
" ds_read_b64_tr_b16 a[122:123], v217 offset:12288\n"
" ds_read_b64_tr_b16 a[124:125], v218 offset:8192\n"
" ds_read_b64_tr_b16 a[126:127], v218 offset:12288\n"
" s_waitcnt lgkmcnt(0)\n"
" s_waitcnt vmcnt(25)\n"
" s_barrier\n"
" v_add_u32 v217, 114688, v213\n"
" v_add_u32 v218, 114688, v214\n"
" v_add_u32 v219, 114688, v215\n"
" ds_read_b64_tr_b16 a[128:129], v217 offset:0\n"
" ds_read_b64_tr_b16 a[130:131], v217 offset:4096\n"
" ds_read_b64_tr_b16 a[132:133], v218 offset:0\n"
" ds_read_b64_tr_b16 a[134:135], v218 offset:4096\n"
" ds_read_b64_tr_b16 a[136:137], v217 offset:8192\n"
" ds_read_b64_tr_b16 a[138:139], v217 offset:12288\n"
" ds_read_b64_tr_b16 a[140:141], v218 offset:8192\n"
" ds_read_b64_tr_b16 a[142:143], v218 offset:12288\n"
" s_waitcnt lgkmcnt(0)\n"
" s_waitcnt vmcnt(21)\n"
" s_barrier\n"
" v_add_u32 v217, 131072, v213\n"
" v_add_u32 v218, 131072, v214\n"
" v_add_u32 v219, 131072, v215\n"
" ds_read_b64_tr_b16 a[144:145], v217 offset:0\n"
" ds_read_b64_tr_b16 a[146:147], v217 offset:4096\n"
" ds_read_b64_tr_b16 a[148:149], v218 offset:0\n"
" ds_read_b64_tr_b16 a[150:151], v218 offset:4096\n"
" ds_read_b64_tr_b16 a[152:153], v217 offset:8192\n"
" ds_read_b64_tr_b16 a[154:155], v217 offset:12288\n"
" ds_read_b64_tr_b16 a[156:157], v218 offset:8192\n"
" ds_read_b64_tr_b16 a[158:159], v218 offset:12288\n"
" s_waitcnt lgkmcnt(0)\n"
" s_waitcnt vmcnt(17)\n"
" s_barrier\n"
" ds_read_b64_tr_b16 v[160:161], v213 offset:0\n"
" ds_read_b64_tr_b16 v[162:163], v213 offset:4096\n"
" ds_read_b64_tr_b16 v[164:165], v213 offset:8192\n"
" ds_read_b64_tr_b16 v[166:167], v213 offset:12288\n"
" ds_read_b64_tr_b16 v[168:169], v214 offset:0\n"
" ds_read_b64_tr_b16 v[170:171], v214 offset:4096\n"
" ds_read_b64_tr_b16 v[172:173], v214 offset:8192\n"
" ds_read_b64_tr_b16 v[174:175], v214 offset:12288\n"
" s_waitcnt lgkmcnt(7)\n"
" v_pk_min_u16 v160, v160, -2 op_sel_hi:[1,0]\n"
" v_pk_min_u16 v161, v161, -2 op_sel_hi:[1,0]\n"
" s_waitcnt lgkmcnt(6)\n"
" v_pk_min_u16 v162, v162, -2 op_sel_hi:[1,0]\n"
" v_pk_min_u16 v163, v163, -2 op_sel_hi:[1,0]\n"
" s_waitcnt lgkmcnt(5)\n"
" v_pk_min_u16 v164, v164, -2 op_sel_hi:[1,0]\n"
" v_pk_min_u16 v165, v165, -2 op_sel_hi:[1,0]\n"
" s_waitcnt lgkmcnt(4)\n"
" v_pk_min_u16 v166, v166, -2 op_sel_hi:[1,0]\n"
" v_pk_min_u16 v167, v167, -2 op_sel_hi:[1,0]\n"
" s_waitcnt lgkmcnt(3)\n"
" v_pk_min_u16 v168, v168, -2 op_sel_hi:[1,0]\n"
" v_pk_min_u16 v169, v169, -2 op_sel_hi:[1,0]\n"
" s_waitcnt lgkmcnt(2)\n"
" v_pk_min_u16 v170, v170, -2 op_sel_hi:[1,0]\n"
" v_pk_min_u16 v171, v171, -2 op_sel_hi:[1,0]\n"
" s_waitcnt lgkmcnt(1)\n"
" v_pk_min_u16 v172, v172, -2 op_sel_hi:[1,0]\n"
" v_pk_min_u16 v173, v173, -2 op_sel_hi:[1,0]\n"
" s_waitcnt lgkmcnt(0)\n"
" v_pk_min_u16 v174, v174, -2 op_sel_hi:[1,0]\n"
" v_pk_min_u16 v175, v175, -2 op_sel_hi:[1,0]\n"
" ds_write_b128 v228, v[160:163] offset:0\n"
" ds_write_b128 v228, v[164:167] offset:1024\n"
" ds_write_b128 v228, v[168:171] offset:2048\n"
" ds_write_b128 v228, v[172:175] offset:3072\n"
" s_waitcnt lgkmcnt(0)\n"
" s_waitcnt vmcnt(13)\n"
" s_barrier\n"
" ds_read_b64_tr_b16 v[160:161], v213 offset:16384\n"
" ds_read_b64_tr_b16 v[162:163], v213 offset:20480\n"
" ds_read_b64_tr_b16 v[164:165], v213 offset:24576\n"
" ds_read_b64_tr_b16 v[166:167], v213 offset:28672\n"
" ds_read_b64_tr_b16 v[168:169], v214 offset:16384\n"
" ds_read_b64_tr_b16 v[170:171], v214 offset:20480\n"
" ds_read_b64_tr_b16 v[172:173], v214 offset:24576\n"
" ds_read_b64_tr_b16 v[174:175], v214 offset:28672\n"
" s_waitcnt lgkmcnt(7)\n"
" v_pk_min_u16 v160, v160, -2 op_sel_hi:[1,0]\n"
" v_pk_min_u16 v161, v161, -2 op_sel_hi:[1,0]\n"
" s_waitcnt lgkmcnt(6)\n"
" v_pk_min_u16 v162, v162, -2 op_sel_hi:[1,0]\n"
" v_pk_min_u16 v163, v163, -2 op_sel_hi:[1,0]\n"
" s_waitcnt lgkmcnt(5)\n"
" v_pk_min_u16 v164, v164, -2 op_sel_hi:[1,0]\n"
" v_pk_min_u16 v165, v165, -2 op_sel_hi:[1,0]\n"
" s_waitcnt lgkmcnt(4)\n"
" v_pk_min_u16 v166, v166, -2 op_sel_hi:[1,0]\n"
" v_pk_min_u16 v167, v167, -2 op_sel_hi:[1,0]\n"
" s_waitcnt lgkmcnt(3)\n"
" v_pk_min_u16 v168, v168, -2 op_sel_hi:[1,0]\n"
" v_pk_min_u16 v169, v169, -2 op_sel_hi:[1,0]\n"
" s_waitcnt lgkmcnt(2)\n"
" v_pk_min_u16 v170, v170, -2 op_sel_hi:[1,0]\n"
" v_pk_min_u16 v171, v171, -2 op_sel_hi:[1,0]\n"
" s_waitcnt lgkmcnt(1)\n"
" v_pk_min_u16 v172, v172, -2 op_sel_hi:[1,0]\n"
" v_pk_min_u16 v173, v173, -2 op_sel_hi:[1,0]\n"
" s_waitcnt lgkmcnt(0)\n"
" v_pk_min_u16 v174, v174, -2 op_sel_hi:[1,0]\n"
" v_pk_min_u16 v175, v175, -2 op_sel_hi:[1,0]\n"
" ds_write_b128 v228, v[160:163] offset:4096\n"
" ds_write_b128 v228, v[164:167] offset:5120\n"
" ds_write_b128 v228, v[168:171] offset:6144\n"
" ds_write_b128 v228, v[172:175] offset:7168\n"
" s_waitcnt lgkmcnt(0)\n"
" s_waitcnt vmcnt(9)\n"
" s_barrier\n"
" ds_read_b64_tr_b16 v[160:161], v213 offset:32768\n"
" ds_read_b64_tr_b16 v[162:163], v213 offset:36864\n"
" ds_read_b64_tr_b16 v[164:165], v213 offset:40960\n"
" ds_read_b64_tr_b16 v[166:167], v213 offset:45056\n"
" ds_read_b64_tr_b16 v[168:169], v214 offset:32768\n"
" ds_read_b64_tr_b16 v[170:171], v214 offset:36864\n"
" ds_read_b64_tr_b16 v[172:173], v214 offset:40960\n"
" ds_read_b64_tr_b16 v[174:175], v214 offset:45056\n"
" s_waitcnt lgkmcnt(7)\n"
" v_pk_min_u16 v160, v160, -2 op_sel_hi:[1,0]\n"
" v_pk_min_u16 v161, v161, -2 op_sel_hi:[1,0]\n"
" s_waitcnt lgkmcnt(6)\n"
" v_pk_min_u16 v162, v162, -2 op_sel_hi:[1,0]\n"
" v_pk_min_u16 v163, v163, -2 op_sel_hi:[1,0]\n"
" s_waitcnt lgkmcnt(5)\n"
" v_pk_min_u16 v164, v164, -2 op_sel_hi:[1,0]\n"
" v_pk_min_u16 v165, v165, -2 op_sel_hi:[1,0]\n"
" s_waitcnt lgkmcnt(4)\n"
" v_pk_min_u16 v166, v166, -2 op_sel_hi:[1,0]\n"
" v_pk_min_u16 v167, v167, -2 op_sel_hi:[1,0]\n"
" s_waitcnt lgkmcnt(3)\n"
" v_pk_min_u16 v168, v168, -2 op_sel_hi:[1,0]\n"
" v_pk_min_u16 v169, v169, -2 op_sel_hi:[1,0]\n"
" s_waitcnt lgkmcnt(2)\n"
" v_pk_min_u16 v170, v170, -2 op_sel_hi:[1,0]\n"
" v_pk_min_u16 v171, v171, -2 op_sel_hi:[1,0]\n"
" s_waitcnt lgkmcnt(1)\n"
" v_pk_min_u16 v172, v172, -2 op_sel_hi:[1,0]\n"
" v_pk_min_u16 v173, v173, -2 op_sel_hi:[1,0]\n"
" s_waitcnt lgkmcnt(0)\n"
" v_pk_min_u16 v174, v174, -2 op_sel_hi:[1,0]\n"
" v_pk_min_u16 v175, v175, -2 op_sel_hi:[1,0]\n"
" ds_write_b128 v228, v[160:163] offset:8192\n"
" ds_write_b128 v228, v[164:167] offset:9216\n"
" ds_write_b128 v228, v[168:171] offset:10240\n"
" ds_write_b128 v228, v[172:175] offset:11264\n"
" s_waitcnt lgkmcnt(0)\n"
" s_waitcnt vmcnt(5)\n"
" s_barrier\n"
" ds_read_b64_tr_b16 v[160:161], v213 offset:49152\n"
" ds_read_b64_tr_b16 v[162:163], v213 offset:53248\n"
" ds_read_b64_tr_b16 v[164:165], v213 offset:57344\n"
" ds_read_b64_tr_b16 v[166:167], v213 offset:61440\n"
" ds_read_b64_tr_b16 v[168:169], v214 offset:49152\n"
" ds_read_b64_tr_b16 v[170:171], v214 offset:53248\n"
" ds_read_b64_tr_b16 v[172:173], v214 offset:57344\n"
" ds_read_b64_tr_b16 v[174:175], v214 offset:61440\n"
" s_waitcnt lgkmcnt(7)\n"
" v_pk_min_u16 v160, v160, -2 op_sel_hi:[1,0]\n"
" v_pk_min_u16 v161, v161, -2 op_sel_hi:[1,0]\n"
" s_waitcnt lgkmcnt(6)\n"
" v_pk_min_u16 v162, v162, -2 op_sel_hi:[1,0]\n"
" v_pk_min_u16 v163, v163, -2 op_sel_hi:[1,0]\n"
" s_waitcnt lgkmcnt(5)\n"
" v_pk_min_u16 v164, v164, -2 op_sel_hi:[1,0]\n"
" v_pk_min_u16 v165, v165, -2 op_sel_hi:[1,0]\n"
" s_waitcnt lgkmcnt(4)\n"
" v_pk_min_u16 v166, v166, -2 op_sel_hi:[1,0]\n"
" v_pk_min_u16 v167, v167, -2 op_sel_hi:[1,0]\n"
" s_waitcnt lgkmcnt(3)\n"
" v_pk_min_u16 v168, v168, -2 op_sel_hi:[1,0]\n"
" v_pk_min_u16 v169, v169, -2 op_sel_hi:[1,0]\n"
" s_waitcnt lgkmcnt(2)\n"
" v_pk_min_u16 v170, v170, -2 op_sel_hi:[1,0]\n"
" v_pk_min_u16 v171, v171, -2 op_sel_hi:[1,0]\n"
" s_waitcnt lgkmcnt(1)\n"
" v_pk_min_u16 v172, v172, -2 op_sel_hi:[1,0]\n"
" v_pk_min_u16 v173, v173, -2 op_sel_hi:[1,0]\n"
" s_waitcnt lgkmcnt(0)\n"
" v_pk_min_u16 v174, v174, -2 op_sel_hi:[1,0]\n"
" v_pk_min_u16 v175, v175, -2 op_sel_hi:[1,0]\n"
" ds_write_b128 v228, v[160:163] offset:12288\n"
" ds_write_b128 v228, v[164:167] offset:13312\n"
" ds_write_b128 v228, v[168:171] offset:14336\n"
" ds_write_b128 v228, v[172:175] offset:15360\n"
" s_waitcnt lgkmcnt(0)\n"
" s_waitcnt vmcnt(1)\n"
" s_barrier\n"
" v_add_u32 v217, 147456, v213\n"
" v_add_u32 v218, 147456, v214\n"
" v_add_u32 v219, 147456, v215\n"
" ds_read_b64_tr_b16 v[160:161], v217 offset:0\n"
" ds_read_b64_tr_b16 v[162:163], v217 offset:4096\n"
" ds_read_b64_tr_b16 v[164:165], v217 offset:8192\n"
" ds_read_b64_tr_b16 v[166:167], v217 offset:12288\n"
" ds_read_b64_tr_b16 v[168:169], v218 offset:0\n"
" ds_read_b64_tr_b16 v[170:171], v218 offset:4096\n"
" ds_read_b64_tr_b16 v[172:173], v218 offset:8192\n"
" ds_read_b64_tr_b16 v[174:175], v218 offset:12288\n"
" s_waitcnt lgkmcnt(7)\n"
" v_pk_min_u16 v160, v160, -2 op_sel_hi:[1,0]\n"
" v_pk_min_u16 v161, v161, -2 op_sel_hi:[1,0]\n"
" s_waitcnt lgkmcnt(6)\n"
" v_pk_min_u16 v162, v162, -2 op_sel_hi:[1,0]\n"
" v_pk_min_u16 v163, v163, -2 op_sel_hi:[1,0]\n"
" s_waitcnt lgkmcnt(5)\n"
" v_pk_min_u16 v164, v164, -2 op_sel_hi:[1,0]\n"
" v_pk_min_u16 v165, v165, -2 op_sel_hi:[1,0]\n"
" s_waitcnt lgkmcnt(4)\n"
" v_pk_min_u16 v166, v166, -2 op_sel_hi:[1,0]\n"
" v_pk_min_u16 v167, v167, -2 op_sel_hi:[1,0]\n"
" s_waitcnt lgkmcnt(3)\n"
" v_pk_min_u16 v168, v168, -2 op_sel_hi:[1,0]\n"
" v_pk_min_u16 v169, v169, -2 op_sel_hi:[1,0]\n"
" s_waitcnt lgkmcnt(2)\n"
" v_pk_min_u16 v170, v170, -2 op_sel_hi:[1,0]\n"
" v_pk_min_u16 v171, v171, -2 op_sel_hi:[1,0]\n"
" s_waitcnt lgkmcnt(1)\n"
" v_pk_min_u16 v172, v172, -2 op_sel_hi:[1,0]\n"
" v_pk_min_u16 v173, v173, -2 op_sel_hi:[1,0]\n"
" s_waitcnt lgkmcnt(0)\n"
" v_pk_min_u16 v174, v174, -2 op_sel_hi:[1,0]\n"
" v_pk_min_u16 v175, v175, -2 op_sel_hi:[1,0]\n"
" ds_write_b128 v228, v[160:163] offset:16384\n"
" ds_write_b128 v228, v[164:167] offset:17408\n"
" ds_write_b128 v228, v[168:171] offset:18432\n"
" ds_write_b128 v228, v[172:175] offset:19456\n"
" s_waitcnt lgkmcnt(0)\n"
" s_waitcnt vmcnt(0)\n"
" s_barrier\n"
" s_mov_b32 s91, 0\n"
" ds_write_b128 v224, v[180:183] offset:0\n"
" ds_write_b128 v224, v[184:187] offset:4096\n"
" ds_write_b128 v224, v[188:191] offset:8192\n"
" ds_write_b128 v224, v[192:195] offset:12288\n"
" ds_write_b128 v224, v[196:199] offset:16384\n"
".Lch_pol_%=:\n"
" v_or_b32 v162, 1, v176\n"
" v_mov_b32 v163, v177\n"
" v_cmp_ne_u64 vcc, s[16:17], v[162:163]\n"
" s_nop 0\n"
" s_cbranch_vccz .Lch_polok_%=\n"
" s_add_i32 s91, s91, 1\n"
" s_or_b32 s91, s91, s90\n"
" s_cmp_ge_u32 s91, 65536\n"
" s_cselect_b32 s90, 65536, 0\n"
" s_cbranch_scc1 .Lch_polok_%=\n"
" s_sleep 2\n"
" buffer_load_dwordx2 v[176:177], v220, s[48:51], 0 offen sc1\n"
" s_waitcnt vmcnt(0)\n"
" s_branch .Lch_pol_%=\n"
".Lch_polok_%=:\n"
" v_and_b32 v160, 1, v176\n"
" v_cmp_eq_u32 vcc, 1, v160\n"
" s_nop 1\n"
" s_and_b32 s29, vcc_lo, 0x1f\n"
" s_bitcmp1_b64 vcc, 5\n"
" s_cselect_b32 s76, 8192, 0\n"
" s_add_u32 s44, s44, s76\n"
" s_lshr_b32 s76, s73, 5\n"
" s_add_i32 s76, s76, 60\n"
" s_bitcmp1_b64 vcc, s76\n"
" s_cselect_b32 s76, 8192, 0\n"
" s_add_u32 s64, s64, s76\n"
" s_bitcmp1_b32 s29, 0\n"
" s_cselect_b32 s76, 8192, 0\n"
" s_add_u32 s48, s48, s76\n"
" s_lshr_b32 s76, s29, 1\n"
" v_lshrrev_b32 v160, 21, v230\n"
" v_lshrrev_b32_e64 v160, v160, s76\n"
" v_and_b32 v160, 1, v160\n"
" v_lshl_or_b32 v230, v160, 13, v230\n"
" v_lshrrev_b32 v160, 21, v231\n"
" v_lshrrev_b32_e64 v160, v160, s76\n"
" v_and_b32 v160, 1, v160\n"
" v_lshl_or_b32 v231, v160, 13, v231\n"
" v_lshrrev_b32 v160, 21, v232\n"
" v_lshrrev_b32_e64 v160, v160, s76\n"
" v_and_b32 v160, 1, v160\n"
" v_lshl_or_b32 v232, v160, 13, v232\n"
" v_lshrrev_b32 v160, 21, v233\n"
" v_lshrrev_b32_e64 v160, v160, s76\n"
" v_and_b32 v160, 1, v160\n"
" v_lshl_or_b32 v233, v160, 13, v233\n"
" v_lshrrev_b32 v160, 21, v234\n"
" v_lshrrev_b32_e64 v160, v160, s76\n"
" v_and_b32 v160, 1, v160\n"
" v_lshl_or_b32 v234, v160, 13, v234\n"
" v_lshrrev_b32 v160, 21, v235\n"
" v_lshrrev_b32_e64 v160, v160, s76\n"
" v_and_b32 v160, 1, v160\n"
" v_lshl_or_b32 v235, v160, 13, v235\n"
" v_lshrrev_b32 v160, 21, v236\n"
" v_lshrrev_b32_e64 v160, v160, s76\n"
" v_and_b32 v160, 1, v160\n"
" v_lshl_or_b32 v236, v160, 13, v236\n"
" s_lshr_b32 s78, s70, 21\n"
" s_lshr_b32 s76, s76, s78\n"
" s_and_b32 s76, s76, 1\n"
" s_lshl_b32 s76, s76, 13\n"
" s_add_i32 s70, s70, s76\n"
" s_and_b32 s84, s29, 1\n"
" s_lshl_b32 s84, s84, 13\n"
" ds_write_b128 v248, v[128:131] offset:0\n"
" ds_write_b128 v248, v[132:135] offset:1024\n"
" ds_write_b128 v248, v[136:139] offset:2048\n"
" ds_write_b128 v248, v[140:143] offset:3072\n"
" ds_write_b128 v248, v[144:147] offset:4096\n"
" v_mov_b32 v160, 0xff800000\n"
" v_mov_b32 v161, 0xff800000\n"
" v_mov_b32 v162, 0xff800000\n"
" v_mov_b32 v163, 0xff800000\n"
" ds_write_b128 v225, v[160:163] offset:32768\n"
" v_mov_b32 v251, v252\n"
" v_mov_b32 v252, 0x3f803f80\n"
" v_mov_b32 v253, 0x3f803f80\n"
" v_mov_b32 v254, 0x3f803f80\n"
" v_mov_b32 v255, 0x3f803f80\n"
" s_mov_b32 s68, 0\n"
" s_mov_b32 s28, 110\n"
" s_mov_b32 s92, 65536\n"
" s_mov_b32 s98, 0x3e0293ee\n"
" s_mov_b32 s99, 0x3e0293ee\n"
" s_mov_b32 s55, 0x00020000\n"
" s_mov_b32 s59, 0x00020000\n"
" s_mov_b32 s63, 0x00020000\n"
" s_lshl_b32 s77, s69, 10\n"
" s_and_b32 s34, s69, 1\n"
" s_lshl_b32 s34, s34, 12\n"
" s_add_i32 s76, s69, -1\n"
" s_cmp_eq_u32 s69, 0\n"
" s_cselect_b32 s76, 15, s76\n"
" s_lshl_b32 s4, s76, 10\n"
" s_lshr_b32 s78, s76, 3\n"
" s_lshl_b32 s78, s78, 13\n"
" s_add_i32 s10, s4, s78\n"
" s_add_i32 s76, s69, 2\n"
" s_cmp_eq_u32 s69, 0\n"
" s_cselect_b32 s76, 16, s76\n"
" s_lshl_b32 s5, s76, 10\n"
" s_lshr_b32 s78, s76, 3\n"
" s_lshl_b32 s78, s78, 13\n"
" s_add_i32 s11, s5, s78\n"
" s_add_i32 s76, s69, 5\n"
" s_cmp_eq_u32 s69, 0\n"
" s_cselect_b32 s76, 17, s76\n"
" s_lshl_b32 s6, s76, 10\n"
" s_lshr_b32 s78, s76, 3\n"
" s_lshl_b32 s78, s78, 13\n"
" s_add_i32 s12, s6, s78\n"
" s_add_i32 s76, s69, 8\n"
" s_cmp_eq_u32 s69, 0\n"
" s_cselect_b32 s76, 18, s76\n"
" s_lshl_b32 s7, s76, 10\n"
" s_lshr_b32 s78, s76, 3\n"
" s_lshl_b32 s78, s78, 13\n"
" s_add_i32 s13, s7, s78\n"
" s_add_i32 s76, s69, 11\n"
" s_cmp_eq_u32 s69, 0\n"
" s_cselect_b32 s76, 19, s76\n"
" s_lshl_b32 s8, s76, 10\n"
" s_lshr_b32 s78, s76, 3\n"
" s_lshl_b32 s78, s78, 13\n"
" s_add_i32 s14, s8, s78\n"
" s_add_i32 s87, s68, 0\n"
" s_cmp_lt_u32 s87, 64\n"
" s_cselect_b32 s86, s87, -1\n"
" s_add_i32 s87, s87, -8\n"
" s_max_i32 s87, s87, -1\n"
" s_max_i32 s76, s86, 0\n"
" s_and_b32 s76, s76, 3\n"
" s_add_i32 s76, s76, 0\n"
" s_lshl_b32 s76, s76, 15\n"
" s_add_u32 s52, s64, s76\n"
" s_addc_u32 s53, s65, 0\n"
" s_cmp_ge_i32 s86, 0\n"
" s_cselect_b32 s54, 24576, 0\n"
" s_max_i32 s76, s87, 0\n"
" s_and_b32 s76, s76, 3\n"
" s_add_i32 s76, s76, 4\n"
" s_lshl_b32 s76, s76, 15\n"
" s_add_u32 s56, s64, s76\n"
" s_addc_u32 s57, s65, 0\n"
" s_cmp_ge_i32 s87, 0\n"
" s_cselect_b32 s58, 24576, 0\n"
" s_cmp_ge_u32 s69, 2\n"
" s_cselect_b32 s60, s56, s52\n"
" s_cselect_b32 s61, s57, s53\n"
" s_cselect_b32 s62, s58, s54\n"
" s_and_b32 s78, s86, 1\n"
" s_add_i32 s78, s78, s73\n"
" s_add_i32 s78, s78, 0\n"
" s_lshl_b32 s78, s78, 14\n"
" s_and_b32 s76, s86, 2\n"
" s_lshl_b32 s76, s76, 11\n"
" s_add_i32 s78, s78, s76\n"
" s_cmp_ge_i32 s86, 0\n"
" s_cselect_b32 s78, s78, 0x40000000\n"
" s_and_b32 s79, s87, 1\n"
" s_add_i32 s79, s79, s73\n"
" s_add_i32 s79, s79, 2\n"
" s_lshl_b32 s79, s79, 14\n"
" s_and_b32 s76, s87, 2\n"
" s_lshl_b32 s76, s76, 11\n"
" s_add_i32 s79, s79, s76\n"
" s_cmp_ge_i32 s87, 0\n"
" s_cselect_b32 s79, s79, 0x40000000\n"
" v_mov_b32 v222, s78\n"
" v_mov_b32 v221, s79\n"
" v_cndmask_b32 v223, v222, v221, s[82:83]\n"
" v_cndmask_b32 v222, v221, v222, s[80:81]\n"
" v_add_u32 v222, v222, v237\n"
" v_add_u32 v223, v223, v238\n"
" v_mov_b32 v220, -2\n"
" s_memrealtime s[74:75]\n"
" s_waitcnt lgkmcnt(0)\n"
" s_mov_b32 s71, s74\n"
" s_mov_b32 s94, s84\n"
" s_mov_b32 s35, 16\n"
" s_branch .Lch_first_%=\n"
".Lch_loop_%=:\n"
" s_mov_b32 s94, s84\n"
".Lch_w2_%=:\n"
" s_memrealtime s[74:75]\n"
" s_waitcnt lgkmcnt(0)\n"
" s_sub_i32 s76, s74, s71\n"
" s_cmp_lt_i32 s76, 0\n"
" s_cbranch_scc1 .Lch_w2_%=\n"
" s_mov_b32 m0, s4\n"
" s_add_i32 s76, s94, s10\n"
" buffer_load_dwordx4 v225, s[36:39], s76 offen sc1 lds\n"
" s_barrier\n"
" s_mov_b32 m0, s5\n"
" s_add_i32 s76, s94, s11\n"
" buffer_load_dwordx4 v225, s[36:39], s76 offen sc1 lds\n"
" s_mov_b32 m0, s6\n"
" s_add_i32 s76, s94, s12\n"
" buffer_load_dwordx4 v225, s[36:39], s76 offen sc1 lds\n"
" s_mov_b32 m0, s7\n"
" s_add_i32 s76, s94, s13\n"
" buffer_load_dwordx4 v225, s[36:39], s76 offen sc1 lds\n"
" s_mov_b32 m0, s8\n"
" s_add_i32 s76, s94, s14\n"
" buffer_load_dwordx4 v225, s[36:39], s76 offen sc1 lds\n"
" s_mov_b32 s35, 0\n"
".Lch_first_%=:\n"
" s_mov_b32 s9, s28\n"
" s_add_i32 s95, s94, 36864\n"
" s_add_i32 s84, s94, 65536\n"
" s_and_b32 s84, s84, 0x3ffff\n"
" s_add_i32 s93, s92, s70\n"
" s_xor_b32 s92, s92, 65536\n"
" s_add_i32 s85, s94, 196608\n"
" s_and_b32 s85, s85, 0x3ffff\n"
" s_add_i32 s79, s93, 4096\n"
" v_mov_b32 v208, -1\n"
" v_mov_b32 v209, -1\n"
" v_mov_b32 v210, -1\n"
" v_mov_b32 v211, -1\n"
" s_cmp_eq_u32 s69, 0\n"
" s_waitcnt vmcnt(6)\n"
" s_cbranch_scc1 .Lch_flin_%=\n"
" s_waitcnt vmcnt(5)\n"
".Lch_flin_%=:\n"
" v_readlane_b32 s74, v220, 0\n"
" v_readlane_b32 s75, v220, 1\n"
" s_sub_i32 s74, s74, s86\n"
" s_sub_i32 s75, s75, s87\n"
" s_or_b32 s74, s74, s75\n"
" s_cmp_ge_i32 s74, 0\n"
" s_barrier\n"
" s_cbranch_scc0 .Lch_late_%=\n"
".Lch_kvok_%=:\n"
" v_xor_b32 v241, 0x80, v241\n"
" v_mov_b32 v220, s68\n"
".Lch_again_%=:\n"
" buffer_load_dwordx4 v[180:183], v225, s[60:63], s34 offen offset:0 sc1\n"
" buffer_load_dwordx4 v[184:187], v225, s[60:63], s34 offen offset:1024 sc1\n"
" buffer_load_dwordx4 v[188:191], v225, s[60:63], s34 offen offset:2048 sc1\n"
" buffer_load_dwordx4 v[192:195], v225, s[60:63], s34 offen offset:3072 sc1\n"
" s_cmp_eq_u32 s69, 0\n"
" s_cbranch_scc1 .Lch_ws1_%=\n"
" s_waitcnt vmcnt(8)\n"
".Lch_ws1_%=:\n"
".Lch_q_%=:\n"
" s_barrier\n"
" ds_read_b128 v[128:131], v225 offset:0\n"
" ds_read_b128 v[132:135], v225 offset:1024\n"
" ds_read_b128 v[136:139], v225 offset:2048\n"
" s_waitcnt lgkmcnt(2)\n"
" v_mfma_f32_16x16x32_bf16 v[160:163], a[0:3], v[128:131], 0\n"
" v_mfma_f32_16x16x32_bf16 v[164:167], a[4:7], v[128:131], 0\n"
" s_cbranch_scc1 .Lch_ws2_%=\n"
" s_waitcnt vmcnt(7)\n"
".Lch_ws2_%=:\n"
" s_waitcnt lgkmcnt(1)\n"
" v_mfma_f32_16x16x32_bf16 v[176:179], v[128:131], v[64:67], 0\n"
" s_barrier\n"
" v_mfma_f32_16x16x32_bf16 v[160:163], a[8:11], v[132:135], v[160:163]\n"
" ds_read_b128 v[140:143], v225 offset:3072\n"
" v_mfma_f32_16x16x32_bf16 v[164:167], a[12:15], v[132:135], v[164:167]\n"
" ds_read_b128 v[144:147], v225 offset:4096\n"
" v_mfma_f32_16x16x32_bf16 v[176:179], v[132:135], v[68:71], v[176:179]\n"
" ds_read_b128 v[148:151], v225 offset:5120\n"
" s_waitcnt lgkmcnt(3)\n"
" v_mfma_f32_16x16x32_bf16 v[160:163], a[16:19], v[136:139], v[160:163]\n"
" v_mfma_f32_16x16x32_bf16 v[164:167], a[20:23], v[136:139], v[164:167]\n"
" s_cbranch_scc1 .Lch_ws3_%=\n"
" s_waitcnt vmcnt(5)\n"
".Lch_ws3_%=:\n"
" s_waitcnt lgkmcnt(2)\n"
" v_mfma_f32_16x16x32_bf16 v[176:179], v[136:139], v[72:75], v[176:179]\n"
" s_barrier\n"
" v_mfma_f32_16x16x32_bf16 v[160:163], a[24:27], v[140:143], v[160:163]\n"
" ds_read_b128 v[152:155], v225 offset:6144\n"
" v_mfma_f32_16x16x32_bf16 v[164:167], a[28:31], v[140:143], v[164:167]\n"
" ds_read_b128 v[156:159], v225 offset:7168\n"
" v_mfma_f32_16x16x32_bf16 v[176:179], v[140:143], v[76:79], v[176:179]\n"
" ds_read_b128 v[128:131], v225 offset:8192\n"
" s_waitcnt lgkmcnt(3)\n"
" v_mfma_f32_16x16x32_bf16 v[160:163], a[32:35], v[144:147], v[160:163]\n"
" ds_read_b128 v[132:135], v225 offset:9216\n"
" v_mfma_f32_16x16x32_bf16 v[164:167], a[36:39], v[144:147], v[164:167]\n"
" ds_read_b128 v[136:139], v225 offset:10240\n"
" v_mfma_f32_16x16x32_bf16 v[160:163], a[40:43], v[148:151], v[160:163]\n"
" ds_read_b128 v[140:143], v225 offset:11264\n"
" v_mfma_f32_16x16x32_bf16 v[164:167], a[44:47], v[148:151], v[164:167]\n"
" s_waitcnt vmcnt(4)\n"
" s_waitcnt lgkmcnt(4)\n"
" v_mfma_f32_16x16x32_bf16 v[160:163], a[48:51], v[152:155], v[160:163]\n"
" s_barrier\n"
" v_mfma_f32_16x16x32_bf16 v[164:167], a[52:55], v[152:155], v[164:167]\n"
" ds_read_b128 v[144:147], v225 offset:12288\n"
" v_mfma_f32_16x16x32_bf16 v[160:163], a[56:59], v[156:159], v[160:163]\n"
" ds_read_b128 v[148:151], v225 offset:13312\n"
" v_mfma_f32_16x16x32_bf16 v[164:167], a[60:63], v[156:159], v[164:167]\n"
" ds_read_b128 v[152:155], v225 offset:14336\n"
" s_waitcnt lgkmcnt(3)\n"
" v_mfma_f32_16x16x32_bf16 v[160:163], a[64:67], v[128:131], v[160:163]\n"
" ds_read_b128 v[156:159], v225 offset:15360\n"
" v_mfma_f32_16x16x32_bf16 v[164:167], a[68:71], v[128:131], v[164:167]\n"
" ds_read_b128 v[128:131], v225 offset:16384\n"
" v_mfma_f32_16x16x32_bf16 v[160:163], a[72:75], v[132:135], v[160:163]\n"
" buffer_load_dwordx4 v[196:199], v224, s[52:55], s66 offen sc1\n"
" v_mfma_f32_16x16x32_bf16 v[164:167], a[76:79], v[132:135], v[164:167]\n"
" ds_read_b128 v[132:135], v225 offset:17408\n"
" v_mfma_f32_16x16x32_bf16 v[160:163], a[80:83], v[136:139], v[160:163]\n"
" ds_read_b128 v[212:215], v248 offset:0\n"
" v_mfma_f32_16x16x32_bf16 v[164:167], a[84:87], v[136:139], v[164:167]\n"
" ds_read_b128 v[136:139], v225 offset:18432\n"
" v_mfma_f32_16x16x32_bf16 v[160:163], a[88:91], v[140:143], v[160:163]\n"
" buffer_load_dwordx4 v[200:203], v224, s[56:59], s66 offen sc1\n"
" v_mfma_f32_16x16x32_bf16 v[164:167], a[92:95], v[140:143], v[164:167]\n"
" ds_read_b128 v[140:143], v225 offset:19456\n"
" s_waitcnt lgkmcnt(2)\n"
" v_mfma_f32_16x16x32_bf16 v[160:163], a[96:99], v[144:147], v[160:163]\n"
" ds_read_b128 v[216:219], v248 offset:1024\n"
" v_mfma_f32_16x16x32_bf16 v[164:167], a[100:103], v[144:147], v[164:167]\n"
" ds_read_b128 v[124:127], v248 offset:2048\n"
" v_mfma_f32_16x16x32_bf16 v[176:179], v[144:147], v[212:215], v[176:179]\n"
" buffer_store_dwordx4 v[208:211], v225, s[40:43], s93 offen offset:0 sc1\n"
" v_mfma_f32_16x16x32_bf16 v[160:163], a[104:107], v[148:151], v[160:163]\n"
" ds_read_b128 v[168:171], v248 offset:3072\n"
" v_mfma_f32_16x16x32_bf16 v[164:167], a[108:111], v[148:151], v[164:167]\n"
" ds_read_b128 v[172:175], v248 offset:4096\n"
" v_mfma_f32_16x16x32_bf16 v[160:163], a[112:115], v[152:155], v[160:163]\n"
" buffer_store_dwordx4 v[208:211], v225, s[40:43], s93 offen offset:1024 sc1\n"
" s_waitcnt lgkmcnt(3)\n"
" v_mfma_f32_16x16x32_bf16 v[176:179], v[148:151], v[216:219], v[176:179]\n"
" v_mfma_f32_16x16x32_bf16 v[164:167], a[116:119], v[152:155], v[164:167]\n"
" s_waitcnt lgkmcnt(1)\n"
" v_mfma_f32_16x16x32_bf16 v[176:179], v[152:155], v[124:127], v[176:179]\n"
" v_mfma_f32_16x16x32_bf16 v[160:163], a[120:123], v[156:159], v[160:163]\n"
" buffer_store_dwordx4 v[208:211], v225, s[40:43], s93 offen offset:2048 sc1\n"
" v_mfma_f32_16x16x32_bf16 v[164:167], a[124:127], v[156:159], v[164:167]\n"
" v_mfma_f32_16x16x32_bf16 v[176:179], v[156:159], v[168:171], v[176:179]\n"
" s_waitcnt lgkmcnt(0)\n"
" v_mfma_f32_16x16x32_bf16 v[160:163], a[128:131], v[128:131], v[160:163]\n"
" buffer_store_dwordx4 v[208:211], v225, s[40:43], s93 offen offset:3072 sc1\n"
" v_mfma_f32_16x16x32_bf16 v[164:167], a[132:135], v[128:131], v[164:167]\n"
" v_mfma_f32_16x16x32_bf16 v[176:179], v[128:131], v[172:175], v[176:179]\n"
" buffer_store_dwordx2 v[208:209], v249, s[36:39], s85 offen sc1\n"
" v_mfma_f32_16x16x32_bf16 v[160:163], a[136:139], v[132:135], v[160:163]\n"
" v_mfma_f32_16x16x32_bf16 v[164:167], a[140:143], v[132:135], v[164:167]\n"
" buffer_store_dwordx4 v[208:211], v225, s[40:43], s79 offen offset:0 sc1\n"
" v_mfma_f32_16x16x32_bf16 v[160:163], a[144:147], v[136:139], v[160:163]\n"
" v_mfma_f32_16x16x32_bf16 v[164:167], a[148:151], v[136:139], v[164:167]\n"
" v_mfma_f32_16x16x32_bf16 v[160:163], a[152:155], v[140:143], v[160:163]\n"
" v_mfma_f32_16x16x32_bf16 v[164:167], a[156:159], v[140:143], v[164:167]\n"
" v_mfma_f32_16x16x32_bf16 v[168:171], a[208:211], v[144:147], 0\n"
" s_nop 2\n"
" v_mfma_f32_16x16x32_bf16 v[172:175], v[32:35], v[144:147], 0\n"
" s_nop 1\n"
" v_cvt_pk_bf16_f32 v204, v160, v161\n"
" v_mfma_f32_16x16x32_bf16 v[168:171], a[212:215], v[148:151], v[168:171]\n"
" v_cvt_pk_bf16_f32 v205, v162, v163\n"
" v_cvt_pk_bf16_f32 v206, v164, v165\n"
" v_mfma_f32_16x16x32_bf16 v[172:175], v[36:39], v[148:151], v[172:175]\n"
" s_waitcnt vmcnt(8)\n"
" v_cvt_pk_bf16_f32 v207, v166, v167\n"
" v_cmp_u_f32 vcc, v164, v164\n"
" v_mfma_f32_16x16x32_bf16 v[168:171], a[216:219], v[152:155], v[168:171]\n"
" ds_write_b128 v224, v[204:207] offset:20480\n"
" v_mfma_f32_16x16x32_bf16 v[172:175], v[40:43], v[152:155], v[172:175]\n"
" s_cbranch_vccnz .Lch_slow2_%=\n"
".Lch_qok_%=:\n"
" s_memrealtime s[74:75]\n"
" v_mfma_f32_16x16x32_bf16 v[168:171], a[220:223], v[156:159], v[168:171]\n"
" v_mfma_f32_16x16x32_bf16 v[172:175], v[44:47], v[156:159], v[172:175]\n"
" v_mfma_f32_16x16x32_bf16 v[168:171], a[224:227], v[128:131], v[168:171]\n"
" s_waitcnt lgkmcnt(0)\n"
" s_barrier\n"
" ds_read_b128 v[160:163], v225 offset:20480\n"
" v_mfma_f32_16x16x32_bf16 v[172:175], v[48:51], v[128:131], v[172:175]\n"
" ds_read_b128 v[164:167], v225 offset:21504\n"
" v_mfma_f32_16x16x32_bf16 v[168:171], a[228:231], v[132:135], v[168:171]\n"
" ds_read_b128 v[212:215], v225 offset:22528\n"
" v_mfma_f32_16x16x32_bf16 v[172:175], v[52:55], v[132:135], v[172:175]\n"
" ds_read_b128 v[216:219], v225 offset:23552\n"
" v_mfma_f32_16x16x32_bf16 v[176:179], v[132:135], v[112:115], v[176:179]\n"
" ds_read_b128 v[144:147], v225 offset:0\n"
" s_waitcnt lgkmcnt(4)\n"
" v_mfma_f32_16x16x32_bf16 v[124:127], v[180:183], v[160:163], 0\n"
" ds_read_b128 v[148:151], v225 offset:1024\n"
" s_waitcnt lgkmcnt(4)\n"
" v_mfma_f32_16x16x32_bf16 v[124:127], v[184:187], v[164:167], v[124:127]\n"
" ds_read_b128 v[152:155], v225 offset:2048\n"
" s_waitcnt lgkmcnt(4)\n"
" v_mfma_f32_16x16x32_bf16 v[124:127], v[188:191], v[212:215], v[124:127]\n"
" ds_read_b128 v[156:159], v225 offset:3072\n"
" s_waitcnt lgkmcnt(4)\n"
" v_mfma_f32_16x16x32_bf16 v[124:127], v[192:195], v[216:219], v[124:127]\n"
" s_mov_b32 s72, s74\n"
" s_add_i32 s76, s74, s9\n"
" v_min_u32_e64 v221, s76, -2\n"
" v_mfma_f32_16x16x32_bf16 v[168:171], a[232:235], v[136:139], v[168:171]\n"
" buffer_store_dword v221, v250, s[36:39], s95 offen sc1\n"
" s_nop 1\n"
" v_mfma_f32_16x16x32_bf16 v[172:175], v[56:59], v[136:139], v[172:175]\n"
" ds_write_b128 v224, v[124:127] offset:24576\n"
" v_mfma_f32_16x16x32_bf16 v[176:179], v[136:139], v[116:119], v[176:179]\n"
" ds_read_b128 v[128:131], v225 offset:4096\n"
" v_mfma_f32_16x16x32_bf16 v[168:171], a[236:239], v[140:143], v[168:171]\n"
" ds_read_b128 v[132:135], v225 offset:5120\n"
" v_mfma_f32_16x16x32_bf16 v[172:175], v[60:63], v[140:143], v[172:175]\n"
" ds_read_b128 v[136:139], v225 offset:6144\n"
" v_mfma_f32_16x16x32_bf16 v[176:179], v[140:143], v[120:123], v[176:179]\n"
" s_waitcnt lgkmcnt(3)\n"
" s_barrier\n"
" ds_read_b128 v[160:163], v226\n"
" v_mfma_f32_16x16x32_bf16 v[168:171], a[160:163], v[144:147], v[168:171]\n"
" ds_read_b128 v[164:167], v226 offset:1024\n"
" v_mfma_f32_16x16x32_bf16 v[172:175], a[240:243], v[144:147], v[172:175]\n"
" ds_read_b128 v[140:143], v225 offset:7168\n"
" v_mfma_f32_16x16x32_bf16 v[168:171], a[164:167], v[148:151], v[168:171]\n"
" ds_read_b128 v[144:147], v225 offset:8192\n"
" v_mfma_f32_16x16x32_bf16 v[172:175], a[244:247], v[148:151], v[172:175]\n"
" ds_read_b128 v[148:151], v225 offset:9216\n"
" v_mfma_f32_16x16x32_bf16 v[168:171], a[168:171], v[152:155], v[168:171]\n"
" s_waitcnt lgkmcnt(3)\n"
" v_max3_f32 v124, v160, v161, v162\n"
" v_max3_f32 v125, v163, v164, v165\n"
" v_mfma_f32_16x16x32_bf16 v[172:175], a[248:251], v[152:155], v[172:175]\n"
" v_max3_f32 v124, v124, v166, v167\n"
" v_max_f32 v125, v124, v125\n"
" v_mfma_f32_16x16x32_bf16 v[168:171], a[172:175], v[156:159], v[168:171]\n"
" ds_read_b128 v[152:155], v225 offset:10240\n"
" v_mov_b32 v124, v125\n"
" v_mfma_f32_16x16x32_bf16 v[172:175], a[252:255], v[156:159], v[172:175]\n"
" s_nop 0\n"
" v_permlane16_swap_b32 v124, v125\n"
" v_mfma_f32_16x16x32_bf16 v[168:171], a[176:179], v[128:131], v[168:171]\n"
" v_max_f32 v124, v124, v125\n"
" v_max_f32 v125, v124, v125\n"
" v_mfma_f32_16x16x32_bf16 v[172:175], v[0:3], v[128:131], v[172:175]\n"
" s_nop 0\n"
" v_permlane32_swap_b32 v124, v125\n"
" v_mfma_f32_16x16x32_bf16 v[168:171], a[180:183], v[132:135], v[168:171]\n"
" s_waitcnt vmcnt(7)\n"
" v_max_f32 v124, v124, v125\n"
" v_mul_f32 v126, s98, v124\n"
" v_mfma_f32_16x16x32_bf16 v[172:175], v[4:7], v[132:135], v[172:175]\n"
" ds_read_b128 v[156:159], v225 offset:11264\n"
" v_fma_f32 v160, v160, s98, -v126\n"
" v_mfma_f32_16x16x32_bf16 v[168:171], a[184:187], v[136:139], v[168:171]\n"
" v_fma_f32 v161, v161, s98, -v126\n"
" v_fma_f32 v162, v162, s98, -v126\n"
" v_mfma_f32_16x16x32_bf16 v[172:175], v[8:11], v[136:139], v[172:175]\n"
" s_waitcnt lgkmcnt(1)\n"
" v_fma_f32 v163, v163, s98, -v126\n"
" v_fma_f32 v164, v164, s98, -v126\n"
" v_mfma_f32_16x16x32_bf16 v[168:171], a[188:191], v[140:143], v[168:171]\n"
" v_fma_f32 v165, v165, s98, -v126\n"
" v_fma_f32 v166, v166, s98, -v126\n"
" v_mfma_f32_16x16x32_bf16 v[172:175], v[12:15], v[140:143], v[172:175]\n"
" v_exp_f32 v160, v160\n"
" v_mfma_f32_16x16x32_bf16 v[168:171], a[192:195], v[144:147], v[168:171]\n"
" v_exp_f32 v161, v161\n"
" v_mfma_f32_16x16x32_bf16 v[172:175], v[16:19], v[144:147], v[172:175]\n"
" v_exp_f32 v162, v162\n"
" v_mfma_f32_16x16x32_bf16 v[168:171], a[196:199], v[148:151], v[168:171]\n"
" v_exp_f32 v163, v163\n"
" v_mfma_f32_16x16x32_bf16 v[172:175], v[20:23], v[148:151], v[172:175]\n"
" s_waitcnt lgkmcnt(0)\n"
" v_exp_f32 v164, v164\n"
" v_mfma_f32_16x16x32_bf16 v[168:171], a[200:203], v[152:155], v[168:171]\n"
" v_exp_f32 v165, v165\n"
" v_mfma_f32_16x16x32_bf16 v[172:175], v[24:27], v[152:155], v[172:175]\n"
" v_exp_f32 v166, v166\n"
" v_mfma_f32_16x16x32_bf16 v[168:171], a[204:207], v[156:159], v[168:171]\n"
" v_fma_f32 v167, v167, s98, -v126\n"
" v_cvt_pk_bf16_f32 v192, v160, v161\n"
" v_mfma_f32_16x16x32_bf16 v[172:175], v[28:31], v[156:159], v[172:175]\n"
" v_exp_f32 v167, v167\n"
" v_mfma_f32_16x16x32_bf16 v[176:179], v[128:131], v[80:83], v[176:179]\n"
" v_cvt_pk_bf16_f32 v193, v162, v163\n"
" v_cvt_pk_bf16_f32 v194, v164, v165\n"
" v_mfma_f32_16x16x32_bf16 v[176:179], v[132:135], v[84:87], v[176:179]\n"
" v_cvt_pk_bf16_f32 v208, v168, v169\n"
" v_cvt_pk_bf16_f32 v209, v170, v171\n"
" v_cvt_pk_bf16_f32 v195, v166, v167\n"
" v_cvt_pk_bf16_f32 v210, v172, v173\n"
" v_cvt_pk_bf16_f32 v211, v174, v175\n"
" v_mfma_f32_16x16x32_bf16 v[180:183], v[196:199], v[192:195], 0\n"
" s_nop 0\n"
" v_mfma_f32_16x16x32_bf16 v[168:171], v[208:211], v[204:207], 0\n"
" s_nop 1\n"
" v_lshlrev_b32 v221, 1, v225\n"
" v_mfma_f32_16x16x32_bf16 v[184:187], v[200:203], v[192:195], 0\n"
" s_nop 2\n"
" v_mfma_f32_16x16x32_bf16 v[188:191], v[252:255], v[192:195], 0\n"
" ds_write_b128 v224, v[168:171] offset:28672\n"
" v_mfma_f32_16x16x32_bf16 v[176:179], v[136:139], v[88:91], v[176:179]\n"
" s_nop 1\n"
" v_cndmask_b32 v180, v184, v180, s[80:81]\n"
" v_cndmask_b32 v181, v185, v181, s[80:81]\n"
" v_mfma_f32_16x16x32_bf16 v[176:179], v[140:143], v[92:95], v[176:179]\n"
" v_cndmask_b32 v182, v186, v182, s[80:81]\n"
" v_cndmask_b32 v183, v187, v183, s[80:81]\n"
" v_mfma_f32_16x16x32_bf16 v[176:179], v[144:147], v[96:99], v[176:179]\n"
" v_mfma_f32_16x16x32_bf16 v[176:179], v[148:151], v[100:103], v[176:179]\n"
" s_waitcnt lgkmcnt(0)\n"
" s_barrier\n"
" ds_read_b128 v[196:199], v227\n"
" ds_read_b128 v[200:203], v225 offset:29696\n"
" v_mfma_f32_16x16x32_bf16 v[176:179], v[152:155], v[104:107], v[176:179]\n"
" ds_read_b128 v[212:215], v225 offset:30720\n"
" v_mfma_f32_16x16x32_bf16 v[176:179], v[156:159], v[108:111], v[176:179]\n"
" ds_read_b128 v[216:219], v225 offset:31744\n"
" ds_read_b128 v[128:131], v228 offset:0\n"
" ds_read_b128 v[136:139], v228 offset:2048\n"
" ds_read_b128 v[132:135], v228 offset:1024\n"
" s_waitcnt lgkmcnt(5)\n"
" v_pk_add_f32 v[196:197], v[196:197], v[200:201]\n"
" v_pk_add_f32 v[198:199], v[198:199], v[202:203]\n"
" s_nop 0\n"
" v_cvt_pk_bf16_f32 v184, v176, v177\n"
" v_cvt_pk_bf16_f32 v185, v178, v179\n"
" s_waitcnt lgkmcnt(3)\n"
" v_pk_add_f32 v[212:213], v[212:213], v[216:217]\n"
" v_pk_add_f32 v[214:215], v[214:215], v[218:219]\n"
" v_pk_add_f32 v[196:197], v[196:197], v[212:213]\n"
" v_pk_add_f32 v[198:199], v[198:199], v[214:215]\n"
" v_pk_fma_f32 v[196:197], v[196:197], s[98:99], v[126:127] op_sel_hi:[1,1,0] neg_lo:[0,0,1] neg_hi:[0,0,1]\n"
" v_pk_fma_f32 v[198:199], v[198:199], s[98:99], v[126:127] op_sel_hi:[1,1,0] neg_lo:[0,0,1] neg_hi:[0,0,1]\n"
" v_max3_f32 v204, v196, v197, v198\n"
" v_exp_f32 v200, v196\n"
" v_max_f32 v204, v204, v199\n"
" v_exp_f32 v201, v197\n"
" v_cmp_lt_f32 vcc, 0x42000000, v204\n"
" v_exp_f32 v202, v198\n"
" v_exp_f32 v203, v199\n"
" s_cbranch_vccnz .Lch_remax_%=\n"
".Lch_maxok_%=:\n"
" v_cvt_pk_bf16_f32 v206, v200, v201\n"
" v_cvt_pk_bf16_f32 v207, v202, v203\n"
" v_min_u32 v204, -2, v184\n"
" v_min_u32 v205, -2, v185\n"
" v_mfma_f32_16x16x16_bf16 v[188:191], v[252:253], v[206:207], v[188:191]\n"
" ds_read_b128 v[140:143], v228 offset:3072\n"
" v_mfma_f32_16x16x16_bf16 v[180:183], v[184:185], v[206:207], v[180:183]\n"
" ds_read_b128 v[144:147], v228 offset:4096\n"
" ds_read_b128 v[148:151], v228 offset:5120\n"
" buffer_load_dword v221, v221, s[36:39], s95 offen sc1\n"
" s_nop 2\n"
" v_rcp_f32 v188, v188\n"
" s_nop 0\n"
" v_pk_mul_f32 v[180:181], v[180:181], v[188:189] op_sel_hi:[1,0]\n"
" v_pk_mul_f32 v[182:183], v[182:183], v[188:189] op_sel_hi:[1,0]\n"
" v_cvt_pk_bf16_f32 v180, v180, v181\n"
" v_cvt_pk_bf16_f32 v181, v182, v183\n"
" v_pk_min_u16 v180, v180, -2 op_sel_hi:[1,0]\n"
" v_pk_min_u16 v181, v181, -2 op_sel_hi:[1,0]\n"
" ds_write_b64 v229, v[180:181]\n"
" ds_read_b128 v[152:155], v228 offset:6144\n"
" ds_read_b128 v[156:159], v228 offset:7168\n"
" ds_read_b128 v[160:163], v228 offset:8192\n"
" ds_read_b128 v[164:167], v228 offset:9216\n"
" s_cmp_eq_u32 s68, 71\n"
" s_waitcnt lgkmcnt(4)\n"
" s_barrier\n"
" ds_read_b128 v[196:199], v225 offset:33792\n"
" ds_read_b128 v[200:203], v225 offset:34816\n"
" ds_read_b128 v[168:171], v228 offset:10240\n"
" ds_read_b128 v[172:175], v228 offset:11264\n"
" s_cbranch_scc1 .Lch_end_%=\n"
" s_waitcnt lgkmcnt(3)\n"
" v_mfma_f32_16x16x32_bf16 v[176:179], v[128:131], v[196:199], 0\n"
" ds_read_b128 v[128:131], v228 offset:12288\n"
" v_mfma_f32_16x16x32_bf16 v[180:183], v[136:139], v[196:199], 0\n"
" ds_read_b128 v[136:139], v228 offset:14336\n"
" s_waitcnt lgkmcnt(4)\n"
" v_mfma_f32_16x16x32_bf16 v[176:179], v[132:135], v[200:203], v[176:179]\n"
" ds_read_b128 v[132:135], v228 offset:13312\n"
" v_mfma_f32_16x16x32_bf16 v[180:183], v[140:143], v[200:203], v[180:183]\n"
" ds_read_b128 v[140:143], v228 offset:15360\n"
" v_mfma_f32_16x16x32_bf16 v[184:187], v[144:147], v[196:199], 0\n"
" ds_read_b128 v[144:147], v228 offset:16384\n"
" v_mfma_f32_16x16x32_bf16 v[184:187], v[148:151], v[200:203], v[184:187]\n"
" ds_read_b128 v[148:151], v228 offset:17408\n"
" s_nop 0\n"
" v_mfma_f32_16x16x32_bf16 v[188:191], v[152:155], v[196:199], 0\n"
" ds_read_b128 v[152:155], v228 offset:18432\n"
" v_cvt_pk_bf16_f32 v192, v176, v177\n"
" v_cvt_pk_bf16_f32 v193, v178, v179\n"
" v_mfma_f32_16x16x32_bf16 v[188:191], v[156:159], v[200:203], v[188:191]\n"
" ds_read_b128 v[156:159], v228 offset:19456\n"
" v_cvt_pk_bf16_f32 v194, v180, v181\n"
" v_cvt_pk_bf16_f32 v195, v182, v183\n"
" s_waitcnt lgkmcnt(8)\n"
" v_mfma_f32_16x16x32_bf16 v[212:215], v[160:163], v[196:199], 0\n"
" buffer_store_dwordx4 v[192:195], v230, s[40:43], s92 offen sc1\n"
" v_mfma_f32_16x16x32_bf16 v[212:215], v[164:167], v[200:203], v[212:215]\n"
" v_cvt_pk_bf16_f32 v124, v184, v185\n"
" v_cvt_pk_bf16_f32 v125, v186, v187\n"
" v_mfma_f32_16x16x32_bf16 v[216:219], v[168:171], v[196:199], 0\n"
" s_nop 0\n"
" v_cvt_pk_bf16_f32 v126, v188, v189\n"
" v_cvt_pk_bf16_f32 v127, v190, v191\n"
" v_mfma_f32_16x16x32_bf16 v[216:219], v[172:175], v[200:203], v[216:219]\n"
" buffer_store_dwordx4 v[124:127], v231, s[40:43], s92 offen sc1\n"
" s_waitcnt lgkmcnt(4)\n"
" v_mfma_f32_16x16x32_bf16 v[176:179], v[128:131], v[196:199], 0\n"
" s_nop 0\n"
" v_cvt_pk_bf16_f32 v192, v212, v213\n"
" v_cvt_pk_bf16_f32 v193, v214, v215\n"
" v_mfma_f32_16x16x32_bf16 v[176:179], v[132:135], v[200:203], v[176:179]\n"
" s_nop 1\n"
" v_mfma_f32_16x16x32_bf16 v[180:183], v[136:139], v[196:199], 0\n"
" v_cvt_pk_bf16_f32 v194, v216, v217\n"
" v_cvt_pk_bf16_f32 v195, v218, v219\n"
" v_mfma_f32_16x16x32_bf16 v[180:183], v[140:143], v[200:203], v[180:183]\n"
" buffer_store_dwordx4 v[192:195], v232, s[40:43], s92 offen sc1\n"
" s_waitcnt lgkmcnt(0)\n"
" v_mfma_f32_16x16x32_bf16 v[184:187], v[144:147], v[196:199], 0\n"
" v_cvt_pk_bf16_f32 v124, v176, v177\n"
" v_cvt_pk_bf16_f32 v125, v178, v179\n"
" v_mfma_f32_16x16x32_bf16 v[184:187], v[148:151], v[200:203], v[184:187]\n"
" s_add_i32 s79, s72, 14\n"
" s_mov_b32 s91, 0\n"
" v_mfma_f32_16x16x32_bf16 v[188:191], v[152:155], v[196:199], 0\n"
" v_cvt_pk_bf16_f32 v126, v180, v181\n"
" v_cvt_pk_bf16_f32 v127, v182, v183\n"
" v_mfma_f32_16x16x32_bf16 v[188:191], v[156:159], v[200:203], v[188:191]\n"
" buffer_store_dwordx4 v[124:127], v233, s[40:43], s92 offen sc1\n"
" s_nop 1\n"
" v_cvt_pk_bf16_f32 v192, v184, v185\n"
" v_cvt_pk_bf16_f32 v193, v186, v187\n"
" s_nop 2\n"
" v_cvt_pk_bf16_f32 v194, v188, v189\n"
" v_cvt_pk_bf16_f32 v195, v190, v191\n"
" buffer_store_dwordx4 v[192:195], v234, s[40:43], s92 offen sc1\n"
" s_memrealtime s[74:75]\n"
" s_waitcnt vmcnt(5)\n"
" v_cmp_eq_u32 vcc, -1, v221\n"
" v_subrev_u32 v221, s72, v221\n"
" s_nop 1\n"
" v_max_i32_dpp v221, v221, v221 quad_perm:[1,0,3,2] row_mask:0xf bank_mask:0xf\n"
" s_nop 1\n"
" v_max_i32_dpp v221, v221, v221 quad_perm:[2,3,0,1] row_mask:0xf bank_mask:0xf\n"
" s_waitcnt lgkmcnt(0)\n"
" s_sub_i32 s76, s74, s72\n"
" v_max_i32_dpp v221, v221, v221 row_half_mirror row_mask:0xf bank_mask:0xf\n"
" s_add_i32 s28, s28, 2\n"
" s_min_i32 s28, s28, s76\n"
" v_max_i32_dpp v221, v221, v221 row_mirror row_mask:0xf bank_mask:0xf\n"
" s_cbranch_vccnz .Lch_tslate_%=\n"
" v_readlane_b32 s75, v221, 0\n"
" v_readlane_b32 s30, v221, 16\n"
" v_readlane_b32 s76, v221, 32\n"
" v_readlane_b32 s78, v221, 48\n"
" s_max_i32 s75, s75, s30\n"
" s_max_i32 s76, s76, s78\n"
" s_max_i32 s76, s76, s75\n"
" s_min_u32 s76, s76, 0x4000\n"
" s_add_i32 s79, s79, s76\n"
".Lch_ts_%=:\n"
" s_sub_i32 s76, s74, s79\n"
" s_cmp_ge_i32 s76, 0\n"
" s_cbranch_scc1 .Lch_lt1_%=\n"
".Lch_w1_%=:\n"
" s_memrealtime s[74:75]\n"
" s_waitcnt lgkmcnt(0)\n"
" s_sub_i32 s76, s74, s79\n"
" s_cmp_lt_i32 s76, 0\n"
" s_cbranch_scc1 .Lch_w1_%=\n"
" s_mov_b32 s76, 0\n"
".Lch_lt1_%=:\n"
" s_mul_i32 s76, s76, 88\n"
" s_lshr_b32 s76, s76, 8\n"
" s_sub_i32 s76, s33, s76\n"
" s_cmp_le_i32 s76, 0\n"
" s_cbranch_scc1 .Lch_fire_%=\n"
" s_nop 7\n"
" s_nop 5\n"
" s_cmp_le_i32 s76, 1\n"
" s_cbranch_scc1 .Lch_fire_%=\n"
" s_nop 7\n"
" s_nop 5\n"
" s_cmp_le_i32 s76, 2\n"
" s_cbranch_scc1 .Lch_fire_%=\n"
" s_nop 7\n"
" s_nop 5\n"
" s_cmp_le_i32 s76, 3\n"
" s_cbranch_scc1 .Lch_fire_%=\n"
" s_nop 7\n"
" s_nop 5\n"
".Lch_fire_%=:\n"
" buffer_load_dwordx4 v[144:147], v236, s[40:43], s92 offen sc1\n"
" buffer_load_dwordx4 v[128:131], v235, s[40:43], s92 offen offset:0 sc1\n"
" buffer_load_dwordx4 v[132:135], v235, s[40:43], s92 offen offset:1280 sc1\n"
" buffer_load_dwordx4 v[136:139], v235, s[40:43], s92 offen offset:2560 sc1\n"
" buffer_load_dwordx4 v[140:143], v235, s[40:43], s92 offen offset:3840 sc1\n"
" s_cmp_lt_u32 s68, 3\n"
" s_cselect_b32 s71, 30, 0\n"
" s_add_i32 s71, s71, s79\n"
" s_add_i32 s71, s71, 76\n"
" s_add_i32 s87, s68, 1\n"
" s_cmp_lt_u32 s87, 64\n"
" s_cselect_b32 s86, s87, -1\n"
" s_add_i32 s87, s87, -8\n"
" s_max_i32 s87, s87, -1\n"
" s_max_i32 s76, s86, 0\n"
" s_and_b32 s76, s76, 3\n"
" s_add_i32 s76, s76, 0\n"
" s_lshl_b32 s76, s76, 15\n"
" s_add_u32 s52, s64, s76\n"
" s_addc_u32 s53, s65, 0\n"
" s_cmp_ge_i32 s86, 0\n"
" s_cselect_b32 s54, 24576, 0\n"
" s_max_i32 s76, s87, 0\n"
" s_and_b32 s76, s76, 3\n"
" s_add_i32 s76, s76, 4\n"
" s_lshl_b32 s76, s76, 15\n"
" s_add_u32 s56, s64, s76\n"
" s_addc_u32 s57, s65, 0\n"
" s_cmp_ge_i32 s87, 0\n"
" s_cselect_b32 s58, 24576, 0\n"
" s_cmp_ge_u32 s69, 2\n"
" s_cselect_b32 s60, s56, s52\n"
" s_cselect_b32 s61, s57, s53\n"
" s_cselect_b32 s62, s58, s54\n"
" s_barrier\n"
" v_min_u32 v208, -2, v208\n"
" v_min_u32 v209, -2, v209\n"
" v_min_u32 v210, -2, v210\n"
" v_min_u32 v211, -2, v211\n"
" buffer_store_dwordx4 v[208:211], v222, s[44:47], 0 offen sc1\n"
" buffer_store_dwordx2 v[204:205], v223, s[44:47], 0 offen sc1\n"
" buffer_store_dword v220, v251, s[48:51], 0 offen sc1\n"
" buffer_load_dword v220, v241, s[48:51], 0 offen sc1\n"
" s_and_b32 s78, s86, 1\n"
" s_add_i32 s78, s78, s73\n"
" s_add_i32 s78, s78, 0\n"
" s_lshl_b32 s78, s78, 14\n"
" s_and_b32 s76, s86, 2\n"
" s_lshl_b32 s76, s76, 11\n"
" s_add_i32 s78, s78, s76\n"
" s_cmp_ge_i32 s86, 0\n"
" s_cselect_b32 s78, s78, 0x40000000\n"
" s_and_b32 s79, s87, 1\n"
" s_add_i32 s79, s79, s73\n"
" s_add_i32 s79, s79, 2\n"
" s_lshl_b32 s79, s79, 14\n"
" s_and_b32 s76, s87, 2\n"
" s_lshl_b32 s76, s76, 11\n"
" s_add_i32 s79, s79, s76\n"
" s_cmp_ge_i32 s87, 0\n"
" s_cselect_b32 s79, s79, 0x40000000\n"
" v_mov_b32 v222, s78\n"
" v_mov_b32 v221, s79\n"
" v_cndmask_b32 v223, v222, v221, s[82:83]\n"
" v_cndmask_b32 v222, v221, v222, s[80:81]\n"
" v_add_u32 v222, v222, v237\n"
" v_add_u32 v223, v223, v238\n"
".Lch_h1_%=:\n"
" s_waitcnt vmcnt(8)\n"
" v_mfma_f32_16x16x32_bf16 v[164:167], v[144:147], v[244:247], 0\n"
" s_waitcnt vmcnt(7)\n"
" v_mfma_f32_16x16x32_bf16 v[160:163], v[244:247], v[128:131], 0\n"
" s_waitcnt vmcnt(6)\n"
" v_mfma_f32_16x16x32_bf16 v[160:163], v[244:247], v[132:135], v[160:163]\n"
" s_waitcnt vmcnt(5)\n"
" v_mfma_f32_16x16x32_bf16 v[160:163], v[244:247], v[136:139], v[160:163]\n"
" s_waitcnt vmcnt(4)\n"
" v_mfma_f32_16x16x32_bf16 v[160:163], v[244:247], v[140:143], v[160:163]\n"
" v_add_f32 v164, v164, v165\n"
" v_add_f32 v166, v166, v167\n"
" v_add_f32 v164, v164, v166\n"
" s_add_i32 s68, s68, 1\n"
" ds_write_b32 v240, v164\n"
" s_nop 2\n"
" ds_write_b128 v239, v[160:163]\n"
" v_cmp_u_f32 vcc, v160, v164\n"
" s_nop 0\n"
" s_cbranch_vccnz .Lch_slow1_%=\n"
".Lch_h1ok_%=:\n"
" s_cmp_lg_u32 s69, 0\n"
" s_waitcnt lgkmcnt(0)\n"
" s_barrier\n"
" s_cbranch_scc1 .Lch_nopub_%=\n"
" ds_read_b128 v[148:151], v225 offset:35840\n"
" ds_read_b128 v[152:155], v225 offset:36480\n"
" ds_read_b128 v[156:159], v225 offset:37120\n"
" ds_read_b128 v[160:163], v225 offset:37760\n"
" s_cmp_le_u32 s68, 8\n"
" s_waitcnt lgkmcnt(2)\n"
" v_pk_add_f32 v[148:149], v[148:149], v[152:153]\n"
" v_pk_add_f32 v[150:151], v[150:151], v[154:155]\n"
" s_waitcnt lgkmcnt(1)\n"
" v_pk_add_f32 v[148:149], v[148:149], v[156:157]\n"
" v_pk_add_f32 v[150:151], v[150:151], v[158:159]\n"
" s_waitcnt lgkmcnt(0)\n"
" v_pk_add_f32 v[148:149], v[148:149], v[160:161]\n"
" v_pk_add_f32 v[150:151], v[150:151], v[162:163]\n"
" v_cvt_pk_bf16_f32 v148, v148, v149\n"
" v_cvt_pk_bf16_f32 v149, v150, v151\n"
" s_cbranch_scc1 .Lch_ovr_%=\n"
".Lch_pub_%=:\n"
" buffer_store_dwordx2 v[148:149], v249, s[36:39], s84 offen sc1\n"
".Lch_nopub_%=:\n"
" s_branch .Lch_loop_%=\n"
".Lch_ovr_%=:\n"
" v_cndmask_b32 v148, v148, v242, s[88:89]\n"
" v_cndmask_b32 v149, v149, v243, s[88:89]\n"
" s_branch .Lch_pub_%=\n"
".Lch_tslate_%=:\n"
" v_lshlrev_b32 v221, 1, v225\n"
" buffer_load_dword v221, v221, s[36:39], s95 offen sc1\n"
" s_waitcnt vmcnt(0)\n"
" s_mov_b32 s91, 0\n"
".Lch_tsr_%=:\n"
" v_cmp_eq_u32 vcc, -1, v221\n"
" s_nop 0\n"
" s_cbranch_vccz .Lch_tsrok_%=\n"
" s_add_i32 s91, s91, 1\n"
" s_or_b32 s91, s91, s90\n"
" s_cmp_ge_u32 s91, 65536\n"
" s_cselect_b32 s90, 65536, 0\n"
" s_cbranch_scc1 .Lch_tsrok_%=\n"
" s_sleep 2\n"
" v_lshlrev_b32 v221, 1, v225\n"
" buffer_load_dword v221, v221, s[36:39], s95 offen sc1\n"
" s_waitcnt vmcnt(0)\n"
" s_branch .Lch_tsr_%=\n"
".Lch_tsrok_%=:\n"
" v_subrev_u32 v221, s72, v221\n"
" s_nop 1\n"
" v_max_i32_dpp v221, v221, v221 quad_perm:[1,0,3,2] row_mask:0xf bank_mask:0xf\n"
" s_nop 1\n"
" v_max_i32_dpp v221, v221, v221 quad_perm:[2,3,0,1] row_mask:0xf bank_mask:0xf\n"
" s_nop 1\n"
" v_max_i32_dpp v221, v221, v221 row_half_mirror row_mask:0xf bank_mask:0xf\n"
" s_nop 1\n"
" v_max_i32_dpp v221, v221, v221 row_mirror row_mask:0xf bank_mask:0xf\n"
" s_nop 0\n"
" v_readlane_b32 s74, v221, 0\n"
" v_readlane_b32 s75, v221, 16\n"
" v_readlane_b32 s76, v221, 32\n"
" v_readlane_b32 s78, v221, 48\n"
" s_max_i32 s74, s74, s75\n"
" s_max_i32 s76, s76, s78\n"
" s_max_i32 s74, s74, s76\n"
" s_max_i32 s74, s74, 0\n"
" s_min_i32 s74, s74, 0x4000\n"
" s_add_i32 s79, s79, s74\n"
" s_mov_b32 s91, 0\n"
" s_memrealtime s[74:75]\n"
" s_waitcnt lgkmcnt(0)\n"
" s_branch .Lch_ts_%=\n"
".Lch_late_%=:\n"
" s_add_i32 s9, s28, 8\n"
" s_mov_b32 s91, 0\n"
".Lch_fl_%=:\n"
" buffer_load_dword v220, v241, s[48:51], 0 offen sc1\n"
" s_waitcnt vmcnt(0)\n"
" v_readlane_b32 s74, v220, 0\n"
" v_readlane_b32 s75, v220, 1\n"
" s_sub_i32 s74, s74, s86\n"
" s_sub_i32 s75, s75, s87\n"
" s_or_b32 s74, s74, s75\n"
" s_cmp_ge_i32 s74, 0\n"
" s_cbranch_scc1 .Lch_flok_%=\n"
" s_add_i32 s91, s91, 1\n"
" s_or_b32 s91, s91, s90\n"
" s_cmp_ge_u32 s91, 65536\n"
" s_cselect_b32 s90, 65536, 0\n"
" s_cbranch_scc1 .Lch_flok_%=\n"
" s_sleep 2\n"
" s_branch .Lch_fl_%=\n"
".Lch_flok_%=:\n"
" s_branch .Lch_kvok_%=\n"
".Lch_remax_%=:\n"
" s_nop 1\n"
" v_max_f32 v204, 0, v204\n"
" v_mov_b32 v205, v204\n"
" s_nop 1\n"
" v_permlane16_swap_b32 v204, v205\n"
" v_max_f32 v204, v204, v205\n"
" v_max_f32 v205, v204, v205\n"
" s_nop 1\n"
" v_permlane32_swap_b32 v204, v205\n"
" v_max_f32 v204, v204, v205\n"
" v_sub_f32 v200, v196, v204\n"
" v_exp_f32 v200, v200\n"
" v_sub_f32 v201, v197, v204\n"
" v_exp_f32 v201, v201\n"
" v_sub_f32 v202, v198, v204\n"
" v_exp_f32 v202, v202\n"
" v_sub_f32 v203, v199, v204\n"
" v_exp_f32 v203, v203\n"
" v_sub_f32 v205, 0, v204\n"
" v_exp_f32 v205, v205\n"
" s_nop 0\n"
" v_mul_f32 v188, v188, v205\n"
" v_mul_f32 v180, v180, v205\n"
" v_mul_f32 v181, v181, v205\n"
" v_mul_f32 v182, v182, v205\n"
" v_mul_f32 v183, v183, v205\n"
" s_nop 1\n"
" s_branch .Lch_maxok_%=\n"
".Lch_slow2_%=:\n"
" s_cmp_ge_u32 s35, 16\n"
" s_cbranch_scc1 .Lch_qok_%=\n"
" s_add_i32 s9, s28, 8\n"
" s_add_i32 s35, s35, 1\n"
" s_cmp_ge_u32 s35, 3\n"
" s_cbranch_scc1 .Lch_exact_%=\n"
" s_waitcnt lgkmcnt(0)\n"
" s_barrier\n"
" s_mov_b32 m0, s4\n"
" s_add_i32 s76, s94, s10\n"
" buffer_load_dwordx4 v225, s[36:39], s76 offen sc1 lds\n"
" s_mov_b32 m0, s5\n"
" s_add_i32 s76, s94, s11\n"
" buffer_load_dwordx4 v225, s[36:39], s76 offen sc1 lds\n"
" s_mov_b32 m0, s6\n"
" s_add_i32 s76, s94, s12\n"
" buffer_load_dwordx4 v225, s[36:39], s76 offen sc1 lds\n"
" s_mov_b32 m0, s7\n"
" s_add_i32 s76, s94, s13\n"
" buffer_load_dwordx4 v225, s[36:39], s76 offen sc1 lds\n"
" s_mov_b32 m0, s8\n"
" s_add_i32 s76, s94, s14\n"
" buffer_load_dwordx4 v225, s[36:39], s76 offen sc1 lds\n"
" s_branch .Lch_again_%=\n"
".Lch_exact_%=:\n"
" s_mov_b32 s91, 0\n"
".Lch_slow2b_%=:\n"
" s_waitcnt lgkmcnt(0)\n"
" s_barrier\n"
" s_mov_b32 m0, s4\n"
" s_add_i32 s76, s94, s10\n"
" buffer_load_dwordx4 v225, s[36:39], s76 offen sc1 lds\n"
" s_mov_b32 m0, s5\n"
" s_add_i32 s76, s94, s11\n"
" buffer_load_dwordx4 v225, s[36:39], s76 offen sc1 lds\n"
" s_mov_b32 m0, s6\n"
" s_add_i32 s76, s94, s12\n"
" buffer_load_dwordx4 v225, s[36:39], s76 offen sc1 lds\n"
" s_mov_b32 m0, s7\n"
" s_add_i32 s76, s94, s13\n"
" buffer_load_dwordx4 v225, s[36:39], s76 offen sc1 lds\n"
" s_mov_b32 m0, s8\n"
" s_add_i32 s76, s94, s14\n"
" buffer_load_dwordx4 v225, s[36:39], s76 offen sc1 lds\n"
" s_waitcnt vmcnt(0)\n"
" s_barrier\n"
" ds_read_b128 v[128:131], v224 offset:0\n"
" ds_read_b128 v[132:135], v224 offset:4096\n"
" ds_read_b128 v[136:139], v224 offset:8192\n"
" ds_read_b128 v[140:143], v224 offset:12288\n"
" ds_read_b128 v[144:147], v224 offset:16384\n"
" v_mov_b32 v160, 0\n"
" s_waitcnt lgkmcnt(4)\n"
" v_max3_u32 v160, v160, v128, v129\n"
" v_max3_u32 v160, v160, v130, v131\n"
" s_waitcnt lgkmcnt(3)\n"
" v_max3_u32 v160, v160, v132, v133\n"
" v_max3_u32 v160, v160, v134, v135\n"
" s_waitcnt lgkmcnt(2)\n"
" v_max3_u32 v160, v160, v136, v137\n"
" v_max3_u32 v160, v160, v138, v139\n"
" s_waitcnt lgkmcnt(1)\n"
" v_max3_u32 v160, v160, v140, v141\n"
" v_max3_u32 v160, v160, v142, v143\n"
" s_waitcnt lgkmcnt(0)\n"
" v_max3_u32 v160, v160, v144, v145\n"
" v_max3_u32 v160, v160, v146, v147\n"
" ds_write_b32 v224, v160 offset:28672\n"
" s_waitcnt lgkmcnt(0)\n"
" s_barrier\n"
" ds_read_b32 v160, v225 offset:28672\n"
" ds_read_b32 v161, v225 offset:29696\n"
" ds_read_b32 v162, v225 offset:30720\n"
" ds_read_b32 v163, v225 offset:31744\n"
" s_waitcnt lgkmcnt(1)\n"
" v_max3_u32 v160, v160, v161, v162\n"
" s_waitcnt lgkmcnt(0)\n"
" v_max_u32 v160, v160, v163\n"
" v_cmp_eq_u32 vcc, -1, v160\n"
" s_mov_b32 s35, 16\n"
" s_cbranch_vccz .Lch_q_%=\n"
" s_add_i32 s91, s91, 1\n"
" s_or_b32 s91, s91, s90\n"
" s_cmp_ge_u32 s91, 65536\n"
" s_cselect_b32 s90, 65536, 0\n"
" s_cbranch_scc1 .Lch_q_%=\n"
" s_branch .Lch_slow2b_%=\n"
".Lch_slow1_%=:\n"
" v_mov_b32 v168, 0\n"
" v_max3_u32 v168, v168, v128, v129\n"
" v_max3_u32 v168, v168, v130, v131\n"
" v_max3_u32 v168, v168, v132, v133\n"
" v_max3_u32 v168, v168, v134, v135\n"
" v_max3_u32 v168, v168, v136, v137\n"
" v_max3_u32 v168, v168, v138, v139\n"
" v_max3_u32 v168, v168, v140, v141\n"
" v_max3_u32 v168, v168, v142, v143\n"
" v_max3_u32 v168, v168, v144, v145\n"
" v_max3_u32 v168, v168, v146, v147\n"
" v_cmp_eq_u32 vcc, -1, v168\n"
" s_nop 0\n"
" s_cbranch_vccz .Lch_h1ok_%=\n"
" s_add_i32 s91, s91, 1\n"
" s_or_b32 s91, s91, s90\n"
" s_cmp_ge_u32 s91, 65536\n"
" s_cselect_b32 s90, 65536, 0\n"
" s_cbranch_scc1 .Lch_h1ok_%=\n"
" s_waitcnt lgkmcnt(0)\n"
" s_add_i32 s68, s68, -1\n"
" buffer_load_dwordx4 v[144:147], v236, s[40:43], s92 offen sc1\n"
" buffer_load_dwordx4 v[128:131], v235, s[40:43], s92 offen offset:0 sc1\n"
" buffer_load_dwordx4 v[132:135], v235, s[40:43], s92 offen offset:1280 sc1\n"
" buffer_load_dwordx4 v[136:139], v235, s[40:43], s92 offen offset:2560 sc1\n"
" buffer_load_dwordx4 v[140:143], v235, s[40:43], s92 offen offset:3840 sc1\n"
" s_waitcnt vmcnt(0)\n"
" s_branch .Lch_h1_%=\n"
".Lch_end_%=:\n"
" v_min_u32 v208, -2, v208\n"
" v_min_u32 v209, -2, v209\n"
" v_min_u32 v210, -2, v210\n"
" v_min_u32 v211, -2, v211\n"
" buffer_store_dwordx4 v[208:211], v222, s[44:47], 0 offen sc1\n"
" buffer_store_dwordx2 v[204:205], v223, s[44:47], 0 offen sc1\n"
" buffer_store_dword v220, v251, s[48:51], 0 offen sc1\n"
" s_waitcnt lgkmcnt(0)\n"
" s_waitcnt vmcnt(0)\n"
      : "+{v[224:239]}"(c0), "+{v[240:255]}"(c1), "+{v[208:223]}"(c2), "+{v[180:195]}"(mi), "+{v[196:199]}"(mj), "+{s[52:55]}"(sQ), "+{s[56:59]}"(sK), "+{s[60:63]}"(sV), "+{s[84:87]}"(sO), "+{s70}"(sci), "+{s[48:51]}"(sPG), "+{s[44:47]}"(sKM), "+{s[64:65]}"(kxb)
      : "{s[36:39]}"(sMI), "{s[40:43]}"(sPR),

"{s[16:17]}"(p.tag), "{s69}"(w), "{s32}"(rot), "{s33}"(slot), "{s73}"(h * 4), "{s66}"(16384 + 4096 * half), "{s[80:81]}"(mlo), "{s[82:83]}"(mgh), "{s[88:89]}"(mred), "{s[96:97]}"(dbg)
      : "memory", "vcc", "scc", "m0", R0_179("v"), "v200", "v201", "v202", "v203", "v204", "v205", "v206", "v207", A_ALL,
        "s4", "s5", "s6", "s7", "s8", "s9", "s10", "s11", "s12", "s13", "s14", "s20", "s21", "s22", "s23", "s24", "s25", "s26", "s27", "s28", "s29", "s30", "s31", "s34", "s35", "s68", "s71", "s72", "s74", "s75", "s76", "s77", "s78", "s79",
        "s90", "s91", "s92", "s93", "s94", "s95", "s98", "s99");
  __builtin_amdgcn_endpgm();
}

#define V_LOW R0_199("v"), R10("v", 20), R10("v", 21), "v220", "v221", "v222", "v223"
#define S_BULK "s86", "s87", "s20", "s21", "s22", "s23", "s24", "s25", "s26", "s27", "s30", "s31", "s74", "s75", "s76", "s77", "s78", "s79", "s90", "s91", "s92", "s93", "s94", "s95", "s98", "s99"
constexpr int LIMG = 65536;
__device__ __forceinline__ unsigned long long dbgp(const Params& p, int w) { return uni64((unsigned long long)p.dbg + (1 << 20) + (p.role * 4 + w) * 2048); }
__device__ __forceinline__ void head_role(const Params& p, int w, int id, int xc) {
  const int lane = lane_id(), tid = w * 64 + lane;
  const int h = id >> 2, l = (id >> 1) & 1, g = lane >> 4, r = lane & 15;
  u8* ws = p.ws;
  const int ninf = (int)0xFF800000;
  i32x16 c0, c1;
  c0[0] = tid * 16; c0[1] = lane * 16; c0[2] = (r >> 2) * CELL + CO + g * 64 + (r & 3) * 16;
  c0[3] = pg(l ? ((lane & 8) ? PG_FIN + xc * 8 + (lane & 7) : PG_XN + xc * 16 + (lane & 7) * 2 + (id & 1)) : PG_LN0 + (lane & 7)); c0[4] = tid == 0 ? pg(PG_HEAD + id) : OOB - 16384; c0[5] = tid < 4 ? pg(PG_O + tid * 128 + id) : OOB;
  c0[6] = w * 512 + (g * 8 + (r & 7)) * 16; c0[7] = 2048 + w * 512 + ((g & 1) * 16 + r) * 8;
  c0[8] = LIMG + w * 20480 + lane * 16; c0[9] = pg(PG_HINT + (2 * h + (w >> 1)) * 2 + l); c0[10] = g < 2 ? 0 : ninf; c0[11] = pol_word(lane);
#pragma unroll
  for (int i = 0; i < 4; ++i) c0[12 + i] = (4 * g + i <= r) ? 0 : ninf;
#pragma unroll
  for (int i = 0; i < 16; ++i) c1[i] = 0;
  c1[4] = (16 * w + g) * 8192 + ((r ^ (g << 1)) << 4); c1[5] = (16 * w + 4 + g) * 8192 + ((r ^ ((4 + g) << 1)) << 4);
  c1[6] = trbase<256>(2 * w, lane); c1[7] = trbase<256>(2 * w + 1, lane);
  const v4u sI = rawrsrc(ws, WSB);
  v4u sO = rawrsrc(ws + OFF_KX + (size_t)(h * 2 + l) * RK * 32768, RK * 32768 - 8192);
  v4u sA = rawrsrc(ws + OFF_KM + id * CELL, 8192);
  v4u sP = rawrsrc(ws + OFF_PROG, PGB);
  const v4u s0 = rawrsrc(p.Wk + h * 256, 640 * 8192 - h * 256), s1 = rawrsrc(p.Wv + h * 256, 640 * 8192 - h * 256), s2 = rawrsrc(p.Wq + h * 256, 640 * 8192 - h * 256);
  int seg = __builtin_amdgcn_readfirstlane(id & 1);
  asm volatile(
" s_nop 4\n"
" s_mov_b32 s90, 0\n"
" s_lshl_b32 s77, s69, 12\n"
" s_add_i32 m0, s77, 0\n"
" s_mov_b32 s76, 0\n"
" buffer_load_dwordx4 v244, s[52:55], s76 offen lds\n"
" s_add_i32 m0, s77, 1024\n"
" s_mov_b32 s76, 0\n"
" buffer_load_dwordx4 v245, s[52:55], s76 offen lds\n"
" s_add_i32 m0, s77, 2048\n"
" s_mov_b32 s76, 65536\n"
" buffer_load_dwordx4 v244, s[52:55], s76 offen lds\n"
" s_add_i32 m0, s77, 3072\n"
" s_mov_b32 s76, 65536\n"
" buffer_load_dwordx4 v245, s[52:55], s76 offen lds\n"
" s_add_i32 m0, s77, 16384\n"
" s_mov_b32 s76, 524288\n"
" buffer_load_dwordx4 v244, s[52:55], s76 offen lds\n"
" s_add_i32 m0, s77, 17408\n"
" s_mov_b32 s76, 524288\n"
" buffer_load_dwordx4 v245, s[52:55], s76 offen lds\n"
" s_add_i32 m0, s77, 18432\n"
" s_mov_b32 s76, 589824\n"
" buffer_load_dwordx4 v244, s[52:55], s76 offen lds\n"
" s_add_i32 m0, s77, 19456\n"
" s_mov_b32 s76, 589824\n"
" buffer_load_dwordx4 v245, s[52:55], s76 offen lds\n"
" s_add_i32 m0, s77, 32768\n"
" s_mov_b32 s76, 1048576\n"
" buffer_load_dwordx4 v244, s[52:55], s76 offen lds\n"
" s_add_i32 m0, s77, 33792\n"
" s_mov_b32 s76, 1048576\n"
" buffer_load_dwordx4 v245, s[52:55], s76 offen lds\n"
" s_add_i32 m0, s77, 34816\n"
" s_mov_b32 s76, 1114112\n"
" buffer_load_dwordx4 v244, s[52:55], s76 offen lds\n"
" s_add_i32 m0, s77, 35840\n"
" s_mov_b32 s76, 1114112\n"
" buffer_load_dwordx4 v245, s[52:55], s76 offen lds\n"
" s_add_i32 m0, s77, 49152\n"
" s_mov_b32 s76, 1572864\n"
" buffer_load_dwordx4 v244, s[52:55], s76 offen lds\n"
" s_add_i32 m0, s77, 50176\n"
" s_mov_b32 s76, 1572864\n"
" buffer_load_dwordx4 v245, s[52:55], s76 offen lds\n"
" s_add_i32 m0, s77, 51200\n"
" s_mov_b32 s76, 1638400\n"
" buffer_load_dwordx4 v244, s[52:55], s76 offen lds\n"
" s_add_i32 m0, s77, 52224\n"
" s_mov_b32 s76, 1638400\n"
" buffer_load_dwordx4 v245, s[52:55], s76 offen lds\n"
" s_add_i32 m0, s77, 147456\n"
" s_mov_b32 s76, 2097152\n"
" buffer_load_dwordx4 v244, s[52:55], s76 offen lds\n"
" s_add_i32 m0, s77, 148480\n"
" s_mov_b32 s76, 2097152\n"
" buffer_load_dwordx4 v245, s[52:55], s76 offen lds\n"
" s_add_i32 m0, s77, 149504\n"
" s_mov_b32 s76, 2162688\n"
" buffer_load_dwordx4 v244, s[52:55], s76 offen lds\n"
" s_add_i32 m0, s77, 150528\n"
" s_mov_b32 s76, 2162688\n"
" buffer_load_dwordx4 v245, s[52:55], s76 offen lds\n"
" s_add_i32 m0, s77, 65536\n"
" s_mov_b32 s76, 2621440\n"
" buffer_load_dwordx4 v244, s[52:55], s76 offen lds\n"
" s_add_i32 m0, s77, 66560\n"
" s_mov_b32 s76, 2621440\n"
" buffer_load_dwordx4 v245, s[52:55], s76 offen lds\n"
" s_add_i32 m0, s77, 67584\n"
" s_mov_b32 s76, 2686976\n"
" buffer_load_dwordx4 v244, s[52:55], s76 offen lds\n"
" s_add_i32 m0, s77, 68608\n"
" s_mov_b32 s76, 2686976\n"
" buffer_load_dwordx4 v245, s[52:55], s76 offen lds\n"
" s_add_i32 m0, s77, 81920\n"
" s_mov_b32 s76, 3145728\n"
" buffer_load_dwordx4 v244, s[52:55], s76 offen lds\n"
" s_add_i32 m0, s77, 82944\n"
" s_mov_b32 s76, 3145728\n"
" buffer_load_dwordx4 v245, s[52:55], s76 offen lds\n"
" s_add_i32 m0, s77, 83968\n"
" s_mov_b32 s76, 3211264\n"
" buffer_load_dwordx4 v244, s[52:55], s76 offen lds\n"
" s_add_i32 m0, s77, 84992\n"
" s_mov_b32 s76, 3211264\n"
" buffer_load_dwordx4 v245, s[52:55], s76 offen lds\n"
" s_add_i32 m0, s77, 98304\n"
" s_mov_b32 s76, 3670016\n"
" buffer_load_dwordx4 v244, s[52:55], s76 offen lds\n"
" s_add_i32 m0, s77, 99328\n"
" s_mov_b32 s76, 3670016\n"
" buffer_load_dwordx4 v245, s[52:55], s76 offen lds\n"
" s_add_i32 m0, s77, 100352\n"
" s_mov_b32 s76, 3735552\n"
" buffer_load_dwordx4 v244, s[52:55], s76 offen lds\n"
" s_add_i32 m0, s77, 101376\n"
" s_mov_b32 s76, 3735552\n"
" buffer_load_dwordx4 v245, s[52:55], s76 offen lds\n"
" s_add_i32 m0, s77, 114688\n"
" s_mov_b32 s76, 4194304\n"
" buffer_load_dwordx4 v244, s[52:55], s76 offen lds\n"
" s_add_i32 m0, s77, 115712\n"
" s_mov_b32 s76, 4194304\n"
" buffer_load_dwordx4 v245, s[52:55], s76 offen lds\n"
" s_add_i32 m0, s77, 116736\n"
" s_mov_b32 s76, 4259840\n"
" buffer_load_dwordx4 v244, s[52:55], s76 offen lds\n"
" s_add_i32 m0, s77, 117760\n"
" s_mov_b32 s76, 4259840\n"
" buffer_load_dwordx4 v245, s[52:55], s76 offen lds\n"
" s_add_i32 m0, s77, 131072\n"
" s_mov_b32 s76, 4718592\n"
" buffer_load_dwordx4 v244, s[52:55], s76 offen lds\n"
" s_add_i32 m0, s77, 132096\n"
" s_mov_b32 s76, 4718592\n"
" buffer_load_dwordx4 v245, s[52:55], s76 offen lds\n"
" s_add_i32 m0, s77, 133120\n"
" s_mov_b32 s76, 4784128\n"
" buffer_load_dwordx4 v244, s[52:55], s76 offen lds\n"
" s_add_i32 m0, s77, 134144\n"
" s_mov_b32 s76, 4784128\n"
" buffer_load_dwordx4 v245, s[52:55], s76 offen lds\n"
" s_waitcnt lgkmcnt(0)\n"
" s_waitcnt vmcnt(36)\n"
" s_barrier\n"
" ds_read_b64_tr_b16 a[0:1], v246 offset:0\n"
" ds_read_b64_tr_b16 a[2:3], v246 offset:4096\n"
" ds_read_b64_tr_b16 a[4:5], v247 offset:0\n"
" ds_read_b64_tr_b16 a[6:7], v247 offset:4096\n"
" ds_read_b64_tr_b16 a[8:9], v246 offset:8192\n"
" ds_read_b64_tr_b16 a[10:11], v246 offset:12288\n"
" ds_read_b64_tr_b16 a[12:13], v247 offset:8192\n"
" ds_read_b64_tr_b16 a[14:15], v247 offset:12288\n"
" s_waitcnt lgkmcnt(0)\n"
" s_waitcnt vmcnt(32)\n"
" s_barrier\n"
" s_add_i32 m0, s77, 0\n"
" s_mov_b32 s76, 0\n"
" buffer_load_dwordx4 v244, s[56:59], s76 offen lds\n"
" s_add_i32 m0, s77, 1024\n"
" s_mov_b32 s76, 0\n"
" buffer_load_dwordx4 v245, s[56:59], s76 offen lds\n"
" s_add_i32 m0, s77, 2048\n"
" s_mov_b32 s76, 65536\n"
" buffer_load_dwordx4 v244, s[56:59], s76 offen lds\n"
" s_add_i32 m0, s77, 3072\n"
" s_mov_b32 s76, 65536\n"
" buffer_load_dwordx4 v245, s[56:59], s76 offen lds\n"
" ds_read_b64_tr_b16 a[16:17], v246 offset:16384\n"
" ds_read_b64_tr_b16 a[18:19], v246 offset:20480\n"
" ds_read_b64_tr_b16 a[20:21], v247 offset:16384\n"
" ds_read_b64_tr_b16 a[22:23], v247 offset:20480\n"
" ds_read_b64_tr_b16 a[24:25], v246 offset:24576\n"
" ds_read_b64_tr_b16 a[26:27], v246 offset:28672\n"
" ds_read_b64_tr_b16 a[28:29], v247 offset:24576\n"
" ds_read_b64_tr_b16 a[30:31], v247 offset:28672\n"
" s_waitcnt lgkmcnt(0)\n"
" s_waitcnt vmcnt(32)\n"
" s_barrier\n"
" s_add_i32 m0, s77, 16384\n"
" s_mov_b32 s76, 524288\n"
" buffer_load_dwordx4 v244, s[56:59], s76 offen lds\n"
" s_add_i32 m0, s77, 17408\n"
" s_mov_b32 s76, 524288\n"
" buffer_load_dwordx4 v245, s[56:59], s76 offen lds\n"
" s_add_i32 m0, s77, 18432\n"
" s_mov_b32 s76, 589824\n"
" buffer_load_dwordx4 v244, s[56:59], s76 offen lds\n"
" s_add_i32 m0, s77, 19456\n"
" s_mov_b32 s76, 589824\n"
" buffer_load_dwordx4 v245, s[56:59], s76 offen lds\n"
" ds_read_b64_tr_b16 a[32:33], v246 offset:32768\n"
" ds_read_b64_tr_b16 a[34:35], v246 offset:36864\n"
" ds_read_b64_tr_b16 a[36:37], v247 offset:32768\n"
" ds_read_b64_tr_b16 a[38:39], v247 offset:36864\n"
" ds_read_b64_tr_b16 a[40:41], v246 offset:40960\n"
" ds_read_b64_tr_b16 a[42:43], v246 offset:45056\n"
" ds_read_b64_tr_b16 a[44:45], v247 offset:40960\n"
" ds_read_b64_tr_b16 a[46:47], v247 offset:45056\n"
" s_waitcnt lgkmcnt(0)\n"
" s_waitcnt vmcnt(32)\n"
" s_barrier\n"
" s_add_i32 m0, s77, 32768\n"
" s_mov_b32 s76, 1048576\n"
" buffer_load_dwordx4 v244, s[56:59], s76 offen lds\n"
" s_add_i32 m0, s77, 33792\n"
" s_mov_b32 s76, 1048576\n"
" buffer_load_dwordx4 v245, s[56:59], s76 offen lds\n"
" s_add_i32 m0, s77, 34816\n"
" s_mov_b32 s76, 1114112\n"
" buffer_load_dwordx4 v244, s[56:59], s76 offen lds\n"
" s_add_i32 m0, s77, 35840\n"
" s_mov_b32 s76, 1114112\n"
" buffer_load_dwordx4 v245, s[56:59], s76 offen lds\n"
" v_add_u32 v216, 49152, v246\n"
" v_add_u32 v217, 49152, v247\n"
" ds_read_b64_tr_b16 a[48:49], v216 offset:0\n"
" ds_read_b64_tr_b16 a[50:51], v216 offset:4096\n"
" ds_read_b64_tr_b16 a[52:53], v217 offset:0\n"
" ds_read_b64_tr_b16 a[54:55], v217 offset:4096\n"
" ds_read_b64_tr_b16 a[56:57], v216 offset:8192\n"
" ds_read_b64_tr_b16 a[58:59], v216 offset:12288\n"
" ds_read_b64_tr_b16 a[60:61], v217 offset:8192\n"
" ds_read_b64_tr_b16 a[62:63], v217 offset:12288\n"
" s_waitcnt lgkmcnt(0)\n"
" s_waitcnt vmcnt(32)\n"
" s_barrier\n"
" s_add_i32 m0, s77, 49152\n"
" s_mov_b32 s76, 1572864\n"
" buffer_load_dwordx4 v244, s[56:59], s76 offen lds\n"
" s_add_i32 m0, s77, 50176\n"
" s_mov_b32 s76, 1572864\n"
" buffer_load_dwordx4 v245, s[56:59], s76 offen lds\n"
" s_add_i32 m0, s77, 51200\n"
" s_mov_b32 s76, 1638400\n"
" buffer_load_dwordx4 v244, s[56:59], s76 offen lds\n"
" s_add_i32 m0, s77, 52224\n"
" s_mov_b32 s76, 1638400\n"
" buffer_load_dwordx4 v245, s[56:59], s76 offen lds\n"
" v_add_u32 v216, 147456, v246\n"
" v_add_u32 v217, 147456, v247\n"
" ds_read_b64_tr_b16 a[64:65], v216 offset:0\n"
" ds_read_b64_tr_b16 a[66:67], v216 offset:4096\n"
" ds_read_b64_tr_b16 a[68:69], v217 offset:0\n"
" ds_read_b64_tr_b16 a[70:71], v217 offset:4096\n"
" ds_read_b64_tr_b16 a[72:73], v216 offset:8192\n"
" ds_read_b64_tr_b16 a[74:75], v216 offset:12288\n"
" ds_read_b64_tr_b16 a[76:77], v217 offset:8192\n"
" ds_read_b64_tr_b16 a[78:79], v217 offset:12288\n"
" s_waitcnt lgkmcnt(0)\n"
" s_waitcnt vmcnt(32)\n"
" s_barrier\n"
" s_add_i32 m0, s77, 147456\n"
" s_mov_b32 s76, 2097152\n"
" buffer_load_dwordx4 v244, s[56:59], s76 offen lds\n"
" s_add_i32 m0, s77, 148480\n"
" s_mov_b32 s76, 2097152\n"
" buffer_load_dwordx4 v245, s[56:59], s76 offen lds\n"
" s_add_i32 m0, s77, 149504\n"
" s_mov_b32 s76, 2162688\n"
" buffer_load_dwordx4 v244, s[56:59], s76 offen lds\n"
" s_add_i32 m0, s77, 150528\n"
" s_mov_b32 s76, 2162688\n"
" buffer_load_dwordx4 v245, s[56:59], s76 offen lds\n"
" v_add_u32 v216, 65536, v246\n"
" v_add_u32 v217, 65536, v247\n"
" ds_read_b64_tr_b16 a[80:81], v216 offset:0\n"
" ds_read_b64_tr_b16 a[82:83], v216 offset:4096\n"
" ds_read_b64_tr_b16 a[84:85], v217 offset:0\n"
" ds_read_b64_tr_b16 a[86:87], v217 offset:4096\n"
" ds_read_b64_tr_b16 a[88:89], v216 offset:8192\n"
" ds_read_b64_tr_b16 a[90:91], v216 offset:12288\n"
" ds_read_b64_tr_b16 a[92:93], v217 offset:8192\n"
" ds_read_b64_tr_b16 a[94:95], v217 offset:12288\n"
" s_waitcnt lgkmcnt(0)\n"
" s_waitcnt vmcnt(32)\n"
" s_barrier\n"
" s_add_i32 m0, s77, 65536\n"
" s_mov_b32 s76, 2621440\n"
" buffer_load_dwordx4 v244, s[56:59], s76 offen lds\n"
" s_add_i32 m0, s77, 66560\n"
" s_mov_b32 s76, 2621440\n"
" buffer_load_dwordx4 v245, s[56:59], s76 offen lds\n"
" s_add_i32 m0, s77, 67584\n"
" s_mov_b32 s76, 2686976\n"
" buffer_load_dwordx4 v244, s[56:59], s76 offen lds\n"
" s_add_i32 m0, s77, 68608\n"
" s_mov_b32 s76, 2686976\n"
" buffer_load_dwordx4 v245, s[56:59], s76 offen lds\n"
" v_add_u32 v216, 81920, v246\n"
" v_add_u32 v217, 81920, v247\n"
" ds_read_b64_tr_b16 a[96:97], v216 offset:0\n"
" ds_read_b64_tr_b16 a[98:99], v216 offset:4096\n"
" ds_read_b64_tr_b16 a[100:101], v217 offset:0\n"
" ds_read_b64_tr_b16 a[102:103], v217 offset:4096\n"
" ds_read_b64_tr_b16 a[104:105], v216 offset:8192\n"
" ds_read_b64_tr_b16 a[106:107], v216 offset:12288\n"
" ds_read_b64_tr_b16 a[108:109], v217 offset:8192\n"
" ds_read_b64_tr_b16 a[110:111], v217 offset:12288\n"
" s_waitcnt lgkmcnt(0)\n"
" s_waitcnt vmcnt(32)\n"
" s_barrier\n"
" s_add_i32 m0, s77, 81920\n"
" s_mov_b32 s76, 3145728\n"
" buffer_load_dwordx4 v244, s[56:59], s76 offen lds\n"
" s_add_i32 m0, s77, 82944\n"
" s_mov_b32 s76, 3145728\n"
" buffer_load_dwordx4 v245, s[56:59], s76 offen lds\n"
" s_add_i32 m0, s77, 83968\n"
" s_mov_b32 s76, 3211264\n"
" buffer_load_dwordx4 v244, s[56:59], s76 offen lds\n"
" s_add_i32 m0, s77, 84992\n"
" s_mov_b32 s76, 3211264\n"
" buffer_load_dwordx4 v245, s[56:59], s76 offen lds\n"
" v_add_u32 v216, 98304, v246\n"
" v_add_u32 v217, 98304, v247\n"
" ds_read_b64_tr_b16 a[112:113], v216 offset:0\n"
" ds_read_b64_tr_b16 a[114:115], v216 offset:4096\n"
" ds_read_b64_tr_b16 a[116:117], v217 offset:0\n"
" ds_read_b64_tr_b16 a[118:119], v217 offset:4096\n"
" ds_read_b64_tr_b16 a[120:121], v216 offset:8192\n"
" ds_read_b64_tr_b16 a[122:123], v216 offset:12288\n"
" ds_read_b64_tr_b16 a[124:125], v217 offset:8192\n"
" ds_read_b64_tr_b16 a[126:127], v217 offset:12288\n"
" s_waitcnt lgkmcnt(0)\n"
" s_waitcnt vmcnt(32)\n"
" s_barrier\n"
" s_add_i32 m0, s77, 98304\n"
" s_mov_b32 s76, 3670016\n"
" buffer_load_dwordx4 v244, s[56:59], s76 offen lds\n"
" s_add_i32 m0, s77, 99328\n"
" s_mov_b32 s76, 3670016\n"
" buffer_load_dwordx4 v245, s[56:59], s76 offen lds\n"
" s_add_i32 m0, s77, 100352\n"
" s_mov_b32 s76, 3735552\n"
" buffer_load_dwordx4 v244, s[56:59], s76 offen lds\n"
" s_add_i32 m0, s77, 101376\n"
" s_mov_b32 s76, 3735552\n"
" buffer_load_dwordx4 v245, s[56:59], s76 offen lds\n"
" v_add_u32 v216, 114688, v246\n"
" v_add_u32 v217, 114688, v247\n"
" ds_read_b64_tr_b16 a[128:129], v216 offset:0\n"
" ds_read_b64_tr_b16 a[130:131], v216 offset:4096\n"
" ds_read_b64_tr_b16 a[132:133], v217 offset:0\n"
" ds_read_b64_tr_b16 a[134:135], v217 offset:4096\n"
" ds_read_b64_tr_b16 a[136:137], v216 offset:8192\n"
" ds_read_b64_tr_b16 a[138:139], v216 offset:12288\n"
" ds_read_b64_tr_b16 a[140:141], v217 offset:8192\n"
" ds_read_b64_tr_b16 a[142:143], v217 offset:12288\n"
" s_waitcnt lgkmcnt(0)\n"
" s_waitcnt vmcnt(32)\n"
" s_barrier\n"
" s_add_i32 m0, s77, 114688\n"
" s_mov_b32 s76, 4194304\n"
" buffer_load_dwordx4 v244, s[56:59], s76 offen lds\n"
" s_add_i32 m0, s77, 115712\n"
" s_mov_b32 s76, 4194304\n"
" buffer_load_dwordx4 v245, s[56:59], s76 offen lds\n"
" s_add_i32 m0, s77, 116736\n"
" s_mov_b32 s76, 4259840\n"
" buffer_load_dwordx4 v244, s[56:59], s76 offen lds\n"
" s_add_i32 m0, s77, 117760\n"
" s_mov_b32 s76, 4259840\n"
" buffer_load_dwordx4 v245, s[56:59], s76 offen lds\n"
" v_add_u32 v216, 131072, v246\n"
" v_add_u32 v217, 131072, v247\n"
" ds_read_b64_tr_b16 a[144:145], v216 offset:0\n"
" ds_read_b64_tr_b16 a[146:147], v216 offset:4096\n"
" ds_read_b64_tr_b16 a[148:149], v217 offset:0\n"
" ds_read_b64_tr_b16 a[150:151], v217 offset:4096\n"
" ds_read_b64_tr_b16 a[152:153], v216 offset:8192\n"
" ds_read_b64_tr_b16 a[154:155], v216 offset:12288\n"
" ds_read_b64_tr_b16 a[156:157], v217 offset:8192\n"
" ds_read_b64_tr_b16 a[158:159], v217 offset:12288\n"
" s_waitcnt lgkmcnt(0)\n"
" s_waitcnt vmcnt(32)\n"
" s_barrier\n"
" s_add_i32 m0, s77, 131072\n"
" s_mov_b32 s76, 4718592\n"
" buffer_load_dwordx4 v244, s[56:59], s76 offen lds\n"
" s_add_i32 m0, s77, 132096\n"
" s_mov_b32 s76, 4718592\n"
" buffer_load_dwordx4 v245, s[56:59], s76 offen lds\n"
" s_add_i32 m0, s77, 133120\n"
" s_mov_b32 s76, 4784128\n"
" buffer_load_dwordx4 v244, s[56:59], s76 offen lds\n"
" s_add_i32 m0, s77, 134144\n"
" s_mov_b32 s76, 4784128\n"
" buffer_load_dwordx4 v245, s[56:59], s76 offen lds\n"
" ds_read_b64_tr_b16 a[160:161], v246 offset:0\n"
" ds_read_b64_tr_b16 a[162:163], v246 offset:4096\n"
" ds_read_b64_tr_b16 a[164:165], v247 offset:0\n"
" ds_read_b64_tr_b16 a[166:167], v247 offset:4096\n"
" ds_read_b64_tr_b16 a[168:169], v246 offset:8192\n"
" ds_read_b64_tr_b16 a[170:171], v246 offset:12288\n"
" ds_read_b64_tr_b16 a[172:173], v247 offset:8192\n"
" ds_read_b64_tr_b16 a[174:175], v247 offset:12288\n"
" s_waitcnt lgkmcnt(0)\n"
" s_waitcnt vmcnt(32)\n"
" s_barrier\n"
" s_add_i32 m0, s77, 0\n"
" s_mov_b32 s76, 0\n"
" buffer_load_dwordx4 v244, s[60:63], s76 offen lds\n"
" s_add_i32 m0, s77, 1024\n"
" s_mov_b32 s76, 0\n"
" buffer_load_dwordx4 v245, s[60:63], s76 offen lds\n"
" s_add_i32 m0, s77, 2048\n"
" s_mov_b32 s76, 65536\n"
" buffer_load_dwordx4 v244, s[60:63], s76 offen lds\n"
" s_add_i32 m0, s77, 3072\n"
" s_mov_b32 s76, 65536\n"
" buffer_load_dwordx4 v245, s[60:63], s76 offen lds\n"
" ds_read_b64_tr_b16 a[176:177], v246 offset:16384\n"
" ds_read_b64_tr_b16 a[178:179], v246 offset:20480\n"
" ds_read_b64_tr_b16 a[180:181], v247 offset:16384\n"
" ds_read_b64_tr_b16 a[182:183], v247 offset:20480\n"
" ds_read_b64_tr_b16 a[184:185], v246 offset:24576\n"
" ds_read_b64_tr_b16 a[186:187], v246 offset:28672\n"
" ds_read_b64_tr_b16 a[188:189], v247 offset:24576\n"
" ds_read_b64_tr_b16 a[190:191], v247 offset:28672\n"
" s_waitcnt lgkmcnt(0)\n"
" s_waitcnt vmcnt(32)\n"
" s_barrier\n"
" s_add_i32 m0, s77, 16384\n"
" s_mov_b32 s76, 524288\n"
" buffer_load_dwordx4 v244, s[60:63], s76 offen lds\n"
" s_add_i32 m0, s77, 17408\n"
" s_mov_b32 s76, 524288\n"
" buffer_load_dwordx4 v245, s[60:63], s76 offen lds\n"
" s_add_i32 m0, s77, 18432\n"
" s_mov_b32 s76, 589824\n"
" buffer_load_dwordx4 v244, s[60:63], s76 offen lds\n"
" s_add_i32 m0, s77, 19456\n"
" s_mov_b32 s76, 589824\n"
" buffer_load_dwordx4 v245, s[60:63], s76 offen lds\n"
" ds_read_b64_tr_b16 a[192:193], v246 offset:32768\n"
" ds_read_b64_tr_b16 a[194:195], v246 offset:36864\n"
" ds_read_b64_tr_b16 a[196:197], v247 offset:32768\n"
" ds_read_b64_tr_b16 a[198:199], v247 offset:36864\n"
" ds_read_b64_tr_b16 a[200:201], v246 offset:40960\n"
" ds_read_b64_tr_b16 a[202:203], v246 offset:45056\n"
" ds_read_b64_tr_b16 a[204:205], v247 offset:40960\n"
" ds_read_b64_tr_b16 a[206:207], v247 offset:45056\n"
" s_waitcnt lgkmcnt(0)\n"
" s_waitcnt vmcnt(32)\n"
" s_barrier\n"
" s_add_i32 m0, s77, 32768\n"
" s_mov_b32 s76, 1048576\n"
" buffer_load_dwordx4 v244, s[60:63], s76 offen lds\n"
" s_add_i32 m0, s77, 33792\n"
" s_mov_b32 s76, 1048576\n"
" buffer_load_dwordx4 v245, s[60:63], s76 offen lds\n"
" s_add_i32 m0, s77, 34816\n"
" s_mov_b32 s76, 1114112\n"
" buffer_load_dwordx4 v244, s[60:63], s76 offen lds\n"
" s_add_i32 m0, s77, 35840\n"
" s_mov_b32 s76, 1114112\n"
" buffer_load_dwordx4 v245, s[60:63], s76 offen lds\n"
" v_add_u32 v216, 49152, v246\n"
" v_add_u32 v217, 49152, v247\n"
" ds_read_b64_tr_b16 a[208:209], v216 offset:0\n"
" ds_read_b64_tr_b16 a[210:211], v216 offset:4096\n"
" ds_read_b64_tr_b16 a[212:213], v217 offset:0\n"
" ds_read_b64_tr_b16 a[214:215], v217 offset:4096\n"
" ds_read_b64_tr_b16 a[216:217], v216 offset:8192\n"
" ds_read_b64_tr_b16 a[218:219], v216 offset:12288\n"
" ds_read_b64_tr_b16 a[220:221], v217 offset:8192\n"
" ds_read_b64_tr_b16 a[222:223], v217 offset:12288\n"
" s_waitcnt lgkmcnt(0)\n"
" s_waitcnt vmcnt(32)\n"
" s_barrier\n"
" s_add_i32 m0, s77, 49152\n"
" s_mov_b32 s76, 1572864\n"
" buffer_load_dwordx4 v244, s[60:63], s76 offen lds\n"
" s_add_i32 m0, s77, 50176\n"
" s_mov_b32 s76, 1572864\n"
" buffer_load_dwordx4 v245, s[60:63], s76 offen lds\n"
" s_add_i32 m0, s77, 51200\n"
" s_mov_b32 s76, 1638400\n"
" buffer_load_dwordx4 v244, s[60:63], s76 offen lds\n"
" s_add_i32 m0, s77, 52224\n"
" s_mov_b32 s76, 1638400\n"
" buffer_load_dwordx4 v245, s[60:63], s76 offen lds\n"
" v_add_u32 v216, 147456, v246\n"
" v_add_u32 v217, 147456, v247\n"
" ds_read_b64_tr_b16 a[224:225], v216 offset:0\n"
" ds_read_b64_tr_b16 a[226:227], v216 offset:4096\n"
" ds_read_b64_tr_b16 a[228:229], v217 offset:0\n"
" ds_read_b64_tr_b16 a[230:231], v217 offset:4096\n"
" ds_read_b64_tr_b16 a[232:233], v216 offset:8192\n"
" ds_read_b64_tr_b16 a[234:235], v216 offset:12288\n"
" ds_read_b64_tr_b16 a[236:237], v217 offset:8192\n"
" ds_read_b64_tr_b16 a[238:239], v217 offset:12288\n"
" s_waitcnt lgkmcnt(0)\n"
" s_waitcnt vmcnt(32)\n"
" s_barrier\n"
" s_add_i32 m0, s77, 147456\n"
" s_mov_b32 s76, 2097152\n"
" buffer_load_dwordx4 v244, s[60:63], s76 offen lds\n"
" s_add_i32 m0, s77, 148480\n"
" s_mov_b32 s76, 2097152\n"
" buffer_load_dwordx4 v245, s[60:63], s76 offen lds\n"
" s_add_i32 m0, s77, 149504\n"
" s_mov_b32 s76, 2162688\n"
" buffer_load_dwordx4 v244, s[60:63], s76 offen lds\n"
" s_add_i32 m0, s77, 150528\n"
" s_mov_b32 s76, 2162688\n"
" buffer_load_dwordx4 v245, s[60:63], s76 offen lds\n"
" v_add_u32 v216, 65536, v246\n"
" v_add_u32 v217, 65536, v247\n"
" ds_read_b64_tr_b16 a[240:241], v216 offset:0\n"
" ds_read_b64_tr_b16 a[242:243], v216 offset:4096\n"
" ds_read_b64_tr_b16 a[244:245], v217 offset:0\n"
" ds_read_b64_tr_b16 a[246:247], v217 offset:4096\n"
" ds_read_b64_tr_b16 a[248:249], v216 offset:8192\n"
" ds_read_b64_tr_b16 a[250:251], v216 offset:12288\n"
" ds_read_b64_tr_b16 a[252:253], v217 offset:8192\n"
" ds_read_b64_tr_b16 a[254:255], v217 offset:12288\n"
" s_waitcnt lgkmcnt(0)\n"
" s_waitcnt vmcnt(32)\n"
" s_barrier\n"
" v_add_u32 v216, 81920, v246\n"
" v_add_u32 v217, 81920, v247\n"
" ds_read_b64_tr_b16 v[0:1], v216 offset:0\n"
" ds_read_b64_tr_b16 v[2:3], v216 offset:4096\n"
" ds_read_b64_tr_b16 v[4:5], v217 offset:0\n"
" ds_read_b64_tr_b16 v[6:7], v217 offset:4096\n"
" ds_read_b64_tr_b16 v[8:9], v216 offset:8192\n"
" ds_read_b64_tr_b16 v[10:11], v216 offset:12288\n"
" ds_read_b64_tr_b16 v[12:13], v217 offset:8192\n"
" ds_read_b64_tr_b16 v[14:15], v217 offset:12288\n"
" s_waitcnt lgkmcnt(0)\n"
" s_waitcnt vmcnt(28)\n"
" s_barrier\n"
" v_add_u32 v216, 98304, v246\n"
" v_add_u32 v217, 98304, v247\n"
" ds_read_b64_tr_b16 v[16:17], v216 offset:0\n"
" ds_read_b64_tr_b16 v[18:19], v216 offset:4096\n"
" ds_read_b64_tr_b16 v[20:21], v217 offset:0\n"
" ds_read_b64_tr_b16 v[22:23], v217 offset:4096\n"
" ds_read_b64_tr_b16 v[24:25], v216 offset:8192\n"
" ds_read_b64_tr_b16 v[26:27], v216 offset:12288\n"
" ds_read_b64_tr_b16 v[28:29], v217 offset:8192\n"
" ds_read_b64_tr_b16 v[30:31], v217 offset:12288\n"
" s_waitcnt lgkmcnt(0)\n"
" s_waitcnt vmcnt(24)\n"
" s_barrier\n"
" v_add_u32 v216, 114688, v246\n"
" v_add_u32 v217, 114688, v247\n"
" ds_read_b64_tr_b16 v[32:33], v216 offset:0\n"
" ds_read_b64_tr_b16 v[34:35], v216 offset:4096\n"
" ds_read_b64_tr_b16 v[36:37], v217 offset:0\n"
" ds_read_b64_tr_b16 v[38:39], v217 offset:4096\n"
" ds_read_b64_tr_b16 v[40:41], v216 offset:8192\n"
" ds_read_b64_tr_b16 v[42:43], v216 offset:12288\n"
" ds_read_b64_tr_b16 v[44:45], v217 offset:8192\n"
" ds_read_b64_tr_b16 v[46:47], v217 offset:12288\n"
" s_waitcnt lgkmcnt(0)\n"
" s_waitcnt vmcnt(20)\n"
" s_barrier\n"
" v_add_u32 v216, 131072, v246\n"
" v_add_u32 v217, 131072, v247\n"
" ds_read_b64_tr_b16 v[48:49], v216 offset:0\n"
" ds_read_b64_tr_b16 v[50:51], v216 offset:4096\n"
" ds_read_b64_tr_b16 v[52:53], v217 offset:0\n"
" ds_read_b64_tr_b16 v[54:55], v217 offset:4096\n"
" ds_read_b64_tr_b16 v[56:57], v216 offset:8192\n"
" ds_read_b64_tr_b16 v[58:59], v216 offset:12288\n"
" ds_read_b64_tr_b16 v[60:61], v217 offset:8192\n"
" ds_read_b64_tr_b16 v[62:63], v217 offset:12288\n"
" s_waitcnt lgkmcnt(0)\n"
" s_waitcnt vmcnt(16)\n"
" s_barrier\n"
" ds_read_b64_tr_b16 v[64:65], v246 offset:0\n"
" ds_read_b64_tr_b16 v[66:67], v246 offset:4096\n"
" ds_read_b64_tr_b16 v[144:145], v247 offset:0\n"
" ds_read_b64_tr_b16 v[146:147], v247 offset:4096\n"
" ds_read_b64_tr_b16 v[68:69], v246 offset:8192\n"
" ds_read_b64_tr_b16 v[70:71], v246 offset:12288\n"
" ds_read_b64_tr_b16 v[148:149], v247 offset:8192\n"
" ds_read_b64_tr_b16 v[150:151], v247 offset:12288\n"
" s_waitcnt lgkmcnt(4)\n"
" ds_write_b128 v232, v[144:147] offset:0\n"
" s_waitcnt lgkmcnt(1)\n"
" ds_write_b128 v232, v[148:151] offset:1024\n"
" s_waitcnt lgkmcnt(0)\n"
" s_waitcnt vmcnt(12)\n"
" s_barrier\n"
" s_add_i32 m0, s77, 0\n"
" s_mov_b32 s76, 2621440\n"
" buffer_load_dwordx4 v244, s[60:63], s76 offen lds\n"
" s_add_i32 m0, s77, 1024\n"
" s_mov_b32 s76, 2621440\n"
" buffer_load_dwordx4 v245, s[60:63], s76 offen lds\n"
" s_add_i32 m0, s77, 2048\n"
" s_mov_b32 s76, 2686976\n"
" buffer_load_dwordx4 v244, s[60:63], s76 offen lds\n"
" s_add_i32 m0, s77, 3072\n"
" s_mov_b32 s76, 2686976\n"
" buffer_load_dwordx4 v245, s[60:63], s76 offen lds\n"
" ds_read_b64_tr_b16 v[72:73], v246 offset:16384\n"
" ds_read_b64_tr_b16 v[74:75], v246 offset:20480\n"
" ds_read_b64_tr_b16 v[144:145], v247 offset:16384\n"
" ds_read_b64_tr_b16 v[146:147], v247 offset:20480\n"
" ds_read_b64_tr_b16 v[76:77], v246 offset:24576\n"
" ds_read_b64_tr_b16 v[78:79], v246 offset:28672\n"
" ds_read_b64_tr_b16 v[148:149], v247 offset:24576\n"
" ds_read_b64_tr_b16 v[150:151], v247 offset:28672\n"
" s_waitcnt lgkmcnt(4)\n"
" ds_write_b128 v232, v[144:147] offset:2048\n"
" s_waitcnt lgkmcnt(1)\n"
" ds_write_b128 v232, v[148:151] offset:3072\n"
" s_waitcnt lgkmcnt(0)\n"
" s_waitcnt vmcnt(12)\n"
" s_barrier\n"
" s_add_i32 m0, s77, 16384\n"
" s_mov_b32 s76, 3145728\n"
" buffer_load_dwordx4 v244, s[60:63], s76 offen lds\n"
" s_add_i32 m0, s77, 17408\n"
" s_mov_b32 s76, 3145728\n"
" buffer_load_dwordx4 v245, s[60:63], s76 offen lds\n"
" s_add_i32 m0, s77, 18432\n"
" s_mov_b32 s76, 3211264\n"
" buffer_load_dwordx4 v244, s[60:63], s76 offen lds\n"
" s_add_i32 m0, s77, 19456\n"
" s_mov_b32 s76, 3211264\n"
" buffer_load_dwordx4 v245, s[60:63], s76 offen lds\n"
" ds_read_b64_tr_b16 v[80:81], v246 offset:32768\n"
" ds_read_b64_tr_b16 v[82:83], v246 offset:36864\n"
" ds_read_b64_tr_b16 v[144:145], v247 offset:32768\n"
" ds_read_b64_tr_b16 v[146:147], v247 offset:36864\n"
" ds_read_b64_tr_b16 v[84:85], v246 offset:40960\n"
" ds_read_b64_tr_b16 v[86:87], v246 offset:45056\n"
" ds_read_b64_tr_b16 v[148:149], v247 offset:40960\n"
" ds_read_b64_tr_b16 v[150:151], v247 offset:45056\n"
" s_waitcnt lgkmcnt(4)\n"
" ds_write_b128 v232, v[144:147] offset:4096\n"
" s_waitcnt lgkmcnt(1)\n"
" ds_write_b128 v232, v[148:151] offset:5120\n"
" s_waitcnt lgkmcnt(0)\n"
" s_waitcnt vmcnt(12)\n"
" s_barrier\n"
" s_add_i32 m0, s77, 32768\n"
" s_mov_b32 s76, 3670016\n"
" buffer_load_dwordx4 v244, s[60:63], s76 offen lds\n"
" s_add_i32 m0, s77, 33792\n"
" s_mov_b32 s76, 3670016\n"
" buffer_load_dwordx4 v245, s[60:63], s76 offen lds\n"
" s_add_i32 m0, s77, 34816\n"
" s_mov_b32 s76, 3735552\n"
" buffer_load_dwordx4 v244, s[60:63], s76 offen lds\n"
" s_add_i32 m0, s77, 35840\n"
" s_mov_b32 s76, 3735552\n"
" buffer_load_dwordx4 v245, s[60:63], s76 offen lds\n"
" v_add_u32 v216, 49152, v246\n"
" v_add_u32 v217, 49152, v247\n"
" ds_read_b64_tr_b16 v[88:89], v216 offset:0\n"
" ds_read_b64_tr_b16 v[90:91], v216 offset:4096\n"
" ds_read_b64_tr_b16 v[144:145], v217 offset:0\n"
" ds_read_b64_tr_b16 v[146:147], v217 offset:4096\n"
" ds_read_b64_tr_b16 v[92:93], v216 offset:8192\n"
" ds_read_b64_tr_b16 v[94:95], v216 offset:12288\n"
" ds_read_b64_tr_b16 v[148:149], v217 offset:8192\n"
" ds_read_b64_tr_b16 v[150:151], v217 offset:12288\n"
" s_waitcnt lgkmcnt(4)\n"
" ds_write_b128 v232, v[144:147] offset:6144\n"
" s_waitcnt lgkmcnt(1)\n"
" ds_write_b128 v232, v[148:151] offset:7168\n"
" s_waitcnt lgkmcnt(0)\n"
" s_waitcnt vmcnt(12)\n"
" s_barrier\n"
" s_add_i32 m0, s77, 49152\n"
" s_mov_b32 s76, 4194304\n"
" buffer_load_dwordx4 v244, s[60:63], s76 offen lds\n"
" s_add_i32 m0, s77, 50176\n"
" s_mov_b32 s76, 4194304\n"
" buffer_load_dwordx4 v245, s[60:63], s76 offen lds\n"
" s_add_i32 m0, s77, 51200\n"
" s_mov_b32 s76, 4259840\n"
" buffer_load_dwordx4 v244, s[60:63], s76 offen lds\n"
" s_add_i32 m0, s77, 52224\n"
" s_mov_b32 s76, 4259840\n"
" buffer_load_dwordx4 v245, s[60:63], s76 offen lds\n"
" v_add_u32 v216, 147456, v246\n"
" v_add_u32 v217, 147456, v247\n"
" ds_read_b64_tr_b16 v[96:97], v216 offset:0\n"
" ds_read_b64_tr_b16 v[98:99], v216 offset:4096\n"
" ds_read_b64_tr_b16 v[144:145], v217 offset:0\n"
" ds_read_b64_tr_b16 v[146:147], v217 offset:4096\n"
" ds_read_b64_tr_b16 v[100:101], v216 offset:8192\n"
" ds_read_b64_tr_b16 v[102:103], v216 offset:12288\n"
" ds_read_b64_tr_b16 v[148:149], v217 offset:8192\n"
" ds_read_b64_tr_b16 v[150:151], v217 offset:12288\n"
" s_waitcnt lgkmcnt(4)\n"
" ds_write_b128 v232, v[144:147] offset:8192\n"
" s_waitcnt lgkmcnt(1)\n"
" ds_write_b128 v232, v[148:151] offset:9216\n"
" s_waitcnt lgkmcnt(0)\n"
" s_waitcnt vmcnt(12)\n"
" s_barrier\n"
" s_add_i32 m0, s77, 147456\n"
" s_mov_b32 s76, 4718592\n"
" buffer_load_dwordx4 v244, s[60:63], s76 offen lds\n"
" s_add_i32 m0, s77, 148480\n"
" s_mov_b32 s76, 4718592\n"
" buffer_load_dwordx4 v245, s[60:63], s76 offen lds\n"
" s_add_i32 m0, s77, 149504\n"
" s_mov_b32 s76, 4784128\n"
" buffer_load_dwordx4 v244, s[60:63], s76 offen lds\n"
" s_add_i32 m0, s77, 150528\n"
" s_mov_b32 s76, 4784128\n"
" buffer_load_dwordx4 v245, s[60:63], s76 offen lds\n"
" ds_read_b64_tr_b16 v[104:105], v246 offset:0\n"
" ds_read_b64_tr_b16 v[106:107], v246 offset:4096\n"
" ds_read_b64_tr_b16 v[144:145], v247 offset:0\n"
" ds_read_b64_tr_b16 v[146:147], v247 offset:4096\n"
" ds_read_b64_tr_b16 v[108:109], v246 offset:8192\n"
" ds_read_b64_tr_b16 v[110:111], v246 offset:12288\n"
" ds_read_b64_tr_b16 v[148:149], v247 offset:8192\n"
" ds_read_b64_tr_b16 v[150:151], v247 offset:12288\n"
" s_waitcnt lgkmcnt(4)\n"
" ds_write_b128 v232, v[144:147] offset:10240\n"
" s_waitcnt lgkmcnt(1)\n"
" ds_write_b128 v232, v[148:151] offset:11264\n"
" s_waitcnt lgkmcnt(0)\n"
" s_waitcnt vmcnt(12)\n"
" s_barrier\n"
" ds_read_b64_tr_b16 v[112:113], v246 offset:16384\n"
" ds_read_b64_tr_b16 v[114:115], v246 offset:20480\n"
" ds_read_b64_tr_b16 v[144:145], v247 offset:16384\n"
" ds_read_b64_tr_b16 v[146:147], v247 offset:20480\n"
" ds_read_b64_tr_b16 v[116:117], v246 offset:24576\n"
" ds_read_b64_tr_b16 v[118:119], v246 offset:28672\n"
" ds_read_b64_tr_b16 v[148:149], v247 offset:24576\n"
" ds_read_b64_tr_b16 v[150:151], v247 offset:28672\n"
" s_waitcnt lgkmcnt(4)\n"
" ds_write_b128 v232, v[144:147] offset:12288\n"
" s_waitcnt lgkmcnt(1)\n"
" ds_write_b128 v232, v[148:151] offset:13312\n"
" s_waitcnt lgkmcnt(0)\n"
" s_waitcnt vmcnt(8)\n"
" s_barrier\n"
" ds_read_b64_tr_b16 v[120:121], v246 offset:32768\n"
" ds_read_b64_tr_b16 v[122:123], v246 offset:36864\n"
" ds_read_b64_tr_b16 v[144:145], v247 offset:32768\n"
" ds_read_b64_tr_b16 v[146:147], v247 offset:36864\n"
" ds_read_b64_tr_b16 v[124:125], v246 offset:40960\n"
" ds_read_b64_tr_b16 v[126:127], v246 offset:45056\n"
" ds_read_b64_tr_b16 v[148:149], v247 offset:40960\n"
" ds_read_b64_tr_b16 v[150:151], v247 offset:45056\n"
" s_waitcnt lgkmcnt(4)\n"
" ds_write_b128 v232, v[144:147] offset:14336\n"
" s_waitcnt lgkmcnt(1)\n"
" ds_write_b128 v232, v[148:151] offset:15360\n"
" s_waitcnt lgkmcnt(0)\n"
" s_waitcnt vmcnt(4)\n"
" s_barrier\n"
" v_add_u32 v216, 49152, v246\n"
" v_add_u32 v217, 49152, v247\n"
" ds_read_b64_tr_b16 v[128:129], v216 offset:0\n"
" ds_read_b64_tr_b16 v[130:131], v216 offset:4096\n"
" ds_read_b64_tr_b16 v[144:145], v217 offset:0\n"
" ds_read_b64_tr_b16 v[146:147], v217 offset:4096\n"
" ds_read_b64_tr_b16 v[132:133], v216 offset:8192\n"
" ds_read_b64_tr_b16 v[134:135], v216 offset:12288\n"
" ds_read_b64_tr_b16 v[148:149], v217 offset:8192\n"
" ds_read_b64_tr_b16 v[150:151], v217 offset:12288\n"
" s_waitcnt lgkmcnt(4)\n"
" ds_write_b128 v232, v[144:147] offset:16384\n"
" s_waitcnt lgkmcnt(1)\n"
" ds_write_b128 v232, v[148:151] offset:17408\n"
" s_waitcnt lgkmcnt(0)\n"
" s_waitcnt vmcnt(0)\n"
" s_barrier\n"
" v_add_u32 v216, 147456, v246\n"
" v_add_u32 v217, 147456, v247\n"
" ds_read_b64_tr_b16 v[136:137], v216 offset:0\n"
" ds_read_b64_tr_b16 v[138:139], v216 offset:4096\n"
" ds_read_b64_tr_b16 v[144:145], v217 offset:0\n"
" ds_read_b64_tr_b16 v[146:147], v217 offset:4096\n"
" ds_read_b64_tr_b16 v[140:141], v216 offset:8192\n"
" ds_read_b64_tr_b16 v[142:143], v216 offset:12288\n"
" ds_read_b64_tr_b16 v[148:149], v217 offset:8192\n"
" ds_read_b64_tr_b16 v[150:151], v217 offset:12288\n"
" s_waitcnt lgkmcnt(4)\n"
" ds_write_b128 v232, v[144:147] offset:18432\n"
" s_waitcnt lgkmcnt(1)\n"
" ds_write_b128 v232, v[148:151] offset:19456\n"
" s_waitcnt lgkmcnt(0)\n"
" s_waitcnt vmcnt(0)\n"
" s_barrier\n"
" v_mov_b32 v240, 0x3f803f80\n"
" v_mov_b32 v241, 0x3f803f80\n"
" v_mov_b32 v242, 0x3f803f80\n"
" v_mov_b32 v243, 0x3f803f80\n"
" s_mov_b32 s98, 0x3e0293ee\n"
" s_mov_b32 s99, 0x3e0293ee\n"
" s_lshl_b32 s77, s69, 10\n"
" s_add_i32 s95, s68, -2\n"
" s_mov_b32 s91, 0\n"
".Lhd_pol_%=:\n"
" buffer_load_dwordx2 v[220:221], v235, s[48:51], 0 offen sc1\n"
" s_waitcnt vmcnt(0)\n"
" v_or_b32 v222, 1, v220\n"
" v_mov_b32 v223, v221\n"
" v_cmp_ne_u64 vcc, s[16:17], v[222:223]\n"
" s_nop 0\n"
" s_cbranch_vccz .Lhd_polok_%=\n"
" s_add_i32 s91, s91, 1\n"
" s_or_b32 s91, s91, s90\n"
" s_cmp_ge_u32 s91, 65536\n"
" s_cselect_b32 s90, 65536, 0\n"
" s_cbranch_scc1 .Lhd_polok_%=\n"
" s_sleep 4\n"
" s_branch .Lhd_pol_%=\n"
".Lhd_polok_%=:\n"
" v_and_b32 v220, 1, v220\n"
" v_cmp_ne_u32_e64 s[86:87], 1, v220\n"
" s_nop 1\n"
" s_bitcmp1_b32 s86, 0\n"
" s_cselect_b32 s76, 8192, 0\n"
" s_add_u32 s48, s48, s76\n"
" s_lshl_b32 s76, s76, 1\n"
" s_sub_i32 s76, 8192, s76\n"
" v_add_u32 v228, s76, v228\n"
" v_add_u32 v233, s76, v233\n"
" s_cmp_eq_u32 s70, 0\n"
" s_cselect_b32 s76, s76, 0\n"
" v_add_u32 v227, s76, v227\n"
" s_bitcmp0_b64 s[86:87], 5\n"
" s_cselect_b32 s76, 8192, 0\n"
" s_add_u32 s44, s44, s76\n"
" s_bitcmp0_b64 s[86:87], s71\n"
" s_cselect_b32 s76, 8192, 0\n"
" s_add_u32 s40, s40, s76\n"
" v_mov_b32 v144, -1\n"
" v_mov_b32 v145, -1\n"
" v_mov_b32 v146, -1\n"
" v_mov_b32 v147, -1\n"
" s_movk_i32 s76, 4096\n"
" buffer_store_dwordx4 v[144:147], v224, s[44:47], 0 offen sc1\n"
" buffer_store_dwordx4 v[144:147], v224, s[44:47], s76 offen sc1\n"
".Lhd_loop_%=:\n"
" s_cmp_ge_u32 s68, 64\n"
" s_cbranch_scc1 .Lhd_att_%=\n"
" s_mov_b32 s91, 0\n"
" buffer_load_dword v235, v227, s[48:51], 0 offen sc1\n"
" s_waitcnt vmcnt(0)\n"
".Lhd_in_%=:\n"
" v_cmp_gt_i32 vcc, s68, v235\n"
" s_nop 0\n"
" s_cbranch_vccz .Lhd_inok_%=\n"
" s_add_i32 s91, s91, 1\n"
" s_or_b32 s91, s91, s90\n"
" s_cmp_ge_u32 s91, 65536\n"
" s_cselect_b32 s90, 65536, 0\n"
" s_cbranch_scc1 .Lhd_inok_%=\n"
" s_sleep 2\n"
" buffer_load_dword v235, v227, s[48:51], 0 offen sc1\n"
" s_waitcnt vmcnt(0)\n"
" s_branch .Lhd_in_%=\n"
".Lhd_inok_%=:\n"
" s_and_b32 s92, s68, s73\n"
" s_lshl_b32 s92, s92, 17\n"
" s_add_i32 s92, s92, s84\n"
" s_lshr_b32 s76, s92, 21\n"
" s_bitcmp1_b64 s[86:87], s76\n"
" s_cselect_b32 s76, 8192, 0\n"
" s_add_i32 s92, s92, s76\n"
" s_lshl_b32 s78, s69, 8\n"
" s_add_i32 s92, s92, s78\n"
" s_barrier\n"
" s_add_i32 m0, s77, 0\n"
" s_add_i32 s76, s92, 0\n"
" buffer_load_dwordx4 v226, s[36:39], s76 offen sc1 lds\n"
" s_add_i32 m0, s77, 20480\n"
" s_add_i32 s76, s92, 65536\n"
" buffer_load_dwordx4 v226, s[36:39], s76 offen sc1 lds\n"
" s_add_i32 m0, s77, 4096\n"
" s_add_i32 s76, s92, 1024\n"
" buffer_load_dwordx4 v226, s[36:39], s76 offen sc1 lds\n"
" s_add_i32 m0, s77, 24576\n"
" s_add_i32 s76, s92, 66560\n"
" buffer_load_dwordx4 v226, s[36:39], s76 offen sc1 lds\n"
" s_add_i32 m0, s77, 8192\n"
" s_add_i32 s76, s92, 2048\n"
" buffer_load_dwordx4 v226, s[36:39], s76 offen sc1 lds\n"
" s_add_i32 m0, s77, 28672\n"
" s_add_i32 s76, s92, 67584\n"
" buffer_load_dwordx4 v226, s[36:39], s76 offen sc1 lds\n"
" s_add_i32 m0, s77, 12288\n"
" s_add_i32 s76, s92, 3072\n"
" buffer_load_dwordx4 v226, s[36:39], s76 offen sc1 lds\n"
" s_add_i32 m0, s77, 32768\n"
" s_add_i32 s76, s92, 68608\n"
" buffer_load_dwordx4 v226, s[36:39], s76 offen sc1 lds\n"
" s_add_i32 m0, s77, 16384\n"
" s_add_i32 s76, s92, 4096\n"
" buffer_load_dwordx4 v226, s[36:39], s76 offen sc1 lds\n"
" s_add_i32 m0, s77, 36864\n"
" s_add_i32 s76, s92, 69632\n"
" buffer_load_dwordx4 v226, s[36:39], s76 offen sc1 lds\n"
" s_and_b32 s93, s68, 3\n"
" s_lshl_b32 s93, s93, 15\n"
" s_waitcnt vmcnt(8)\n"
" s_barrier\n"
" ds_read_b128 v[144:147], v225 offset:0\n"
" ds_read_b128 v[148:151], v225 offset:20480\n"
" ds_read_b128 v[152:155], v225 offset:1024\n"
" ds_read_b128 v[156:159], v225 offset:21504\n"
" s_waitcnt lgkmcnt(3)\n"
" v_mfma_f32_16x16x32_bf16 v[168:171], a[0:3], v[144:147], 0\n"
" v_mfma_f32_16x16x32_bf16 v[172:175], a[4:7], v[144:147], 0\n"
" v_mfma_f32_16x16x32_bf16 v[184:187], v[144:147], a[160:163], 0\n"
" ds_read_b128 v[160:163], v225 offset:2048\n"
" v_mfma_f32_16x16x32_bf16 v[192:195], v[144:147], a[164:167], 0\n"
" s_waitcnt lgkmcnt(3)\n"
" v_mfma_f32_16x16x32_bf16 v[176:179], a[0:3], v[148:151], 0\n"
" ds_read_b128 v[164:167], v225 offset:22528\n"
" v_mfma_f32_16x16x32_bf16 v[180:183], a[4:7], v[148:151], 0\n"
" v_mfma_f32_16x16x32_bf16 v[188:191], v[148:151], a[160:163], 0\n"
" v_mfma_f32_16x16x32_bf16 v[196:199], v[148:151], a[164:167], 0\n"
" s_waitcnt lgkmcnt(3)\n"
" v_mfma_f32_16x16x32_bf16 v[168:171], a[8:11], v[152:155], v[168:171]\n"
" s_waitcnt vmcnt(4)\n"
" v_mfma_f32_16x16x32_bf16 v[172:175], a[12:15], v[152:155], v[172:175]\n"
" s_barrier\n"
" v_mfma_f32_16x16x32_bf16 v[184:187], v[152:155], a[168:171], v[184:187]\n"
" ds_read_b128 v[144:147], v225 offset:3072\n"
" v_mfma_f32_16x16x32_bf16 v[192:195], v[152:155], a[172:175], v[192:195]\n"
" s_waitcnt lgkmcnt(3)\n"
" v_mfma_f32_16x16x32_bf16 v[176:179], a[8:11], v[156:159], v[176:179]\n"
" ds_read_b128 v[148:151], v225 offset:23552\n"
" v_mfma_f32_16x16x32_bf16 v[180:183], a[12:15], v[156:159], v[180:183]\n"
" v_mfma_f32_16x16x32_bf16 v[188:191], v[156:159], a[168:171], v[188:191]\n"
" v_mfma_f32_16x16x32_bf16 v[196:199], v[156:159], a[172:175], v[196:199]\n"
" s_waitcnt lgkmcnt(3)\n"
" v_mfma_f32_16x16x32_bf16 v[168:171], a[16:19], v[160:163], v[168:171]\n"
" v_mfma_f32_16x16x32_bf16 v[172:175], a[20:23], v[160:163], v[172:175]\n"
" v_mfma_f32_16x16x32_bf16 v[184:187], v[160:163], a[176:179], v[184:187]\n"
" ds_read_b128 v[152:155], v225 offset:4096\n"
" v_mfma_f32_16x16x32_bf16 v[192:195], v[160:163], a[180:183], v[192:195]\n"
" s_waitcnt lgkmcnt(3)\n"
" v_mfma_f32_16x16x32_bf16 v[176:179], a[16:19], v[164:167], v[176:179]\n"
" ds_read_b128 v[156:159], v225 offset:24576\n"
" v_mfma_f32_16x16x32_bf16 v[180:183], a[20:23], v[164:167], v[180:183]\n"
" v_mfma_f32_16x16x32_bf16 v[188:191], v[164:167], a[176:179], v[188:191]\n"
" v_mfma_f32_16x16x32_bf16 v[196:199], v[164:167], a[180:183], v[196:199]\n"
" s_waitcnt lgkmcnt(3)\n"
" v_mfma_f32_16x16x32_bf16 v[168:171], a[24:27], v[144:147], v[168:171]\n"
" v_mfma_f32_16x16x32_bf16 v[172:175], a[28:31], v[144:147], v[172:175]\n"
" v_mfma_f32_16x16x32_bf16 v[184:187], v[144:147], a[184:187], v[184:187]\n"
" ds_read_b128 v[160:163], v225 offset:5120\n"
" v_mfma_f32_16x16x32_bf16 v[192:195], v[144:147], a[188:191], v[192:195]\n"
" s_waitcnt lgkmcnt(3)\n"
" v_mfma_f32_16x16x32_bf16 v[176:179], a[24:27], v[148:151], v[176:179]\n"
" ds_read_b128 v[164:167], v225 offset:25600\n"
" v_mfma_f32_16x16x32_bf16 v[180:183], a[28:31], v[148:151], v[180:183]\n"
" v_mfma_f32_16x16x32_bf16 v[188:191], v[148:151], a[184:187], v[188:191]\n"
" v_mfma_f32_16x16x32_bf16 v[196:199], v[148:151], a[188:191], v[196:199]\n"
" s_waitcnt lgkmcnt(3)\n"
" v_mfma_f32_16x16x32_bf16 v[168:171], a[32:35], v[152:155], v[168:171]\n"
" v_mfma_f32_16x16x32_bf16 v[172:175], a[36:39], v[152:155], v[172:175]\n"
" v_mfma_f32_16x16x32_bf16 v[184:187], v[152:155], a[192:195], v[184:187]\n"
" ds_read_b128 v[144:147], v225 offset:6144\n"
" v_mfma_f32_16x16x32_bf16 v[192:195], v[152:155], a[196:199], v[192:195]\n"
" s_waitcnt lgkmcnt(3)\n"
" v_mfma_f32_16x16x32_bf16 v[176:179], a[32:35], v[156:159], v[176:179]\n"
" ds_read_b128 v[148:151], v225 offset:26624\n"
" v_mfma_f32_16x16x32_bf16 v[180:183], a[36:39], v[156:159], v[180:183]\n"
" v_mfma_f32_16x16x32_bf16 v[188:191], v[156:159], a[192:195], v[188:191]\n"
" v_mfma_f32_16x16x32_bf16 v[196:199], v[156:159], a[196:199], v[196:199]\n"
" s_waitcnt lgkmcnt(3)\n"
" v_mfma_f32_16x16x32_bf16 v[168:171], a[40:43], v[160:163], v[168:171]\n"
" v_mfma_f32_16x16x32_bf16 v[172:175], a[44:47], v[160:163], v[172:175]\n"
" v_mfma_f32_16x16x32_bf16 v[184:187], v[160:163], a[200:203], v[184:187]\n"
" ds_read_b128 v[152:155], v225 offset:7168\n"
" v_mfma_f32_16x16x32_bf16 v[192:195], v[160:163], a[204:207], v[192:195]\n"
" s_waitcnt lgkmcnt(3)\n"
" v_mfma_f32_16x16x32_bf16 v[176:179], a[40:43], v[164:167], v[176:179]\n"
" ds_read_b128 v[156:159], v225 offset:27648\n"
" v_mfma_f32_16x16x32_bf16 v[180:183], a[44:47], v[164:167], v[180:183]\n"
" v_mfma_f32_16x16x32_bf16 v[188:191], v[164:167], a[200:203], v[188:191]\n"
" v_mfma_f32_16x16x32_bf16 v[196:199], v[164:167], a[204:207], v[196:199]\n"
" s_waitcnt lgkmcnt(3)\n"
" v_mfma_f32_16x16x32_bf16 v[168:171], a[48:51], v[144:147], v[168:171]\n"
" v_mfma_f32_16x16x32_bf16 v[172:175], a[52:55], v[144:147], v[172:175]\n"
" v_mfma_f32_16x16x32_bf16 v[184:187], v[144:147], a[208:211], v[184:187]\n"
" ds_read_b128 v[160:163], v225 offset:8192\n"
" v_mfma_f32_16x16x32_bf16 v[192:195], v[144:147], a[212:215], v[192:195]\n"
" s_waitcnt lgkmcnt(3)\n"
" v_mfma_f32_16x16x32_bf16 v[176:179], a[48:51], v[148:151], v[176:179]\n"
" ds_read_b128 v[164:167], v225 offset:28672\n"
" v_mfma_f32_16x16x32_bf16 v[180:183], a[52:55], v[148:151], v[180:183]\n"
" v_mfma_f32_16x16x32_bf16 v[188:191], v[148:151], a[208:211], v[188:191]\n"
" v_mfma_f32_16x16x32_bf16 v[196:199], v[148:151], a[212:215], v[196:199]\n"
" s_waitcnt lgkmcnt(3)\n"
" v_mfma_f32_16x16x32_bf16 v[168:171], a[56:59], v[152:155], v[168:171]\n"
" s_waitcnt vmcnt(0)\n"
" v_mfma_f32_16x16x32_bf16 v[172:175], a[60:63], v[152:155], v[172:175]\n"
" s_barrier\n"
" v_mfma_f32_16x16x32_bf16 v[184:187], v[152:155], a[216:219], v[184:187]\n"
" ds_read_b128 v[144:147], v225 offset:9216\n"
" v_mfma_f32_16x16x32_bf16 v[192:195], v[152:155], a[220:223], v[192:195]\n"
" s_waitcnt lgkmcnt(3)\n"
" v_mfma_f32_16x16x32_bf16 v[176:179], a[56:59], v[156:159], v[176:179]\n"
" ds_read_b128 v[148:151], v225 offset:29696\n"
" v_mfma_f32_16x16x32_bf16 v[180:183], a[60:63], v[156:159], v[180:183]\n"
" v_mfma_f32_16x16x32_bf16 v[188:191], v[156:159], a[216:219], v[188:191]\n"
" v_mfma_f32_16x16x32_bf16 v[196:199], v[156:159], a[220:223], v[196:199]\n"
" s_waitcnt lgkmcnt(3)\n"
" v_mfma_f32_16x16x32_bf16 v[168:171], a[64:67], v[160:163], v[168:171]\n"
" v_mfma_f32_16x16x32_bf16 v[172:175], a[68:71], v[160:163], v[172:175]\n"
" v_mfma_f32_16x16x32_bf16 v[184:187], v[160:163], a[224:227], v[184:187]\n"
" ds_read_b128 v[152:155], v225 offset:10240\n"
" v_mfma_f32_16x16x32_bf16 v[192:195], v[160:163], a[228:231], v[192:195]\n"
" s_waitcnt lgkmcnt(3)\n"
" v_mfma_f32_16x16x32_bf16 v[176:179], a[64:67], v[164:167], v[176:179]\n"
" ds_read_b128 v[156:159], v225 offset:30720\n"
" v_mfma_f32_16x16x32_bf16 v[180:183], a[68:71], v[164:167], v[180:183]\n"
" v_mfma_f32_16x16x32_bf16 v[188:191], v[164:167], a[224:227], v[188:191]\n"
" v_mfma_f32_16x16x32_bf16 v[196:199], v[164:167], a[228:231], v[196:199]\n"
" s_waitcnt lgkmcnt(3)\n"
" v_mfma_f32_16x16x32_bf16 v[168:171], a[72:75], v[144:147], v[168:171]\n"
" v_mfma_f32_16x16x32_bf16 v[172:175], a[76:79], v[144:147], v[172:175]\n"
" v_mfma_f32_16x16x32_bf16 v[184:187], v[144:147], a[232:235], v[184:187]\n"
" ds_read_b128 v[160:163], v225 offset:11264\n"
" v_mfma_f32_16x16x32_bf16 v[192:195], v[144:147], a[236:239], v[192:195]\n"
" s_waitcnt lgkmcnt(3)\n"
" v_mfma_f32_16x16x32_bf16 v[176:179], a[72:75], v[148:151], v[176:179]\n"
" ds_read_b128 v[164:167], v225 offset:31744\n"
" v_mfma_f32_16x16x32_bf16 v[180:183], a[76:79], v[148:151], v[180:183]\n"
" v_mfma_f32_16x16x32_bf16 v[188:191], v[148:151], a[232:235], v[188:191]\n"
" v_mfma_f32_16x16x32_bf16 v[196:199], v[148:151], a[236:239], v[196:199]\n"
" s_waitcnt lgkmcnt(3)\n"
" v_mfma_f32_16x16x32_bf16 v[168:171], a[80:83], v[152:155], v[168:171]\n"
" v_mfma_f32_16x16x32_bf16 v[172:175], a[84:87], v[152:155], v[172:175]\n"
" v_mfma_f32_16x16x32_bf16 v[184:187], v[152:155], a[240:243], v[184:187]\n"
" ds_read_b128 v[144:147], v225 offset:12288\n"
" v_mfma_f32_16x16x32_bf16 v[192:195], v[152:155], a[244:247], v[192:195]\n"
" s_waitcnt lgkmcnt(3)\n"
" v_mfma_f32_16x16x32_bf16 v[176:179], a[80:83], v[156:159], v[176:179]\n"
" ds_read_b128 v[148:151], v225 offset:32768\n"
" v_mfma_f32_16x16x32_bf16 v[180:183], a[84:87], v[156:159], v[180:183]\n"
" v_mfma_f32_16x16x32_bf16 v[188:191], v[156:159], a[240:243], v[188:191]\n"
" v_mfma_f32_16x16x32_bf16 v[196:199], v[156:159], a[244:247], v[196:199]\n"
" s_waitcnt lgkmcnt(3)\n"
" v_mfma_f32_16x16x32_bf16 v[168:171], a[88:91], v[160:163], v[168:171]\n"
" v_mfma_f32_16x16x32_bf16 v[172:175], a[92:95], v[160:163], v[172:175]\n"
" v_mfma_f32_16x16x32_bf16 v[184:187], v[160:163], a[248:251], v[184:187]\n"
" ds_read_b128 v[152:155], v225 offset:13312\n"
" v_mfma_f32_16x16x32_bf16 v[192:195], v[160:163], a[252:255], v[192:195]\n"
" s_waitcnt lgkmcnt(3)\n"
" v_mfma_f32_16x16x32_bf16 v[176:179], a[88:91], v[164:167], v[176:179]\n"
" ds_read_b128 v[156:159], v225 offset:33792\n"
" v_mfma_f32_16x16x32_bf16 v[180:183], a[92:95], v[164:167], v[180:183]\n"
" v_mfma_f32_16x16x32_bf16 v[188:191], v[164:167], a[248:251], v[188:191]\n"
" v_mfma_f32_16x16x32_bf16 v[196:199], v[164:167], a[252:255], v[196:199]\n"
" s_waitcnt lgkmcnt(3)\n"
" v_mfma_f32_16x16x32_bf16 v[168:171], a[96:99], v[144:147], v[168:171]\n"
" v_mfma_f32_16x16x32_bf16 v[172:175], a[100:103], v[144:147], v[172:175]\n"
" v_mfma_f32_16x16x32_bf16 v[184:187], v[144:147], v[0:3], v[184:187]\n"
" ds_read_b128 v[160:163], v225 offset:14336\n"
" v_mfma_f32_16x16x32_bf16 v[192:195], v[144:147], v[4:7], v[192:195]\n"
" s_waitcnt lgkmcnt(3)\n"
" v_mfma_f32_16x16x32_bf16 v[176:179], a[96:99], v[148:151], v[176:179]\n"
" ds_read_b128 v[164:167], v225 offset:34816\n"
" v_mfma_f32_16x16x32_bf16 v[180:183], a[100:103], v[148:151], v[180:183]\n"
" v_mfma_f32_16x16x32_bf16 v[188:191], v[148:151], v[0:3], v[188:191]\n"
" v_mfma_f32_16x16x32_bf16 v[196:199], v[148:151], v[4:7], v[196:199]\n"
" s_waitcnt lgkmcnt(3)\n"
" v_mfma_f32_16x16x32_bf16 v[168:171], a[104:107], v[152:155], v[168:171]\n"
" v_mfma_f32_16x16x32_bf16 v[172:175], a[108:111], v[152:155], v[172:175]\n"
" v_mfma_f32_16x16x32_bf16 v[184:187], v[152:155], v[8:11], v[184:187]\n"
" ds_read_b128 v[144:147], v225 offset:15360\n"
" v_mfma_f32_16x16x32_bf16 v[192:195], v[152:155], v[12:15], v[192:195]\n"
" s_waitcnt lgkmcnt(3)\n"
" v_mfma_f32_16x16x32_bf16 v[176:179], a[104:107], v[156:159], v[176:179]\n"
" ds_read_b128 v[148:151], v225 offset:35840\n"
" v_mfma_f32_16x16x32_bf16 v[180:183], a[108:111], v[156:159], v[180:183]\n"
" v_mfma_f32_16x16x32_bf16 v[188:191], v[156:159], v[8:11], v[188:191]\n"
" v_mfma_f32_16x16x32_bf16 v[196:199], v[156:159], v[12:15], v[196:199]\n"
" s_waitcnt lgkmcnt(3)\n"
" v_mfma_f32_16x16x32_bf16 v[168:171], a[112:115], v[160:163], v[168:171]\n"
" v_mfma_f32_16x16x32_bf16 v[172:175], a[116:119], v[160:163], v[172:175]\n"
" v_mfma_f32_16x16x32_bf16 v[184:187], v[160:163], v[16:19], v[184:187]\n"
" ds_read_b128 v[152:155], v225 offset:16384\n"
" v_mfma_f32_16x16x32_bf16 v[192:195], v[160:163], v[20:23], v[192:195]\n"
" s_waitcnt lgkmcnt(3)\n"
" v_mfma_f32_16x16x32_bf16 v[176:179], a[112:115], v[164:167], v[176:179]\n"
" ds_read_b128 v[156:159], v225 offset:36864\n"
" v_mfma_f32_16x16x32_bf16 v[180:183], a[116:119], v[164:167], v[180:183]\n"
" v_mfma_f32_16x16x32_bf16 v[188:191], v[164:167], v[16:19], v[188:191]\n"
" v_mfma_f32_16x16x32_bf16 v[196:199], v[164:167], v[20:23], v[196:199]\n"
" s_waitcnt lgkmcnt(3)\n"
" v_mfma_f32_16x16x32_bf16 v[168:171], a[120:123], v[144:147], v[168:171]\n"
" v_mfma_f32_16x16x32_bf16 v[172:175], a[124:127], v[144:147], v[172:175]\n"
" v_mfma_f32_16x16x32_bf16 v[184:187], v[144:147], v[24:27], v[184:187]\n"
" ds_read_b128 v[160:163], v225 offset:17408\n"
" v_mfma_f32_16x16x32_bf16 v[192:195], v[144:147], v[28:31], v[192:195]\n"
" s_waitcnt lgkmcnt(3)\n"
" v_mfma_f32_16x16x32_bf16 v[176:179], a[120:123], v[148:151], v[176:179]\n"
" ds_read_b128 v[164:167], v225 offset:37888\n"
" v_mfma_f32_16x16x32_bf16 v[180:183], a[124:127], v[148:151], v[180:183]\n"
" v_mfma_f32_16x16x32_bf16 v[188:191], v[148:151], v[24:27], v[188:191]\n"
" v_mfma_f32_16x16x32_bf16 v[196:199], v[148:151], v[28:31], v[196:199]\n"
" s_waitcnt lgkmcnt(3)\n"
" v_mfma_f32_16x16x32_bf16 v[168:171], a[128:131], v[152:155], v[168:171]\n"
" v_mfma_f32_16x16x32_bf16 v[172:175], a[132:135], v[152:155], v[172:175]\n"
" v_mfma_f32_16x16x32_bf16 v[184:187], v[152:155], v[32:35], v[184:187]\n"
" ds_read_b128 v[144:147], v225 offset:18432\n"
" v_mfma_f32_16x16x32_bf16 v[192:195], v[152:155], v[36:39], v[192:195]\n"
" s_waitcnt lgkmcnt(3)\n"
" v_mfma_f32_16x16x32_bf16 v[176:179], a[128:131], v[156:159], v[176:179]\n"
" ds_read_b128 v[148:151], v225 offset:38912\n"
" v_mfma_f32_16x16x32_bf16 v[180:183], a[132:135], v[156:159], v[180:183]\n"
" v_mfma_f32_16x16x32_bf16 v[188:191], v[156:159], v[32:35], v[188:191]\n"
" v_mfma_f32_16x16x32_bf16 v[196:199], v[156:159], v[36:39], v[196:199]\n"
" s_waitcnt lgkmcnt(3)\n"
" v_mfma_f32_16x16x32_bf16 v[168:171], a[136:139], v[160:163], v[168:171]\n"
" v_mfma_f32_16x16x32_bf16 v[172:175], a[140:143], v[160:163], v[172:175]\n"
" v_mfma_f32_16x16x32_bf16 v[184:187], v[160:163], v[40:43], v[184:187]\n"
" ds_read_b128 v[152:155], v225 offset:19456\n"
" v_mfma_f32_16x16x32_bf16 v[192:195], v[160:163], v[44:47], v[192:195]\n"
" s_waitcnt lgkmcnt(3)\n"
" v_mfma_f32_16x16x32_bf16 v[176:179], a[136:139], v[164:167], v[176:179]\n"
" ds_read_b128 v[156:159], v225 offset:39936\n"
" v_mfma_f32_16x16x32_bf16 v[180:183], a[140:143], v[164:167], v[180:183]\n"
" v_mfma_f32_16x16x32_bf16 v[188:191], v[164:167], v[40:43], v[188:191]\n"
" v_mfma_f32_16x16x32_bf16 v[196:199], v[164:167], v[44:47], v[196:199]\n"
" s_waitcnt lgkmcnt(3)\n"
" v_mfma_f32_16x16x32_bf16 v[168:171], a[144:147], v[144:147], v[168:171]\n"
" v_mfma_f32_16x16x32_bf16 v[172:175], a[148:151], v[144:147], v[172:175]\n"
" v_mfma_f32_16x16x32_bf16 v[184:187], v[144:147], v[48:51], v[184:187]\n"
" v_mfma_f32_16x16x32_bf16 v[192:195], v[144:147], v[52:55], v[192:195]\n"
" s_waitcnt lgkmcnt(2)\n"
" v_mfma_f32_16x16x32_bf16 v[176:179], a[144:147], v[148:151], v[176:179]\n"
" v_mfma_f32_16x16x32_bf16 v[180:183], a[148:151], v[148:151], v[180:183]\n"
" v_mfma_f32_16x16x32_bf16 v[188:191], v[148:151], v[48:51], v[188:191]\n"
" v_mfma_f32_16x16x32_bf16 v[196:199], v[148:151], v[52:55], v[196:199]\n"
" s_waitcnt lgkmcnt(1)\n"
" v_mfma_f32_16x16x32_bf16 v[168:171], a[152:155], v[152:155], v[168:171]\n"
" v_mfma_f32_16x16x32_bf16 v[172:175], a[156:159], v[152:155], v[172:175]\n"
" v_mfma_f32_16x16x32_bf16 v[184:187], v[152:155], v[56:59], v[184:187]\n"
" v_mfma_f32_16x16x32_bf16 v[192:195], v[152:155], v[60:63], v[192:195]\n"
" s_waitcnt lgkmcnt(0)\n"
" v_mfma_f32_16x16x32_bf16 v[176:179], a[152:155], v[156:159], v[176:179]\n"
" v_mfma_f32_16x16x32_bf16 v[180:183], a[156:159], v[156:159], v[180:183]\n"
" v_mfma_f32_16x16x32_bf16 v[188:191], v[156:159], v[56:59], v[188:191]\n"
" v_mfma_f32_16x16x32_bf16 v[196:199], v[156:159], v[60:63], v[196:199]\n"
" v_cvt_pk_bf16_f32 v200, v168, v169\n"
" v_cvt_pk_bf16_f32 v201, v170, v171\n"
" v_cvt_pk_bf16_f32 v202, v172, v173\n"
" v_cvt_pk_bf16_f32 v203, v174, v175\n"
" s_add_i32 s76, s93, 0\n"
" buffer_store_dwordx4 v[200:203], v224, s[40:43], s76 offen sc1\n"
" v_cvt_pk_bf16_f32 v204, v176, v177\n"
" v_cvt_pk_bf16_f32 v205, v178, v179\n"
" v_cvt_pk_bf16_f32 v206, v180, v181\n"
" v_cvt_pk_bf16_f32 v207, v182, v183\n"
" s_add_i32 s76, s93, 4096\n"
" buffer_store_dwordx4 v[204:207], v224, s[40:43], s76 offen sc1\n"
" s_add_i32 s76, s93, 16384\n"
" s_add_i32 s76, s76, s77\n"
" v_cvt_pk_bf16_f32 v208, v184, v185\n"
" v_cvt_pk_bf16_f32 v209, v186, v187\n"
" v_cvt_pk_bf16_f32 v210, v188, v189\n"
" v_cvt_pk_bf16_f32 v211, v190, v191\n"
" buffer_store_dwordx4 v[208:211], v224, s[40:43], s76 offen offset:0 sc1\n"
" v_cvt_pk_bf16_f32 v212, v192, v193\n"
" v_cvt_pk_bf16_f32 v213, v194, v195\n"
" v_cvt_pk_bf16_f32 v214, v196, v197\n"
" v_cvt_pk_bf16_f32 v215, v198, v199\n"
" buffer_store_dwordx4 v[212:215], v224, s[40:43], s76 offen offset:1024 sc1\n"
" s_cmp_lt_i32 s95, 0\n"
" s_cbranch_scc1 .Lhd_qph_%=\n"
".Lhd_att_%=:\n"
" s_waitcnt lgkmcnt(0)\n"
" s_barrier\n"
" ds_read_b128 v[168:171], v225 offset:40960\n"
" ds_read_b128 v[172:175], v225 offset:41984\n"
" ds_read_b128 v[176:179], v225 offset:43008\n"
" ds_read_b128 v[180:183], v225 offset:44032\n"
" ds_read_b128 v[184:187], v225 offset:45056\n"
" ds_read_b128 v[188:191], v225 offset:46080\n"
" ds_read_b128 v[192:195], v225 offset:47104\n"
" ds_read_b128 v[196:199], v225 offset:48128\n"
" s_waitcnt lgkmcnt(6)\n"
" v_pk_add_f32 v[168:169], v[168:169], v[172:173]\n"
" v_pk_add_f32 v[170:171], v[170:171], v[174:175]\n"
" s_waitcnt lgkmcnt(4)\n"
" v_pk_add_f32 v[176:177], v[176:177], v[180:181]\n"
" v_pk_add_f32 v[178:179], v[178:179], v[182:183]\n"
" v_pk_add_f32 v[152:153], v[168:169], v[176:177]\n"
" v_pk_add_f32 v[154:155], v[170:171], v[178:179]\n"
" ds_read_b128 v[168:171], v225 offset:49152\n"
" ds_read_b128 v[172:175], v225 offset:50176\n"
" ds_read_b128 v[176:179], v225 offset:51200\n"
" ds_read_b128 v[180:183], v225 offset:52224\n"
" s_waitcnt lgkmcnt(6)\n"
" v_pk_add_f32 v[184:185], v[184:185], v[188:189]\n"
" v_pk_add_f32 v[186:187], v[186:187], v[190:191]\n"
" s_waitcnt lgkmcnt(4)\n"
" v_pk_add_f32 v[192:193], v[192:193], v[196:197]\n"
" v_pk_add_f32 v[194:195], v[194:195], v[198:199]\n"
" v_pk_add_f32 v[156:157], v[184:185], v[192:193]\n"
" v_pk_add_f32 v[158:159], v[186:187], v[194:195]\n"
" s_waitcnt lgkmcnt(2)\n"
" v_pk_add_f32 v[168:169], v[168:169], v[172:173]\n"
" v_pk_add_f32 v[170:171], v[170:171], v[174:175]\n"
" s_waitcnt lgkmcnt(0)\n"
" v_pk_add_f32 v[176:177], v[176:177], v[180:181]\n"
" v_pk_add_f32 v[178:179], v[178:179], v[182:183]\n"
" v_pk_add_f32 v[160:161], v[168:169], v[176:177]\n"
" v_pk_add_f32 v[162:163], v[170:171], v[178:179]\n"
" s_add_i32 s79, s95, s70\n"
" s_min_u32 s78, s68, 63\n"
" s_mov_b32 s91, 0\n"
" buffer_load_dword v235, v233, s[48:51], 0 offen sc1\n"
" s_waitcnt vmcnt(0)\n"
" s_barrier\n"
" v_mov_b32 v144, s78\n"
" buffer_store_dword v144, v228, s[48:51], 0 offen sc1\n"
".Lhd_hint_%=:\n"
" v_cmp_gt_i32 vcc, s79, v235\n"
" s_nop 0\n"
" s_cbranch_vccz .Lhd_hintok_%=\n"
" s_add_i32 s91, s91, 1\n"
" s_or_b32 s91, s91, s90\n"
" s_cmp_ge_u32 s91, 65536\n"
" s_cselect_b32 s90, 65536, 0\n"
" s_cbranch_scc1 .Lhd_hintok_%=\n"
" s_sleep 2\n"
" buffer_load_dword v235, v233, s[48:51], 0 offen sc1\n"
" s_waitcnt vmcnt(0)\n"
" s_branch .Lhd_hint_%=\n"
".Lhd_hintok_%=:\n"
" s_cmp_eq_u32 s91, 0\n"
" s_cbranch_scc1 .Lhd_nosl_%=\n"
" s_sleep 1\n"
".Lhd_nosl_%=:\n"
" s_and_b32 s79, s95, 2\n"
" s_lshl_b32 s79, s79, 11\n"
" s_and_b32 s94, s95, 15\n"
" s_lshl_b32 s94, s94, 19\n"
" s_add_i32 s94, s94, s85\n"
" s_lshr_b32 s76, s94, 21\n"
" s_bitcmp1_b64 s[86:87], s76\n"
" s_cselect_b32 s76, 8192, 0\n"
" s_add_i32 s94, s94, s76\n"
" buffer_load_dwordx4 v[144:147], v230, s[44:47], s79 offen sc1\n"
" buffer_load_dwordx2 v[148:149], v231, s[44:47], s79 offen offset:0 sc1\n"
" buffer_load_dwordx2 v[150:151], v231, s[44:47], s79 offen offset:256 sc1\n"
" s_mov_b32 s91, 0\n"
" v_mov_b32 v168, v234\n"
" v_mov_b32 v169, v234\n"
" v_mov_b32 v170, v234\n"
" v_mov_b32 v171, v234\n"
" v_mov_b32 v172, -1\n"
" v_mov_b32 v173, -1\n"
" v_mov_b32 v174, -1\n"
" v_mov_b32 v175, -1\n"
" s_waitcnt vmcnt(0)\n"
".Lhd_km_%=:\n"
" v_max3_u32 v235, v144, v145, v146\n"
" v_max3_u32 v235, v235, v147, v148\n"
" v_max3_u32 v235, v235, v149, v150\n"
" v_max_u32 v235, v235, v151\n"
" v_cmp_eq_u32 vcc, -1, v235\n"
" s_nop 0\n"
" s_cbranch_vccz .Lhd_kmok_%=\n"
" s_add_i32 s91, s91, 1\n"
" s_or_b32 s91, s91, s90\n"
" s_cmp_ge_u32 s91, 65536\n"
" s_cselect_b32 s90, 65536, 0\n"
" s_cbranch_scc1 .Lhd_kmok_%=\n"
" s_sleep 4\n"
" buffer_load_dwordx4 v[144:147], v230, s[44:47], s79 offen sc1\n"
" buffer_load_dwordx2 v[148:149], v231, s[44:47], s79 offen offset:0 sc1\n"
" buffer_load_dwordx2 v[150:151], v231, s[44:47], s79 offen offset:256 sc1\n"
" s_waitcnt vmcnt(0)\n"
" s_branch .Lhd_km_%=\n"
".Lhd_kmok_%=:\n"
" v_mfma_f32_16x16x32_bf16 v[164:167], v[144:147], v[244:247], v[168:171]\n"
" v_mfma_f32_16x16x32_bf16 v[252:255], v[144:147], v[248:251], v[168:171]\n"
" buffer_store_dwordx4 v[172:175], v230, s[44:47], s79 offen sc1\n"
" buffer_store_dwordx2 v[172:173], v231, s[44:47], s79 offen offset:0 sc1\n"
" buffer_store_dwordx2 v[172:173], v231, s[44:47], s79 offen offset:256 sc1\n"
" s_nop 3\n"
" ds_write_b128 v224, v[164:167] offset:53248\n"
" ds_write_b128 v224, v[252:255] offset:57344\n"
" s_waitcnt lgkmcnt(0)\n"
" s_barrier\n"
" ds_read_b128 v[168:171], v225 offset:53248\n"
" ds_read_b128 v[172:175], v225 offset:54272\n"
" ds_read_b128 v[176:179], v225 offset:55296\n"
" ds_read_b128 v[180:183], v225 offset:56320\n"
" ds_read_b128 v[184:187], v225 offset:57344\n"
" ds_read_b128 v[188:191], v225 offset:58368\n"
" ds_read_b128 v[192:195], v225 offset:59392\n"
" ds_read_b128 v[196:199], v225 offset:60416\n"
" s_waitcnt lgkmcnt(6)\n"
" v_pk_add_f32 v[168:169], v[168:169], v[172:173]\n"
" v_pk_add_f32 v[170:171], v[170:171], v[174:175]\n"
" s_waitcnt lgkmcnt(4)\n"
" v_pk_add_f32 v[176:177], v[176:177], v[180:181]\n"
" v_pk_add_f32 v[178:179], v[178:179], v[182:183]\n"
" v_pk_add_f32 v[164:165], v[168:169], v[176:177]\n"
" v_pk_add_f32 v[166:167], v[170:171], v[178:179]\n"
" s_waitcnt lgkmcnt(2)\n"
" v_pk_add_f32 v[184:185], v[184:185], v[188:189]\n"
" v_pk_add_f32 v[186:187], v[186:187], v[190:191]\n"
" s_waitcnt lgkmcnt(0)\n"
" v_pk_add_f32 v[192:193], v[192:193], v[196:197]\n"
" v_pk_add_f32 v[194:195], v[194:195], v[198:199]\n"
" v_pk_add_f32 v[252:253], v[184:185], v[192:193]\n"
" v_pk_add_f32 v[254:255], v[186:187], v[194:195]\n"
" v_max3_f32 v168, v152, v153, v154\n"
" v_max3_f32 v170, v156, v157, v158\n"
" v_max3_f32 v168, v168, v155, v164\n"
" v_max3_f32 v170, v170, v159, v160\n"
" v_max3_f32 v168, v168, v165, v166\n"
" v_max3_f32 v170, v170, v161, v162\n"
" v_max_f32 v168, v168, v167\n"
" v_max3_f32 v170, v170, v163, v252\n"
" v_max3_f32 v170, v170, v253, v254\n"
" v_max_f32 v170, v170, v255\n"
" v_mov_b32 v169, v168\n"
" v_mov_b32 v171, v170\n"
" s_nop 0\n"
" v_permlane16_swap_b32 v168, v169\n"
" v_permlane16_swap_b32 v170, v171\n"
" v_max_f32 v168, v168, v169\n"
" v_max_f32 v170, v170, v171\n"
" v_mov_b32 v169, v168\n"
" v_mov_b32 v171, v170\n"
" s_nop 0\n"
" v_permlane32_swap_b32 v168, v169\n"
" v_permlane32_swap_b32 v170, v171\n"
" v_max_f32 v168, v168, v169\n"
" v_max_f32 v170, v170, v171\n"
" v_sub_f32 v152, v152, v168\n"
" v_sub_f32 v153, v153, v168\n"
" v_sub_f32 v154, v154, v168\n"
" v_sub_f32 v155, v155, v168\n"
" v_exp_f32 v152, v152\n"
" v_exp_f32 v153, v153\n"
" v_exp_f32 v154, v154\n"
" v_exp_f32 v155, v155\n"
" v_sub_f32 v164, v164, v168\n"
" v_sub_f32 v165, v165, v168\n"
" v_sub_f32 v166, v166, v168\n"
" v_sub_f32 v167, v167, v168\n"
" v_exp_f32 v164, v164\n"
" v_exp_f32 v165, v165\n"
" v_exp_f32 v166, v166\n"
" v_exp_f32 v167, v167\n"
" v_sub_f32 v156, v156, v170\n"
" v_sub_f32 v157, v157, v170\n"
" v_sub_f32 v158, v158, v170\n"
" v_sub_f32 v159, v159, v170\n"
" v_exp_f32 v156, v156\n"
" v_exp_f32 v157, v157\n"
" v_exp_f32 v158, v158\n"
" v_exp_f32 v159, v159\n"
" v_sub_f32 v160, v160, v170\n"
" v_sub_f32 v161, v161, v170\n"
" v_sub_f32 v162, v162, v170\n"
" v_sub_f32 v163, v163, v170\n"
" v_exp_f32 v160, v160\n"
" v_exp_f32 v161, v161\n"
" v_exp_f32 v162, v162\n"
" v_exp_f32 v163, v163\n"
" v_sub_f32 v252, v252, v170\n"
" v_sub_f32 v253, v253, v170\n"
" v_sub_f32 v254, v254, v170\n"
" v_sub_f32 v255, v255, v170\n"
" v_exp_f32 v252, v252\n"
" v_exp_f32 v253, v253\n"
" v_exp_f32 v254, v254\n"
" v_exp_f32 v255, v255\n"
" v_cvt_pk_bf16_f32 v172, v152, v153\n"
" v_cvt_pk_bf16_f32 v173, v154, v155\n"
" v_cvt_pk_bf16_f32 v174, v164, v165\n"
" v_cvt_pk_bf16_f32 v175, v166, v167\n"
" v_cvt_pk_bf16_f32 v176, v156, v157\n"
" v_cvt_pk_bf16_f32 v177, v158, v159\n"
" v_cvt_pk_bf16_f32 v178, v160, v161\n"
" v_cvt_pk_bf16_f32 v179, v162, v163\n"
" v_cvt_pk_bf16_f32 v180, v252, v253\n"
" v_cvt_pk_bf16_f32 v181, v254, v255\n"
" v_mfma_f32_16x16x16_bf16 v[152:155], v[240:241], v[172:173], 0\n"
" v_mfma_f32_16x16x32_bf16 v[156:159], v[240:243], v[176:179], 0\n"
" v_mfma_f32_16x16x16_bf16 v[184:187], v[216:217], v[172:173], 0\n"
" v_mfma_f32_16x16x32_bf16 v[192:195], v[216:219], v[176:179], 0\n"
" v_mfma_f32_16x16x16_bf16 v[188:191], v[220:221], v[172:173], 0\n"
" v_mfma_f32_16x16x32_bf16 v[196:199], v[220:223], v[176:179], 0\n"
" v_mfma_f32_16x16x16_bf16 v[152:155], v[240:241], v[174:175], v[152:155]\n"
" v_mfma_f32_16x16x16_bf16 v[156:159], v[240:241], v[180:181], v[156:159]\n"
" v_mfma_f32_16x16x16_bf16 v[184:187], v[148:149], v[174:175], v[184:187]\n"
" v_mfma_f32_16x16x16_bf16 v[192:195], v[148:149], v[180:181], v[192:195]\n"
" v_mfma_f32_16x16x16_bf16 v[188:191], v[150:151], v[174:175], v[188:191]\n"
" v_mfma_f32_16x16x16_bf16 v[196:199], v[150:151], v[180:181], v[196:199]\n"
" s_nop 2\n"
" v_rcp_f32 v160, v152\n"
" v_rcp_f32 v162, v156\n"
" v_pk_mul_f32 v[184:185], v[184:185], v[160:161] op_sel_hi:[1,0]\n"
" v_pk_mul_f32 v[186:187], v[186:187], v[160:161] op_sel_hi:[1,0]\n"
" v_pk_mul_f32 v[188:189], v[188:189], v[160:161] op_sel_hi:[1,0]\n"
" v_pk_mul_f32 v[190:191], v[190:191], v[160:161] op_sel_hi:[1,0]\n"
" v_cvt_pk_bf16_f32 v216, v184, v185\n"
" v_cvt_pk_bf16_f32 v217, v186, v187\n"
" v_cvt_pk_bf16_f32 v218, v188, v189\n"
" v_cvt_pk_bf16_f32 v219, v190, v191\n"
" s_add_i32 s76, s94, 0\n"
" buffer_store_dwordx4 v[216:219], v224, s[36:39], s76 offen sc1\n"
" v_pk_mul_f32 v[192:193], v[192:193], v[162:163] op_sel_hi:[1,0]\n"
" v_pk_mul_f32 v[194:195], v[194:195], v[162:163] op_sel_hi:[1,0]\n"
" v_pk_mul_f32 v[196:197], v[196:197], v[162:163] op_sel_hi:[1,0]\n"
" v_pk_mul_f32 v[198:199], v[198:199], v[162:163] op_sel_hi:[1,0]\n"
" v_cvt_pk_bf16_f32 v220, v192, v193\n"
" v_cvt_pk_bf16_f32 v221, v194, v195\n"
" v_cvt_pk_bf16_f32 v222, v196, v197\n"
" v_cvt_pk_bf16_f32 v223, v198, v199\n"
" s_add_i32 s76, s94, 4096\n"
" buffer_store_dwordx4 v[220:223], v224, s[36:39], s76 offen sc1\n"
" s_cmp_ge_u32 s68, 64\n"
" s_cbranch_scc1 .Lhd_end_%=\n"
".Lhd_qph_%=:\n"
" ds_read_b128 v[144:147], v225 offset:0\n"
" ds_read_b128 v[148:151], v225 offset:20480\n"
" ds_read_b128 v[152:155], v225 offset:1024\n"
" ds_read_b128 v[156:159], v225 offset:21504\n"
" ds_read_b128 v[184:187], v232 offset:0\n"
" ds_read_b128 v[188:191], v232 offset:1024\n"
" s_waitcnt lgkmcnt(5)\n"
" v_mfma_f32_16x16x32_bf16 v[168:171], v[64:67], v[144:147], 0\n"
" ds_read_b128 v[192:195], v232 offset:2048\n"
" s_waitcnt lgkmcnt(5)\n"
" v_mfma_f32_16x16x32_bf16 v[176:179], v[64:67], v[148:151], 0\n"
" ds_read_b128 v[160:163], v225 offset:2048\n"
" s_waitcnt lgkmcnt(3)\n"
" v_mfma_f32_16x16x32_bf16 v[172:175], v[184:187], v[144:147], 0\n"
" ds_read_b128 v[164:167], v225 offset:22528\n"
" v_mfma_f32_16x16x32_bf16 v[180:183], v[184:187], v[148:151], 0\n"
" v_mfma_f32_16x16x32_bf16 v[168:171], v[68:71], v[152:155], v[168:171]\n"
" ds_read_b128 v[184:187], v232 offset:3072\n"
" v_mfma_f32_16x16x32_bf16 v[176:179], v[68:71], v[156:159], v[176:179]\n"
" ds_read_b128 v[144:147], v225 offset:3072\n"
" s_waitcnt lgkmcnt(5)\n"
" v_mfma_f32_16x16x32_bf16 v[172:175], v[188:191], v[152:155], v[172:175]\n"
" ds_read_b128 v[148:151], v225 offset:23552\n"
" v_mfma_f32_16x16x32_bf16 v[180:183], v[188:191], v[156:159], v[180:183]\n"
" s_waitcnt lgkmcnt(4)\n"
" v_mfma_f32_16x16x32_bf16 v[168:171], v[72:75], v[160:163], v[168:171]\n"
" ds_read_b128 v[188:191], v232 offset:4096\n"
" s_waitcnt lgkmcnt(4)\n"
" v_mfma_f32_16x16x32_bf16 v[176:179], v[72:75], v[164:167], v[176:179]\n"
" ds_read_b128 v[152:155], v225 offset:4096\n"
" v_mfma_f32_16x16x32_bf16 v[172:175], v[192:195], v[160:163], v[172:175]\n"
" ds_read_b128 v[156:159], v225 offset:24576\n"
" v_mfma_f32_16x16x32_bf16 v[180:183], v[192:195], v[164:167], v[180:183]\n"
" v_mov_b32 v216, v208\n"
" s_waitcnt lgkmcnt(4)\n"
" v_mfma_f32_16x16x32_bf16 v[168:171], v[76:79], v[144:147], v[168:171]\n"
" ds_read_b128 v[192:195], v232 offset:5120\n"
" s_waitcnt lgkmcnt(4)\n"
" v_mfma_f32_16x16x32_bf16 v[176:179], v[76:79], v[148:151], v[176:179]\n"
" ds_read_b128 v[160:163], v225 offset:5120\n"
" v_mfma_f32_16x16x32_bf16 v[172:175], v[184:187], v[144:147], v[172:175]\n"
" ds_read_b128 v[164:167], v225 offset:25600\n"
" v_mfma_f32_16x16x32_bf16 v[180:183], v[184:187], v[148:151], v[180:183]\n"
" v_mov_b32 v217, v209\n"
" s_waitcnt lgkmcnt(4)\n"
" v_mfma_f32_16x16x32_bf16 v[168:171], v[80:83], v[152:155], v[168:171]\n"
" ds_read_b128 v[184:187], v232 offset:6144\n"
" s_waitcnt lgkmcnt(4)\n"
" v_mfma_f32_16x16x32_bf16 v[176:179], v[80:83], v[156:159], v[176:179]\n"
" ds_read_b128 v[144:147], v225 offset:6144\n"
" v_mfma_f32_16x16x32_bf16 v[172:175], v[188:191], v[152:155], v[172:175]\n"
" ds_read_b128 v[148:151], v225 offset:26624\n"
" v_mfma_f32_16x16x32_bf16 v[180:183], v[188:191], v[156:159], v[180:183]\n"
" v_mov_b32 v218, v210\n"
" s_waitcnt lgkmcnt(4)\n"
" v_mfma_f32_16x16x32_bf16 v[168:171], v[84:87], v[160:163], v[168:171]\n"
" ds_read_b128 v[188:191], v232 offset:7168\n"
" s_waitcnt lgkmcnt(4)\n"
" v_mfma_f32_16x16x32_bf16 v[176:179], v[84:87], v[164:167], v[176:179]\n"
" ds_read_b128 v[152:155], v225 offset:7168\n"
" v_mfma_f32_16x16x32_bf16 v[172:175], v[192:195], v[160:163], v[172:175]\n"
" ds_read_b128 v[156:159], v225 offset:27648\n"
" v_mfma_f32_16x16x32_bf16 v[180:183], v[192:195], v[164:167], v[180:183]\n"
" v_mov_b32 v219, v211\n"
" s_waitcnt lgkmcnt(4)\n"
" v_mfma_f32_16x16x32_bf16 v[168:171], v[88:91], v[144:147], v[168:171]\n"
" ds_read_b128 v[192:195], v232 offset:8192\n"
" s_waitcnt lgkmcnt(4)\n"
" v_mfma_f32_16x16x32_bf16 v[176:179], v[88:91], v[148:151], v[176:179]\n"
" ds_read_b128 v[160:163], v225 offset:8192\n"
" v_mfma_f32_16x16x32_bf16 v[172:175], v[184:187], v[144:147], v[172:175]\n"
" ds_read_b128 v[164:167], v225 offset:28672\n"
" v_mfma_f32_16x16x32_bf16 v[180:183], v[184:187], v[148:151], v[180:183]\n"
" v_mov_b32 v220, v212\n"
" s_waitcnt lgkmcnt(4)\n"
" v_mfma_f32_16x16x32_bf16 v[168:171], v[92:95], v[152:155], v[168:171]\n"
" ds_read_b128 v[184:187], v232 offset:9216\n"
" s_waitcnt lgkmcnt(4)\n"
" v_mfma_f32_16x16x32_bf16 v[176:179], v[92:95], v[156:159], v[176:179]\n"
" ds_read_b128 v[144:147], v225 offset:9216\n"
" v_mfma_f32_16x16x32_bf16 v[172:175], v[188:191], v[152:155], v[172:175]\n"
" ds_read_b128 v[148:151], v225 offset:29696\n"
" v_mfma_f32_16x16x32_bf16 v[180:183], v[188:191], v[156:159], v[180:183]\n"
" v_mov_b32 v221, v213\n"
" s_waitcnt lgkmcnt(4)\n"
" v_mfma_f32_16x16x32_bf16 v[168:171], v[96:99], v[160:163], v[168:171]\n"
" ds_read_b128 v[188:191], v232 offset:10240\n"
" s_waitcnt lgkmcnt(4)\n"
" v_mfma_f32_16x16x32_bf16 v[176:179], v[96:99], v[164:167], v[176:179]\n"
" ds_read_b128 v[152:155], v225 offset:10240\n"
" v_mfma_f32_16x16x32_bf16 v[172:175], v[192:195], v[160:163], v[172:175]\n"
" ds_read_b128 v[156:159], v225 offset:30720\n"
" v_mfma_f32_16x16x32_bf16 v[180:183], v[192:195], v[164:167], v[180:183]\n"
" v_mov_b32 v222, v214\n"
" s_waitcnt lgkmcnt(4)\n"
" v_mfma_f32_16x16x32_bf16 v[168:171], v[100:103], v[144:147], v[168:171]\n"
" ds_read_b128 v[192:195], v232 offset:11264\n"
" s_waitcnt lgkmcnt(4)\n"
" v_mfma_f32_16x16x32_bf16 v[176:179], v[100:103], v[148:151], v[176:179]\n"
" ds_read_b128 v[160:163], v225 offset:11264\n"
" v_mfma_f32_16x16x32_bf16 v[172:175], v[184:187], v[144:147], v[172:175]\n"
" ds_read_b128 v[164:167], v225 offset:31744\n"
" v_mfma_f32_16x16x32_bf16 v[180:183], v[184:187], v[148:151], v[180:183]\n"
" v_mov_b32 v223, v215\n"
" s_waitcnt lgkmcnt(4)\n"
" v_mfma_f32_16x16x32_bf16 v[168:171], v[104:107], v[152:155], v[168:171]\n"
" ds_read_b128 v[184:187], v232 offset:12288\n"
" s_waitcnt lgkmcnt(4)\n"
" v_mfma_f32_16x16x32_bf16 v[176:179], v[104:107], v[156:159], v[176:179]\n"
" ds_read_b128 v[144:147], v225 offset:12288\n"
" v_mfma_f32_16x16x32_bf16 v[172:175], v[188:191], v[152:155], v[172:175]\n"
" ds_read_b128 v[148:151], v225 offset:32768\n"
" v_mfma_f32_16x16x32_bf16 v[180:183], v[188:191], v[156:159], v[180:183]\n"
" s_waitcnt lgkmcnt(4)\n"
" v_mfma_f32_16x16x32_bf16 v[168:171], v[108:111], v[160:163], v[168:171]\n"
" ds_read_b128 v[188:191], v232 offset:13312\n"
" s_waitcnt lgkmcnt(4)\n"
" v_mfma_f32_16x16x32_bf16 v[176:179], v[108:111], v[164:167], v[176:179]\n"
" ds_read_b128 v[152:155], v225 offset:13312\n"
" v_mfma_f32_16x16x32_bf16 v[172:175], v[192:195], v[160:163], v[172:175]\n"
" ds_read_b128 v[156:159], v225 offset:33792\n"
" v_mfma_f32_16x16x32_bf16 v[180:183], v[192:195], v[164:167], v[180:183]\n"
" s_waitcnt lgkmcnt(4)\n"
" v_mfma_f32_16x16x32_bf16 v[168:171], v[112:115], v[144:147], v[168:171]\n"
" ds_read_b128 v[192:195], v232 offset:14336\n"
" s_waitcnt lgkmcnt(4)\n"
" v_mfma_f32_16x16x32_bf16 v[176:179], v[112:115], v[148:151], v[176:179]\n"
" ds_read_b128 v[160:163], v225 offset:14336\n"
" v_mfma_f32_16x16x32_bf16 v[172:175], v[184:187], v[144:147], v[172:175]\n"
" ds_read_b128 v[164:167], v225 offset:34816\n"
" v_mfma_f32_16x16x32_bf16 v[180:183], v[184:187], v[148:151], v[180:183]\n"
" s_waitcnt vmcnt(0)\n"
" s_barrier\n"
" v_mov_b32 v235, s95\n"
" buffer_store_dword v235, v229, s[48:51], 0 offen sc1\n"
" v_mov_b32 v252, s68\n"
" buffer_store_dword v252, v228, s[48:51], 0 offen sc1\n"
" s_waitcnt lgkmcnt(4)\n"
" v_mfma_f32_16x16x32_bf16 v[168:171], v[116:119], v[152:155], v[168:171]\n"
" ds_read_b128 v[184:187], v232 offset:15360\n"
" s_waitcnt lgkmcnt(4)\n"
" v_mfma_f32_16x16x32_bf16 v[176:179], v[116:119], v[156:159], v[176:179]\n"
" ds_read_b128 v[144:147], v225 offset:15360\n"
" v_mfma_f32_16x16x32_bf16 v[172:175], v[188:191], v[152:155], v[172:175]\n"
" ds_read_b128 v[148:151], v225 offset:35840\n"
" v_mfma_f32_16x16x32_bf16 v[180:183], v[188:191], v[156:159], v[180:183]\n"
" s_waitcnt lgkmcnt(4)\n"
" v_mfma_f32_16x16x32_bf16 v[168:171], v[120:123], v[160:163], v[168:171]\n"
" ds_read_b128 v[188:191], v232 offset:16384\n"
" s_waitcnt lgkmcnt(4)\n"
" v_mfma_f32_16x16x32_bf16 v[176:179], v[120:123], v[164:167], v[176:179]\n"
" ds_read_b128 v[152:155], v225 offset:16384\n"
" v_mfma_f32_16x16x32_bf16 v[172:175], v[192:195], v[160:163], v[172:175]\n"
" ds_read_b128 v[156:159], v225 offset:36864\n"
" v_mfma_f32_16x16x32_bf16 v[180:183], v[192:195], v[164:167], v[180:183]\n"
" s_waitcnt lgkmcnt(4)\n"
" v_mfma_f32_16x16x32_bf16 v[168:171], v[124:127], v[144:147], v[168:171]\n"
" ds_read_b128 v[192:195], v232 offset:17408\n"
" s_waitcnt lgkmcnt(4)\n"
" v_mfma_f32_16x16x32_bf16 v[176:179], v[124:127], v[148:151], v[176:179]\n"
" ds_read_b128 v[160:163], v225 offset:17408\n"
" v_mfma_f32_16x16x32_bf16 v[172:175], v[184:187], v[144:147], v[172:175]\n"
" ds_read_b128 v[164:167], v225 offset:37888\n"
" v_mfma_f32_16x16x32_bf16 v[180:183], v[184:187], v[148:151], v[180:183]\n"
" s_waitcnt lgkmcnt(4)\n"
" v_mfma_f32_16x16x32_bf16 v[168:171], v[128:131], v[152:155], v[168:171]\n"
" ds_read_b128 v[184:187], v232 offset:18432\n"
" s_waitcnt lgkmcnt(4)\n"
" v_mfma_f32_16x16x32_bf16 v[176:179], v[128:131], v[156:159], v[176:179]\n"
" ds_read_b128 v[144:147], v225 offset:18432\n"
" v_mfma_f32_16x16x32_bf16 v[172:175], v[188:191], v[152:155], v[172:175]\n"
" ds_read_b128 v[148:151], v225 offset:38912\n"
" v_mfma_f32_16x16x32_bf16 v[180:183], v[188:191], v[156:159], v[180:183]\n"
" s_waitcnt lgkmcnt(4)\n"
" v_mfma_f32_16x16x32_bf16 v[168:171], v[132:135], v[160:163], v[168:171]\n"
" ds_read_b128 v[188:191], v232 offset:19456\n"
" s_waitcnt lgkmcnt(4)\n"
" v_mfma_f32_16x16x32_bf16 v[176:179], v[132:135], v[164:167], v[176:179]\n"
" ds_read_b128 v[152:155], v225 offset:19456\n"
" v_mfma_f32_16x16x32_bf16 v[172:175], v[192:195], v[160:163], v[172:175]\n"
" ds_read_b128 v[156:159], v225 offset:39936\n"
" v_mfma_f32_16x16x32_bf16 v[180:183], v[192:195], v[164:167], v[180:183]\n"
" s_waitcnt lgkmcnt(4)\n"
" v_mfma_f32_16x16x32_bf16 v[168:171], v[136:139], v[144:147], v[168:171]\n"
" s_waitcnt lgkmcnt(3)\n"
" v_mfma_f32_16x16x32_bf16 v[176:179], v[136:139], v[148:151], v[176:179]\n"
" v_mfma_f32_16x16x32_bf16 v[172:175], v[184:187], v[144:147], v[172:175]\n"
" v_mfma_f32_16x16x32_bf16 v[180:183], v[184:187], v[148:151], v[180:183]\n"
" s_waitcnt lgkmcnt(1)\n"
" v_mfma_f32_16x16x32_bf16 v[168:171], v[140:143], v[152:155], v[168:171]\n"
" s_waitcnt lgkmcnt(0)\n"
" v_mfma_f32_16x16x32_bf16 v[176:179], v[140:143], v[156:159], v[176:179]\n"
" v_mfma_f32_16x16x32_bf16 v[172:175], v[188:191], v[152:155], v[172:175]\n"
" v_mfma_f32_16x16x32_bf16 v[180:183], v[188:191], v[156:159], v[180:183]\n"
" s_nop 3\n"
" v_pk_mul_f32 v[168:169], v[168:169], s[98:99]\n"
" v_pk_mul_f32 v[170:171], v[170:171], s[98:99]\n"
" s_nop 0\n"
" v_pk_mul_f32 v[172:173], v[172:173], s[98:99]\n"
" v_pk_mul_f32 v[174:175], v[174:175], s[98:99]\n"
" v_cvt_pk_bf16_f32 v244, v168, v169\n"
" v_cvt_pk_bf16_f32 v245, v170, v171\n"
" v_cvt_pk_bf16_f32 v246, v172, v173\n"
" v_cvt_pk_bf16_f32 v247, v174, v175\n"
" v_pk_mul_f32 v[176:177], v[176:177], s[98:99]\n"
" v_pk_mul_f32 v[178:179], v[178:179], s[98:99]\n"
" v_pk_mul_f32 v[180:181], v[180:181], s[98:99]\n"
" v_pk_mul_f32 v[182:183], v[182:183], s[98:99]\n"
" v_cvt_pk_bf16_f32 v248, v176, v177\n"
" v_cvt_pk_bf16_f32 v249, v178, v179\n"
" v_cvt_pk_bf16_f32 v250, v180, v181\n"
" v_cvt_pk_bf16_f32 v251, v182, v183\n"
" v_mfma_f32_16x16x32_bf16 v[152:155], v[200:203], v[244:247], v[236:239]\n"
" s_nop 0\n"
" v_mfma_f32_16x16x32_bf16 v[156:159], v[200:203], v[248:251], 0\n"
" v_mfma_f32_16x16x32_bf16 v[160:163], v[204:207], v[248:251], v[236:239]\n"
" s_nop 4\n"
" ds_write_b128 v224, v[152:155] offset:40960\n"
" s_nop 0\n"
" ds_write_b128 v224, v[156:159] offset:45056\n"
" ds_write_b128 v224, v[160:163] offset:49152\n"
" s_waitcnt lgkmcnt(0)\n"
" s_mov_b32 s95, s68\n"
" s_add_i32 s68, s68, 2\n"
" s_branch .Lhd_loop_%=\n"
".Lhd_end_%=:\n"
" s_waitcnt vmcnt(0)\n"
" s_barrier\n"
" v_mov_b32 v235, s95\n"
" buffer_store_dword v235, v229, s[48:51], 0 offen sc1\n"
" s_waitcnt vmcnt(0)\n"
      : "+{v[224:239]}"(c0), "+{v[240:255]}"(c1), "+{s68}"(seg), "+{s[48:51]}"(sP)
      , "+{s[40:43]}"(sO), "+{s[44:47]}"(sA)
      : "{s[36:39]}"(sI), "{s71}"(uni(60 + (h >> 3))), "{s[52:55]}"(s0), "{s[56:59]}"(s1), "{s[60:63]}"(s2), "{s84}"(uni((int)OFF_XN + l * 64 * 8 * CELL)), "{s85}"(uni((int)OFF_O + (l * RS * 32 + h) * CELL)), "{s69}"(w), "{s70}"(__builtin_amdgcn_readfirstlane(l * LAG)), "{s73}"(uni(63 - 32 * l)), "{s[16:17]}"(p.tag), "{s[96:97]}"(dbgp(p, w))
      : "memory", "vcc", "scc", "m0", V_LOW, A_ALL, S_BULK, "s72");
  __builtin_amdgcn_endpgm();
}
__device__ __forceinline__ void wo_role(const Params& p, int w, int id) {
  const int lane = lane_id(), tid = w * 64 + lane;
  const int l = id >> 4, gr = (id >> 2) & 3, j = id & 3, g = lane >> 4, r = lane & 15;
  u8* ws = p.ws;
  i32x16 c0, c1;
#pragma unroll
  for (int i = 0; i < 16; ++i) { c0[i] = 0; c1[i] = 0; }
  const int t0 = w == 0 ? 0 : 2 * w + 2, t2 = 2 + (w & 1), ma = w >> 1;
  const int ob = (96 + (r >> 2) * 4 + gr) * CELL + CO + g * 64 + (r & 3) * 16, om = 16 * CELL;
  c0[11] = pol_word(lane);
  c0[0] = tid * 16; c0[1] = lane * 16 + ma * 4096; c0[7] = lane * 16 + (1 - ma) * 4096;
  c0[2] = pg(PG_O + j * 128 + (gr * 8 + (lane & 7)) * 4 + l * 2); c0[3] = lane < 4 ? pg(PG_WO + lane * 32 + id) : OOB;
  c0[4] = ob + ma * om + (5 * j + (t0 >> 1)) * 256; c0[5] = ob + ma * om + (5 * j + 1) * 256 + (w & 1) * 8; c0[8] = ob + (1 - ma) * om + (5 * j + (t0 >> 1)) * 256;
#pragma unroll
  for (int i = 0; i < 5; ++i) { const int q = (w * 5 + i) * 1024 + lane * 16; c1[i] = (q / 320) * 1280 + q % 320; }
  const int tb = (4 * g + ((lane >> 2) & 3)) * 320 + (lane & 3) * 8;
  c1[5] = tb + t0 * 32; c1[6] = tb + t0 * 32 + 32; c1[7] = tb + t2 * 32;
  v4u sI = rawrsrc(ws, WSB), sO = rawrsrc(ws, WSB);
  v4u sP = rawrsrc(ws + OFF_PROG, PGB);
  const v4u s0 = rawrsrc(p.Wo + (size_t)gr * 1024 * 1280 + j * 320, 1024 * 1280 - j * 320);
  asm volatile(
" s_nop 4\n"
" s_mov_b32 s90, 0\n"
" s_mul_i32 s77, s69, 5120\n"
" s_add_i32 m0, s77, 0\n"
" s_mov_b32 s76, 0\n"
" buffer_load_dwordx4 v240, s[52:55], s76 offen lds\n"
" s_add_i32 m0, s77, 1024\n"
" s_mov_b32 s76, 0\n"
" buffer_load_dwordx4 v241, s[52:55], s76 offen lds\n"
" s_add_i32 m0, s77, 2048\n"
" s_mov_b32 s76, 0\n"
" buffer_load_dwordx4 v242, s[52:55], s76 offen lds\n"
" s_add_i32 m0, s77, 3072\n"
" s_mov_b32 s76, 0\n"
" buffer_load_dwordx4 v243, s[52:55], s76 offen lds\n"
" s_add_i32 m0, s77, 4096\n"
" s_mov_b32 s76, 0\n"
" buffer_load_dwordx4 v244, s[52:55], s76 offen lds\n"
" s_add_i32 m0, s77, 20480\n"
" s_mov_b32 s76, 81920\n"
" buffer_load_dwordx4 v240, s[52:55], s76 offen lds\n"
" s_add_i32 m0, s77, 21504\n"
" s_mov_b32 s76, 81920\n"
" buffer_load_dwordx4 v241, s[52:55], s76 offen lds\n"
" s_add_i32 m0, s77, 22528\n"
" s_mov_b32 s76, 81920\n"
" buffer_load_dwordx4 v242, s[52:55], s76 offen lds\n"
" s_add_i32 m0, s77, 23552\n"
" s_mov_b32 s76, 81920\n"
" buffer_load_dwordx4 v243, s[52:55], s76 offen lds\n"
" s_add_i32 m0, s77, 24576\n"
" s_mov_b32 s76, 81920\n"
" buffer_load_dwordx4 v244, s[52:55], s76 offen lds\n"
" s_add_i32 m0, s77, 40960\n"
" s_mov_b32 s76, 163840\n"
" buffer_load_dwordx4 v240, s[52:55], s76 offen lds\n"
" s_add_i32 m0, s77, 41984\n"
" s_mov_b32 s76, 163840\n"
" buffer_load_dwordx4 v241, s[52:55], s76 offen lds\n"
" s_add_i32 m0, s77, 43008\n"
" s_mov_b32 s76, 163840\n"
" buffer_load_dwordx4 v242, s[52:55], s76 offen lds\n"
" s_add_i32 m0, s77, 44032\n"
" s_mov_b32 s76, 163840\n"
" buffer_load_dwordx4 v243, s[52:55], s76 offen lds\n"
" s_add_i32 m0, s77, 45056\n"
" s_mov_b32 s76, 163840\n"
" buffer_load_dwordx4 v244, s[52:55], s76 offen lds\n"
" s_add_i32 m0, s77, 61440\n"
" s_mov_b32 s76, 245760\n"
" buffer_load_dwordx4 v240, s[52:55], s76 offen lds\n"
" s_add_i32 m0, s77, 62464\n"
" s_mov_b32 s76, 245760\n"
" buffer_load_dwordx4 v241, s[52:55], s76 offen lds\n"
" s_add_i32 m0, s77, 63488\n"
" s_mov_b32 s76, 245760\n"
" buffer_load_dwordx4 v242, s[52:55], s76 offen lds\n"
" s_add_i32 m0, s77, 64512\n"
" s_mov_b32 s76, 245760\n"
" buffer_load_dwordx4 v243, s[52:55], s76 offen lds\n"
" s_add_i32 m0, s77, 65536\n"
" s_mov_b32 s76, 245760\n"
" buffer_load_dwordx4 v244, s[52:55], s76 offen lds\n"
" s_add_i32 m0, s77, 81920\n"
" s_mov_b32 s76, 327680\n"
" buffer_load_dwordx4 v240, s[52:55], s76 offen lds\n"
" s_add_i32 m0, s77, 82944\n"
" s_mov_b32 s76, 327680\n"
" buffer_load_dwordx4 v241, s[52:55], s76 offen lds\n"
" s_add_i32 m0, s77, 83968\n"
" s_mov_b32 s76, 327680\n"
" buffer_load_dwordx4 v242, s[52:55], s76 offen lds\n"
" s_add_i32 m0, s77, 84992\n"
" s_mov_b32 s76, 327680\n"
" buffer_load_dwordx4 v243, s[52:55], s76 offen lds\n"
" s_add_i32 m0, s77, 86016\n"
" s_mov_b32 s76, 327680\n"
" buffer_load_dwordx4 v244, s[52:55], s76 offen lds\n"
" s_add_i32 m0, s77, 102400\n"
" s_mov_b32 s76, 409600\n"
" buffer_load_dwordx4 v240, s[52:55], s76 offen lds\n"
" s_add_i32 m0, s77, 103424\n"
" s_mov_b32 s76, 409600\n"
" buffer_load_dwordx4 v241, s[52:55], s76 offen lds\n"
" s_add_i32 m0, s77, 104448\n"
" s_mov_b32 s76, 409600\n"
" buffer_load_dwordx4 v242, s[52:55], s76 offen lds\n"
" s_add_i32 m0, s77, 105472\n"
" s_mov_b32 s76, 409600\n"
" buffer_load_dwordx4 v243, s[52:55], s76 offen lds\n"
" s_add_i32 m0, s77, 106496\n"
" s_mov_b32 s76, 409600\n"
" buffer_load_dwordx4 v244, s[52:55], s76 offen lds\n"
" s_add_i32 m0, s77, 122880\n"
" s_mov_b32 s76, 491520\n"
" buffer_load_dwordx4 v240, s[52:55], s76 offen lds\n"
" s_add_i32 m0, s77, 123904\n"
" s_mov_b32 s76, 491520\n"
" buffer_load_dwordx4 v241, s[52:55], s76 offen lds\n"
" s_add_i32 m0, s77, 124928\n"
" s_mov_b32 s76, 491520\n"
" buffer_load_dwordx4 v242, s[52:55], s76 offen lds\n"
" s_add_i32 m0, s77, 125952\n"
" s_mov_b32 s76, 491520\n"
" buffer_load_dwordx4 v243, s[52:55], s76 offen lds\n"
" s_add_i32 m0, s77, 126976\n"
" s_mov_b32 s76, 491520\n"
" buffer_load_dwordx4 v244, s[52:55], s76 offen lds\n"
" s_add_i32 m0, s77, 143360\n"
" s_mov_b32 s76, 573440\n"
" buffer_load_dwordx4 v240, s[52:55], s76 offen lds\n"
" s_add_i32 m0, s77, 144384\n"
" s_mov_b32 s76, 573440\n"
" buffer_load_dwordx4 v241, s[52:55], s76 offen lds\n"
" s_add_i32 m0, s77, 145408\n"
" s_mov_b32 s76, 573440\n"
" buffer_load_dwordx4 v242, s[52:55], s76 offen lds\n"
" s_add_i32 m0, s77, 146432\n"
" s_mov_b32 s76, 573440\n"
" buffer_load_dwordx4 v243, s[52:55], s76 offen lds\n"
" s_add_i32 m0, s77, 147456\n"
" s_mov_b32 s76, 573440\n"
" buffer_load_dwordx4 v244, s[52:55], s76 offen lds\n"
" s_waitcnt lgkmcnt(0)\n"
" s_waitcnt vmcnt(35)\n"
" s_barrier\n"
" ds_read_b64_tr_b16 a[0:1], v245 offset:0\n"
" ds_read_b64_tr_b16 a[2:3], v245 offset:5120\n"
" ds_read_b64_tr_b16 a[4:5], v246 offset:0\n"
" ds_read_b64_tr_b16 a[6:7], v246 offset:5120\n"
" ds_read_b64_tr_b16 a[8:9], v247 offset:0\n"
" ds_read_b64_tr_b16 a[10:11], v247 offset:5120\n"
" ds_read_b64_tr_b16 a[12:13], v245 offset:10240\n"
" ds_read_b64_tr_b16 a[14:15], v245 offset:15360\n"
" ds_read_b64_tr_b16 a[16:17], v246 offset:10240\n"
" ds_read_b64_tr_b16 a[18:19], v246 offset:15360\n"
" ds_read_b64_tr_b16 a[20:21], v247 offset:10240\n"
" ds_read_b64_tr_b16 a[22:23], v247 offset:15360\n"
" s_waitcnt lgkmcnt(0)\n"
" s_waitcnt vmcnt(30)\n"
" s_barrier\n"
" s_add_i32 m0, s77, 0\n"
" s_mov_b32 s76, 655360\n"
" buffer_load_dwordx4 v240, s[52:55], s76 offen lds\n"
" s_add_i32 m0, s77, 1024\n"
" s_mov_b32 s76, 655360\n"
" buffer_load_dwordx4 v241, s[52:55], s76 offen lds\n"
" s_add_i32 m0, s77, 2048\n"
" s_mov_b32 s76, 655360\n"
" buffer_load_dwordx4 v242, s[52:55], s76 offen lds\n"
" s_add_i32 m0, s77, 3072\n"
" s_mov_b32 s76, 655360\n"
" buffer_load_dwordx4 v243, s[52:55], s76 offen lds\n"
" s_add_i32 m0, s77, 4096\n"
" s_mov_b32 s76, 655360\n"
" buffer_load_dwordx4 v244, s[52:55], s76 offen lds\n"
" ds_read_b64_tr_b16 a[24:25], v245 offset:20480\n"
" ds_read_b64_tr_b16 a[26:27], v245 offset:25600\n"
" ds_read_b64_tr_b16 a[28:29], v246 offset:20480\n"
" ds_read_b64_tr_b16 a[30:31], v246 offset:25600\n"
" ds_read_b64_tr_b16 a[32:33], v247 offset:20480\n"
" ds_read_b64_tr_b16 a[34:35], v247 offset:25600\n"
" ds_read_b64_tr_b16 a[36:37], v245 offset:30720\n"
" ds_read_b64_tr_b16 a[38:39], v245 offset:35840\n"
" ds_read_b64_tr_b16 a[40:41], v246 offset:30720\n"
" ds_read_b64_tr_b16 a[42:43], v246 offset:35840\n"
" ds_read_b64_tr_b16 a[44:45], v247 offset:30720\n"
" ds_read_b64_tr_b16 a[46:47], v247 offset:35840\n"
" s_waitcnt lgkmcnt(0)\n"
" s_waitcnt vmcnt(30)\n"
" s_barrier\n"
" s_add_i32 m0, s77, 20480\n"
" s_mov_b32 s76, 737280\n"
" buffer_load_dwordx4 v240, s[52:55], s76 offen lds\n"
" s_add_i32 m0, s77, 21504\n"
" s_mov_b32 s76, 737280\n"
" buffer_load_dwordx4 v241, s[52:55], s76 offen lds\n"
" s_add_i32 m0, s77, 22528\n"
" s_mov_b32 s76, 737280\n"
" buffer_load_dwordx4 v242, s[52:55], s76 offen lds\n"
" s_add_i32 m0, s77, 23552\n"
" s_mov_b32 s76, 737280\n"
" buffer_load_dwordx4 v243, s[52:55], s76 offen lds\n"
" s_add_i32 m0, s77, 24576\n"
" s_mov_b32 s76, 737280\n"
" buffer_load_dwordx4 v244, s[52:55], s76 offen lds\n"
" ds_read_b64_tr_b16 a[48:49], v245 offset:40960\n"
" ds_read_b64_tr_b16 a[50:51], v245 offset:46080\n"
" ds_read_b64_tr_b16 a[52:53], v246 offset:40960\n"
" ds_read_b64_tr_b16 a[54:55], v246 offset:46080\n"
" ds_read_b64_tr_b16 a[56:57], v247 offset:40960\n"
" ds_read_b64_tr_b16 a[58:59], v247 offset:46080\n"
" ds_read_b64_tr_b16 a[60:61], v245 offset:51200\n"
" ds_read_b64_tr_b16 a[62:63], v245 offset:56320\n"
" ds_read_b64_tr_b16 a[64:65], v246 offset:51200\n"
" ds_read_b64_tr_b16 a[66:67], v246 offset:56320\n"
" ds_read_b64_tr_b16 a[68:69], v247 offset:51200\n"
" ds_read_b64_tr_b16 a[70:71], v247 offset:56320\n"
" s_waitcnt lgkmcnt(0)\n"
" s_waitcnt vmcnt(30)\n"
" s_barrier\n"
" s_add_i32 m0, s77, 40960\n"
" s_mov_b32 s76, 819200\n"
" buffer_load_dwordx4 v240, s[52:55], s76 offen lds\n"
" s_add_i32 m0, s77, 41984\n"
" s_mov_b32 s76, 819200\n"
" buffer_load_dwordx4 v241, s[52:55], s76 offen lds\n"
" s_add_i32 m0, s77, 43008\n"
" s_mov_b32 s76, 819200\n"
" buffer_load_dwordx4 v242, s[52:55], s76 offen lds\n"
" s_add_i32 m0, s77, 44032\n"
" s_mov_b32 s76, 819200\n"
" buffer_load_dwordx4 v243, s[52:55], s76 offen lds\n"
" s_add_i32 m0, s77, 45056\n"
" s_mov_b32 s76, 819200\n"
" buffer_load_dwordx4 v244, s[52:55], s76 offen lds\n"
" v_add_u32 v216, 61440, v245\n"
" v_add_u32 v217, 61440, v246\n"
" v_add_u32 v218, 61440, v247\n"
" ds_read_b64_tr_b16 a[72:73], v216 offset:0\n"
" ds_read_b64_tr_b16 a[74:75], v216 offset:5120\n"
" ds_read_b64_tr_b16 a[76:77], v217 offset:0\n"
" ds_read_b64_tr_b16 a[78:79], v217 offset:5120\n"
" ds_read_b64_tr_b16 a[80:81], v218 offset:0\n"
" ds_read_b64_tr_b16 a[82:83], v218 offset:5120\n"
" ds_read_b64_tr_b16 a[84:85], v216 offset:10240\n"
" ds_read_b64_tr_b16 a[86:87], v216 offset:15360\n"
" ds_read_b64_tr_b16 a[88:89], v217 offset:10240\n"
" ds_read_b64_tr_b16 a[90:91], v217 offset:15360\n"
" ds_read_b64_tr_b16 a[92:93], v218 offset:10240\n"
" ds_read_b64_tr_b16 a[94:95], v218 offset:15360\n"
" s_waitcnt lgkmcnt(0)\n"
" s_waitcnt vmcnt(30)\n"
" s_barrier\n"
" s_add_i32 m0, s77, 61440\n"
" s_mov_b32 s76, 901120\n"
" buffer_load_dwordx4 v240, s[52:55], s76 offen lds\n"
" s_add_i32 m0, s77, 62464\n"
" s_mov_b32 s76, 901120\n"
" buffer_load_dwordx4 v241, s[52:55], s76 offen lds\n"
" s_add_i32 m0, s77, 63488\n"
" s_mov_b32 s76, 901120\n"
" buffer_load_dwordx4 v242, s[52:55], s76 offen lds\n"
" s_add_i32 m0, s77, 64512\n"
" s_mov_b32 s76, 901120\n"
" buffer_load_dwordx4 v243, s[52:55], s76 offen lds\n"
" s_add_i32 m0, s77, 65536\n"
" s_mov_b32 s76, 901120\n"
" buffer_load_dwordx4 v244, s[52:55], s76 offen lds\n"
" v_add_u32 v216, 81920, v245\n"
" v_add_u32 v217, 81920, v246\n"
" v_add_u32 v218, 81920, v247\n"
" ds_read_b64_tr_b16 a[96:97], v216 offset:0\n"
" ds_read_b64_tr_b16 a[98:99], v216 offset:5120\n"
" ds_read_b64_tr_b16 a[100:101], v217 offset:0\n"
" ds_read_b64_tr_b16 a[102:103], v217 offset:5120\n"
" ds_read_b64_tr_b16 a[104:105], v218 offset:0\n"
" ds_read_b64_tr_b16 a[106:107], v218 offset:5120\n"
" ds_read_b64_tr_b16 a[108:109], v216 offset:10240\n"
" ds_read_b64_tr_b16 a[110:111], v216 offset:15360\n"
" ds_read_b64_tr_b16 a[112:113], v217 offset:10240\n"
" ds_read_b64_tr_b16 a[114:115], v217 offset:15360\n"
" ds_read_b64_tr_b16 a[116:117], v218 offset:10240\n"
" ds_read_b64_tr_b16 a[118:119], v218 offset:15360\n"
" s_waitcnt lgkmcnt(0)\n"
" s_waitcnt vmcnt(30)\n"
" s_barrier\n"
" s_add_i32 m0, s77, 81920\n"
" s_mov_b32 s76, 983040\n"
" buffer_load_dwordx4 v240, s[52:55], s76 offen lds\n"
" s_add_i32 m0, s77, 82944\n"
" s_mov_b32 s76, 983040\n"
" buffer_load_dwordx4 v241, s[52:55], s76 offen lds\n"
" s_add_i32 m0, s77, 83968\n"
" s_mov_b32 s76, 983040\n"
" buffer_load_dwordx4 v242, s[52:55], s76 offen lds\n"
" s_add_i32 m0, s77, 84992\n"
" s_mov_b32 s76, 983040\n"
" buffer_load_dwordx4 v243, s[52:55], s76 offen lds\n"
" s_add_i32 m0, s77, 86016\n"
" s_mov_b32 s76, 983040\n"
" buffer_load_dwordx4 v244, s[52:55], s76 offen lds\n"
" v_add_u32 v216, 102400, v245\n"
" v_add_u32 v217, 102400, v246\n"
" v_add_u32 v218, 102400, v247\n"
" ds_read_b64_tr_b16 a[120:121], v216 offset:0\n"
" ds_read_b64_tr_b16 a[122:123], v216 offset:5120\n"
" ds_read_b64_tr_b16 a[124:125], v217 offset:0\n"
" ds_read_b64_tr_b16 a[126:127], v217 offset:5120\n"
" ds_read_b64_tr_b16 a[128:129], v218 offset:0\n"
" ds_read_b64_tr_b16 a[130:131], v218 offset:5120\n"
" ds_read_b64_tr_b16 a[132:133], v216 offset:10240\n"
" ds_read_b64_tr_b16 a[134:135], v216 offset:15360\n"
" ds_read_b64_tr_b16 a[136:137], v217 offset:10240\n"
" ds_read_b64_tr_b16 a[138:139], v217 offset:15360\n"
" ds_read_b64_tr_b16 a[140:141], v218 offset:10240\n"
" ds_read_b64_tr_b16 a[142:143], v218 offset:15360\n"
" s_waitcnt lgkmcnt(0)\n"
" s_waitcnt vmcnt(30)\n"
" s_barrier\n"
" s_add_i32 m0, s77, 102400\n"
" s_mov_b32 s76, 1064960\n"
" buffer_load_dwordx4 v240, s[52:55], s76 offen lds\n"
" s_add_i32 m0, s77, 103424\n"
" s_mov_b32 s76, 1064960\n"
" buffer_load_dwordx4 v241, s[52:55], s76 offen lds\n"
" s_add_i32 m0, s77, 104448\n"
" s_mov_b32 s76, 1064960\n"
" buffer_load_dwordx4 v242, s[52:55], s76 offen lds\n"
" s_add_i32 m0, s77, 105472\n"
" s_mov_b32 s76, 1064960\n"
" buffer_load_dwordx4 v243, s[52:55], s76 offen lds\n"
" s_add_i32 m0, s77, 106496\n"
" s_mov_b32 s76, 1064960\n"
" buffer_load_dwordx4 v244, s[52:55], s76 offen lds\n"
" v_add_u32 v216, 122880, v245\n"
" v_add_u32 v217, 122880, v246\n"
" v_add_u32 v218, 122880, v247\n"
" ds_read_b64_tr_b16 a[144:145], v216 offset:0\n"
" ds_read_b64_tr_b16 a[146:147], v216 offset:5120\n"
" ds_read_b64_tr_b16 a[148:149], v217 offset:0\n"
" ds_read_b64_tr_b16 a[150:151], v217 offset:5120\n"
" ds_read_b64_tr_b16 a[152:153], v218 offset:0\n"
" ds_read_b64_tr_b16 a[154:155], v218 offset:5120\n"
" ds_read_b64_tr_b16 a[156:157], v216 offset:10240\n"
" ds_read_b64_tr_b16 a[158:159], v216 offset:15360\n"
" ds_read_b64_tr_b16 a[160:161], v217 offset:10240\n"
" ds_read_b64_tr_b16 a[162:163], v217 offset:15360\n"
" ds_read_b64_tr_b16 a[164:165], v218 offset:10240\n"
" ds_read_b64_tr_b16 a[166:167], v218 offset:15360\n"
" s_waitcnt lgkmcnt(0)\n"
" s_waitcnt vmcnt(30)\n"
" s_barrier\n"
" s_add_i32 m0, s77, 122880\n"
" s_mov_b32 s76, 1146880\n"
" buffer_load_dwordx4 v240, s[52:55], s76 offen lds\n"
" s_add_i32 m0, s77, 123904\n"
" s_mov_b32 s76, 1146880\n"
" buffer_load_dwordx4 v241, s[52:55], s76 offen lds\n"
" s_add_i32 m0, s77, 124928\n"
" s_mov_b32 s76, 1146880\n"
" buffer_load_dwordx4 v242, s[52:55], s76 offen lds\n"
" s_add_i32 m0, s77, 125952\n"
" s_mov_b32 s76, 1146880\n"
" buffer_load_dwordx4 v243, s[52:55], s76 offen lds\n"
" s_add_i32 m0, s77, 126976\n"
" s_mov_b32 s76, 1146880\n"
" buffer_load_dwordx4 v244, s[52:55], s76 offen lds\n"
" v_add_u32 v216, 143360, v245\n"
" v_add_u32 v217, 143360, v246\n"
" v_add_u32 v218, 143360, v247\n"
" ds_read_b64_tr_b16 a[168:169], v216 offset:0\n"
" ds_read_b64_tr_b16 a[170:171], v216 offset:5120\n"
" ds_read_b64_tr_b16 a[172:173], v217 offset:0\n"
" ds_read_b64_tr_b16 a[174:175], v217 offset:5120\n"
" ds_read_b64_tr_b16 a[176:177], v218 offset:0\n"
" ds_read_b64_tr_b16 a[178:179], v218 offset:5120\n"
" ds_read_b64_tr_b16 a[180:181], v216 offset:10240\n"
" ds_read_b64_tr_b16 a[182:183], v216 offset:15360\n"
" ds_read_b64_tr_b16 a[184:185], v217 offset:10240\n"
" ds_read_b64_tr_b16 a[186:187], v217 offset:15360\n"
" ds_read_b64_tr_b16 a[188:189], v218 offset:10240\n"
" ds_read_b64_tr_b16 a[190:191], v218 offset:15360\n"
" s_waitcnt lgkmcnt(0)\n"
" s_waitcnt vmcnt(30)\n"
" s_barrier\n"
" s_add_i32 m0, s77, 143360\n"
" s_mov_b32 s76, 1228800\n"
" buffer_load_dwordx4 v240, s[52:55], s76 offen lds\n"
" s_add_i32 m0, s77, 144384\n"
" s_mov_b32 s76, 1228800\n"
" buffer_load_dwordx4 v241, s[52:55], s76 offen lds\n"
" s_add_i32 m0, s77, 145408\n"
" s_mov_b32 s76, 1228800\n"
" buffer_load_dwordx4 v242, s[52:55], s76 offen lds\n"
" s_add_i32 m0, s77, 146432\n"
" s_mov_b32 s76, 1228800\n"
" buffer_load_dwordx4 v243, s[52:55], s76 offen lds\n"
" s_add_i32 m0, s77, 147456\n"
" s_mov_b32 s76, 1228800\n"
" buffer_load_dwordx4 v244, s[52:55], s76 offen lds\n"
" ds_read_b64_tr_b16 a[192:193], v245 offset:0\n"
" ds_read_b64_tr_b16 a[194:195], v245 offset:5120\n"
" ds_read_b64_tr_b16 a[196:197], v246 offset:0\n"
" ds_read_b64_tr_b16 a[198:199], v246 offset:5120\n"
" ds_read_b64_tr_b16 a[200:201], v247 offset:0\n"
" ds_read_b64_tr_b16 a[202:203], v247 offset:5120\n"
" ds_read_b64_tr_b16 a[204:205], v245 offset:10240\n"
" ds_read_b64_tr_b16 a[206:207], v245 offset:15360\n"
" ds_read_b64_tr_b16 a[208:209], v246 offset:10240\n"
" ds_read_b64_tr_b16 a[210:211], v246 offset:15360\n"
" ds_read_b64_tr_b16 a[212:213], v247 offset:10240\n"
" ds_read_b64_tr_b16 a[214:215], v247 offset:15360\n"
" s_waitcnt lgkmcnt(0)\n"
" s_waitcnt vmcnt(30)\n"
" s_barrier\n"
" ds_read_b64_tr_b16 a[216:217], v245 offset:20480\n"
" ds_read_b64_tr_b16 a[218:219], v245 offset:25600\n"
" ds_read_b64_tr_b16 a[220:221], v246 offset:20480\n"
" ds_read_b64_tr_b16 a[222:223], v246 offset:25600\n"
" ds_read_b64_tr_b16 a[224:225], v247 offset:20480\n"
" ds_read_b64_tr_b16 a[226:227], v247 offset:25600\n"
" ds_read_b64_tr_b16 a[228:229], v245 offset:30720\n"
" ds_read_b64_tr_b16 a[230:231], v245 offset:35840\n"
" ds_read_b64_tr_b16 a[232:233], v246 offset:30720\n"
" ds_read_b64_tr_b16 a[234:235], v246 offset:35840\n"
" ds_read_b64_tr_b16 a[236:237], v247 offset:30720\n"
" ds_read_b64_tr_b16 a[238:239], v247 offset:35840\n"
" s_waitcnt lgkmcnt(0)\n"
" s_waitcnt vmcnt(25)\n"
" s_barrier\n"
" ds_read_b64_tr_b16 a[240:241], v245 offset:40960\n"
" ds_read_b64_tr_b16 a[242:243], v245 offset:46080\n"
" ds_read_b64_tr_b16 a[244:245], v246 offset:40960\n"
" ds_read_b64_tr_b16 a[246:247], v246 offset:46080\n"
" ds_read_b64_tr_b16 a[248:249], v247 offset:40960\n"
" ds_read_b64_tr_b16 a[250:251], v247 offset:46080\n"
" ds_read_b64_tr_b16 a[252:253], v245 offset:51200\n"
" ds_read_b64_tr_b16 a[254:255], v245 offset:56320\n"
" ds_read_b64_tr_b16 v[0:1], v246 offset:51200\n"
" ds_read_b64_tr_b16 v[2:3], v246 offset:56320\n"
" ds_read_b64_tr_b16 v[4:5], v247 offset:51200\n"
" ds_read_b64_tr_b16 v[6:7], v247 offset:56320\n"
" s_waitcnt lgkmcnt(0)\n"
" s_waitcnt vmcnt(20)\n"
" s_barrier\n"
" v_add_u32 v216, 61440, v245\n"
" v_add_u32 v217, 61440, v246\n"
" v_add_u32 v218, 61440, v247\n"
" ds_read_b64_tr_b16 v[8:9], v216 offset:0\n"
" ds_read_b64_tr_b16 v[10:11], v216 offset:5120\n"
" ds_read_b64_tr_b16 v[12:13], v217 offset:0\n"
" ds_read_b64_tr_b16 v[14:15], v217 offset:5120\n"
" ds_read_b64_tr_b16 v[16:17], v218 offset:0\n"
" ds_read_b64_tr_b16 v[18:19], v218 offset:5120\n"
" ds_read_b64_tr_b16 v[20:21], v216 offset:10240\n"
" ds_read_b64_tr_b16 v[22:23], v216 offset:15360\n"
" ds_read_b64_tr_b16 v[24:25], v217 offset:10240\n"
" ds_read_b64_tr_b16 v[26:27], v217 offset:15360\n"
" ds_read_b64_tr_b16 v[28:29], v218 offset:10240\n"
" ds_read_b64_tr_b16 v[30:31], v218 offset:15360\n"
" s_waitcnt lgkmcnt(0)\n"
" s_waitcnt vmcnt(15)\n"
" s_barrier\n"
" v_add_u32 v216, 81920, v245\n"
" v_add_u32 v217, 81920, v246\n"
" v_add_u32 v218, 81920, v247\n"
" ds_read_b64_tr_b16 v[32:33], v216 offset:0\n"
" ds_read_b64_tr_b16 v[34:35], v216 offset:5120\n"
" ds_read_b64_tr_b16 v[36:37], v217 offset:0\n"
" ds_read_b64_tr_b16 v[38:39], v217 offset:5120\n"
" ds_read_b64_tr_b16 v[40:41], v218 offset:0\n"
" ds_read_b64_tr_b16 v[42:43], v218 offset:5120\n"
" ds_read_b64_tr_b16 v[44:45], v216 offset:10240\n"
" ds_read_b64_tr_b16 v[46:47], v216 offset:15360\n"
" ds_read_b64_tr_b16 v[48:49], v217 offset:10240\n"
" ds_read_b64_tr_b16 v[50:51], v217 offset:15360\n"
" ds_read_b64_tr_b16 v[52:53], v218 offset:10240\n"
" ds_read_b64_tr_b16 v[54:55], v218 offset:15360\n"
" s_waitcnt lgkmcnt(0)\n"
" s_waitcnt vmcnt(10)\n"
" s_barrier\n"
" v_add_u32 v216, 102400, v245\n"
" v_add_u32 v217, 102400, v246\n"
" v_add_u32 v218, 102400, v247\n"
" ds_read_b64_tr_b16 v[56:57], v216 offset:0\n"
" ds_read_b64_tr_b16 v[58:59], v216 offset:5120\n"
" ds_read_b64_tr_b16 v[60:61], v217 offset:0\n"
" ds_read_b64_tr_b16 v[62:63], v217 offset:5120\n"
" ds_read_b64_tr_b16 v[64:65], v218 offset:0\n"
" ds_read_b64_tr_b16 v[66:67], v218 offset:5120\n"
" ds_read_b64_tr_b16 v[68:69], v216 offset:10240\n"
" ds_read_b64_tr_b16 v[70:71], v216 offset:15360\n"
" ds_read_b64_tr_b16 v[72:73], v217 offset:10240\n"
" ds_read_b64_tr_b16 v[74:75], v217 offset:15360\n"
" ds_read_b64_tr_b16 v[76:77], v218 offset:10240\n"
" ds_read_b64_tr_b16 v[78:79], v218 offset:15360\n"
" s_waitcnt lgkmcnt(0)\n"
" s_waitcnt vmcnt(5)\n"
" s_barrier\n"
" v_add_u32 v216, 122880, v245\n"
" v_add_u32 v217, 122880, v246\n"
" v_add_u32 v218, 122880, v247\n"
" ds_read_b64_tr_b16 v[80:81], v216 offset:0\n"
" ds_read_b64_tr_b16 v[82:83], v216 offset:5120\n"
" ds_read_b64_tr_b16 v[84:85], v217 offset:0\n"
" ds_read_b64_tr_b16 v[86:87], v217 offset:5120\n"
" ds_read_b64_tr_b16 v[88:89], v218 offset:0\n"
" ds_read_b64_tr_b16 v[90:91], v218 offset:5120\n"
" ds_read_b64_tr_b16 v[92:93], v216 offset:10240\n"
" ds_read_b64_tr_b16 v[94:95], v216 offset:15360\n"
" ds_read_b64_tr_b16 v[96:97], v217 offset:10240\n"
" ds_read_b64_tr_b16 v[98:99], v217 offset:15360\n"
" ds_read_b64_tr_b16 v[100:101], v218 offset:10240\n"
" ds_read_b64_tr_b16 v[102:103], v218 offset:15360\n"
" s_waitcnt lgkmcnt(0)\n"
" s_waitcnt vmcnt(0)\n"
" s_barrier\n"
" v_add_u32 v216, 143360, v245\n"
" v_add_u32 v217, 143360, v246\n"
" v_add_u32 v218, 143360, v247\n"
" ds_read_b64_tr_b16 v[104:105], v216 offset:0\n"
" ds_read_b64_tr_b16 v[106:107], v216 offset:5120\n"
" ds_read_b64_tr_b16 v[108:109], v217 offset:0\n"
" ds_read_b64_tr_b16 v[110:111], v217 offset:5120\n"
" ds_read_b64_tr_b16 v[112:113], v218 offset:0\n"
" ds_read_b64_tr_b16 v[114:115], v218 offset:5120\n"
" ds_read_b64_tr_b16 v[116:117], v216 offset:10240\n"
" ds_read_b64_tr_b16 v[118:119], v216 offset:15360\n"
" ds_read_b64_tr_b16 v[120:121], v217 offset:10240\n"
" ds_read_b64_tr_b16 v[122:123], v217 offset:15360\n"
" ds_read_b64_tr_b16 v[124:125], v218 offset:10240\n"
" ds_read_b64_tr_b16 v[126:127], v218 offset:15360\n"
" s_waitcnt lgkmcnt(0)\n"
" s_waitcnt vmcnt(0)\n"
" s_barrier\n"
" s_lshl_b32 s77, s69, 10\n"
" v_mov_b32 v230, 0x20000\n"
" v_mov_b32 v216, 0\n"
" ds_write_b32 v230, v216\n"
" s_waitcnt lgkmcnt(0)\n"
" s_barrier\n"
" s_mov_b32 s95, -1\n"
" s_mov_b32 s68, 0\n"
" s_mov_b32 s94, 0\n"
" s_mov_b32 s91, 0\n"
".Lwo_pol_%=:\n"
" buffer_load_dwordx2 v[220:221], v235, s[48:51], 0 offen sc1\n"
" s_waitcnt vmcnt(0)\n"
" v_or_b32 v222, 1, v220\n"
" v_mov_b32 v223, v221\n"
" v_cmp_ne_u64 vcc, s[16:17], v[222:223]\n"
" s_nop 0\n"
" s_cbranch_vccz .Lwo_polok_%=\n"
" s_add_i32 s91, s91, 1\n"
" s_or_b32 s91, s91, s90\n"
" s_cmp_ge_u32 s91, 65536\n"
" s_cselect_b32 s90, 65536, 0\n"
" s_cbranch_scc1 .Lwo_polok_%=\n"
" s_sleep 4\n"
" s_branch .Lwo_pol_%=\n"
".Lwo_polok_%=:\n"
" v_and_b32 v220, 1, v220\n"
" v_cmp_ne_u32_e64 s[86:87], 1, v220\n"
" s_nop 1\n"
" s_bitcmp1_b32 s86, 0\n"
" s_cselect_b32 s76, 8192, 0\n"
" s_add_u32 s48, s48, s76\n"
".Lwo_loop_%=:\n"
" s_cmp_lg_u32 s94, 0\n"
" s_cbranch_scc1 .Lwo_have_%=\n"
" s_mov_b32 s91, 0\n"
" buffer_load_dword v235, v226, s[48:51], 0 offen sc1\n"
" s_waitcnt vmcnt(0)\n"
" v_mov_b32 v216, 1\n"
" s_mov_b64 exec, 1\n"
" ds_add_rtn_u32 v217, v230, v216\n"
" s_mov_b64 exec, -1\n"
" s_waitcnt lgkmcnt(0)\n"
" v_readfirstlane_b32 s76, v217\n"
" s_nop 0\n"
" s_and_b32 s76, s76, 3\n"
" s_cmp_lg_u32 s76, 3\n"
" s_cbranch_scc1 .Lwo_lf0_%=\n"
" v_mov_b32 v216, s95\n"
" buffer_store_dword v216, v227, s[48:51], 0 offen sc1\n"
".Lwo_lf0_%=:\n"
" s_nop 3\n"
".Lwo_in_%=:\n"
" v_cmp_gt_i32 vcc, s68, v235\n"
" s_nop 0\n"
" s_cbranch_vccz .Lwo_inok_%=\n"
" s_add_i32 s91, s91, 1\n"
" s_or_b32 s91, s91, s90\n"
" s_cmp_ge_u32 s91, 65536\n"
" s_cselect_b32 s90, 65536, 0\n"
" s_cbranch_scc1 .Lwo_inok_%=\n"
" s_sleep 2\n"
" buffer_load_dword v235, v226, s[48:51], 0 offen sc1\n"
" s_waitcnt vmcnt(0)\n"
" s_branch .Lwo_in_%=\n"
".Lwo_inok_%=:\n"
" s_mov_b32 s38, 134217728\n"
" s_and_b32 s92, s68, 15\n"
" s_lshl_b32 s92, s92, 19\n"
" s_add_i32 s92, s92, s84\n"
" s_lshr_b32 s76, s92, 21\n"
" s_bitcmp1_b64 s[86:87], s76\n"
" s_cselect_b32 s76, 8192, 0\n"
" s_add_i32 s92, s92, s76\n"
" s_add_i32 m0, s77, 0\n"
" s_add_i32 s76, s92, 0\n"
" buffer_load_dwordx4 v224, s[36:39], s76 offen sc1 lds\n"
" s_add_i32 m0, s77, 4096\n"
" s_add_i32 s76, s92, 4096\n"
" buffer_load_dwordx4 v224, s[36:39], s76 offen sc1 lds\n"
" s_add_i32 m0, s77, 8192\n"
" s_add_i32 s76, s92, 16384\n"
" buffer_load_dwordx4 v224, s[36:39], s76 offen sc1 lds\n"
" s_add_i32 m0, s77, 12288\n"
" s_add_i32 s76, s92, 20480\n"
" buffer_load_dwordx4 v224, s[36:39], s76 offen sc1 lds\n"
" s_add_i32 m0, s77, 16384\n"
" s_add_i32 s76, s92, 32768\n"
" buffer_load_dwordx4 v224, s[36:39], s76 offen sc1 lds\n"
" s_add_i32 m0, s77, 20480\n"
" s_add_i32 s76, s92, 36864\n"
" buffer_load_dwordx4 v224, s[36:39], s76 offen sc1 lds\n"
" s_add_i32 m0, s77, 24576\n"
" s_add_i32 s76, s92, 49152\n"
" buffer_load_dwordx4 v224, s[36:39], s76 offen sc1 lds\n"
" s_add_i32 m0, s77, 28672\n"
" s_add_i32 s76, s92, 53248\n"
" buffer_load_dwordx4 v224, s[36:39], s76 offen sc1 lds\n"
" s_add_i32 m0, s77, 32768\n"
" s_add_i32 s76, s92, 65536\n"
" buffer_load_dwordx4 v224, s[36:39], s76 offen sc1 lds\n"
" s_add_i32 m0, s77, 36864\n"
" s_add_i32 s76, s92, 69632\n"
" buffer_load_dwordx4 v224, s[36:39], s76 offen sc1 lds\n"
" s_add_i32 m0, s77, 40960\n"
" s_add_i32 s76, s92, 81920\n"
" buffer_load_dwordx4 v224, s[36:39], s76 offen sc1 lds\n"
" s_add_i32 m0, s77, 45056\n"
" s_add_i32 s76, s92, 86016\n"
" buffer_load_dwordx4 v224, s[36:39], s76 offen sc1 lds\n"
" s_add_i32 m0, s77, 49152\n"
" s_add_i32 s76, s92, 98304\n"
" buffer_load_dwordx4 v224, s[36:39], s76 offen sc1 lds\n"
" s_add_i32 m0, s77, 53248\n"
" s_add_i32 s76, s92, 102400\n"
" buffer_load_dwordx4 v224, s[36:39], s76 offen sc1 lds\n"
" s_add_i32 m0, s77, 57344\n"
" s_add_i32 s76, s92, 114688\n"
" buffer_load_dwordx4 v224, s[36:39], s76 offen sc1 lds\n"
" s_add_i32 m0, s77, 61440\n"
" s_add_i32 s76, s92, 118784\n"
" buffer_load_dwordx4 v224, s[36:39], s76 offen sc1 lds\n"
".Lwo_have_%=:\n"
" s_waitcnt vmcnt(14)\n"
".Lwo_go_%=:\n"
" s_xor_b32 s77, s77, 0x10000\n"
" v_xor_b32 v226, 0x80, v226\n"
" s_and_b32 s93, s68, 15\n"
" s_lshl_b32 s93, s93, 21\n"
" s_add_i32 s93, s93, s85\n"
" s_lshr_b32 s76, s93, 21\n"
" s_bitcmp1_b64 s[86:87], s76\n"
" s_cselect_b32 s76, 8192, 0\n"
" s_add_i32 s93, s93, s76\n"
" s_add_i32 s78, s68, 1\n"
" s_and_b32 s92, s78, 15\n"
" s_lshl_b32 s92, s92, 19\n"
" s_add_i32 s92, s92, s84\n"
" s_lshr_b32 s76, s92, 21\n"
" s_bitcmp1_b64 s[86:87], s76\n"
" s_cselect_b32 s76, 8192, 0\n"
" s_add_i32 s92, s92, s76\n"
" s_barrier\n"
" ds_read_b128 v[128:131], v225 offset:0\n"
" ds_read_b128 v[132:135], v231 offset:0\n"
" ds_read_b128 v[136:139], v225 offset:1024\n"
" ds_read_b128 v[140:143], v231 offset:1024\n"
" s_waitcnt lgkmcnt(3)\n"
" v_mfma_f32_16x16x32_bf16 v[152:155], a[0:3], v[128:131], 0\n"
" ds_read_b128 v[144:147], v225 offset:2048\n"
" v_mfma_f32_16x16x32_bf16 v[156:159], a[4:7], v[128:131], 0\n"
" v_mfma_f32_16x16x32_bf16 v[160:163], a[8:11], v[128:131], 0\n"
" s_waitcnt lgkmcnt(3)\n"
" v_mfma_f32_16x16x32_bf16 v[164:167], a[0:3], v[132:135], 0\n"
" ds_read_b128 v[148:151], v231 offset:2048\n"
" v_mfma_f32_16x16x32_bf16 v[168:171], a[4:7], v[132:135], 0\n"
" s_waitcnt lgkmcnt(3)\n"
" v_mfma_f32_16x16x32_bf16 v[152:155], a[12:15], v[136:139], v[152:155]\n"
" s_waitcnt vmcnt(10)\n"
" v_mfma_f32_16x16x32_bf16 v[156:159], a[16:19], v[136:139], v[156:159]\n"
" s_barrier\n"
" v_mfma_f32_16x16x32_bf16 v[160:163], a[20:23], v[136:139], v[160:163]\n"
" ds_read_b128 v[128:131], v225 offset:3072\n"
" s_waitcnt lgkmcnt(3)\n"
" v_mfma_f32_16x16x32_bf16 v[164:167], a[12:15], v[140:143], v[164:167]\n"
" ds_read_b128 v[132:135], v231 offset:3072\n"
" v_mfma_f32_16x16x32_bf16 v[168:171], a[16:19], v[140:143], v[168:171]\n"
" s_waitcnt lgkmcnt(3)\n"
" v_mfma_f32_16x16x32_bf16 v[152:155], a[24:27], v[144:147], v[152:155]\n"
" ds_read_b128 v[136:139], v225 offset:8192\n"
" v_mfma_f32_16x16x32_bf16 v[156:159], a[28:31], v[144:147], v[156:159]\n"
" v_mfma_f32_16x16x32_bf16 v[160:163], a[32:35], v[144:147], v[160:163]\n"
" s_waitcnt lgkmcnt(3)\n"
" v_mfma_f32_16x16x32_bf16 v[164:167], a[24:27], v[148:151], v[164:167]\n"
" ds_read_b128 v[140:143], v231 offset:8192\n"
" v_mfma_f32_16x16x32_bf16 v[168:171], a[28:31], v[148:151], v[168:171]\n"
" s_waitcnt lgkmcnt(3)\n"
" v_mfma_f32_16x16x32_bf16 v[152:155], a[36:39], v[128:131], v[152:155]\n"
" ds_read_b128 v[144:147], v225 offset:9216\n"
" v_mfma_f32_16x16x32_bf16 v[156:159], a[40:43], v[128:131], v[156:159]\n"
" v_mfma_f32_16x16x32_bf16 v[160:163], a[44:47], v[128:131], v[160:163]\n"
" s_waitcnt lgkmcnt(3)\n"
" v_mfma_f32_16x16x32_bf16 v[164:167], a[36:39], v[132:135], v[164:167]\n"
" ds_read_b128 v[148:151], v231 offset:9216\n"
" v_mfma_f32_16x16x32_bf16 v[168:171], a[40:43], v[132:135], v[168:171]\n"
" s_waitcnt lgkmcnt(3)\n"
" v_mfma_f32_16x16x32_bf16 v[152:155], a[48:51], v[136:139], v[152:155]\n"
" ds_read_b128 v[128:131], v225 offset:10240\n"
" v_mfma_f32_16x16x32_bf16 v[156:159], a[52:55], v[136:139], v[156:159]\n"
" v_mfma_f32_16x16x32_bf16 v[160:163], a[56:59], v[136:139], v[160:163]\n"
" s_waitcnt lgkmcnt(3)\n"
" v_mfma_f32_16x16x32_bf16 v[164:167], a[48:51], v[140:143], v[164:167]\n"
" ds_read_b128 v[132:135], v231 offset:10240\n"
" v_mfma_f32_16x16x32_bf16 v[168:171], a[52:55], v[140:143], v[168:171]\n"
" s_waitcnt lgkmcnt(3)\n"
" v_mfma_f32_16x16x32_bf16 v[152:155], a[60:63], v[144:147], v[152:155]\n"
" ds_read_b128 v[136:139], v225 offset:11264\n"
" v_mfma_f32_16x16x32_bf16 v[156:159], a[64:67], v[144:147], v[156:159]\n"
" v_mfma_f32_16x16x32_bf16 v[160:163], a[68:71], v[144:147], v[160:163]\n"
" s_waitcnt lgkmcnt(3)\n"
" v_mfma_f32_16x16x32_bf16 v[164:167], a[60:63], v[148:151], v[164:167]\n"
" ds_read_b128 v[140:143], v231 offset:11264\n"
" v_mfma_f32_16x16x32_bf16 v[168:171], a[64:67], v[148:151], v[168:171]\n"
" s_waitcnt lgkmcnt(3)\n"
" v_mfma_f32_16x16x32_bf16 v[152:155], a[72:75], v[128:131], v[152:155]\n"
" ds_read_b128 v[144:147], v225 offset:16384\n"
" v_mfma_f32_16x16x32_bf16 v[156:159], a[76:79], v[128:131], v[156:159]\n"
" v_mfma_f32_16x16x32_bf16 v[160:163], a[80:83], v[128:131], v[160:163]\n"
" s_waitcnt lgkmcnt(3)\n"
" v_mfma_f32_16x16x32_bf16 v[164:167], a[72:75], v[132:135], v[164:167]\n"
" ds_read_b128 v[148:151], v231 offset:16384\n"
" v_mfma_f32_16x16x32_bf16 v[168:171], a[76:79], v[132:135], v[168:171]\n"
" s_waitcnt lgkmcnt(3)\n"
" v_mfma_f32_16x16x32_bf16 v[152:155], a[84:87], v[136:139], v[152:155]\n"
" s_waitcnt vmcnt(4)\n"
" v_mfma_f32_16x16x32_bf16 v[156:159], a[88:91], v[136:139], v[156:159]\n"
" s_barrier\n"
" v_mfma_f32_16x16x32_bf16 v[160:163], a[92:95], v[136:139], v[160:163]\n"
" ds_read_b128 v[128:131], v225 offset:17408\n"
" s_waitcnt lgkmcnt(3)\n"
" v_mfma_f32_16x16x32_bf16 v[164:167], a[84:87], v[140:143], v[164:167]\n"
" ds_read_b128 v[132:135], v231 offset:17408\n"
" v_mfma_f32_16x16x32_bf16 v[168:171], a[88:91], v[140:143], v[168:171]\n"
" s_waitcnt lgkmcnt(3)\n"
" v_mfma_f32_16x16x32_bf16 v[152:155], a[96:99], v[144:147], v[152:155]\n"
" ds_read_b128 v[136:139], v225 offset:18432\n"
" v_mfma_f32_16x16x32_bf16 v[156:159], a[100:103], v[144:147], v[156:159]\n"
" v_mfma_f32_16x16x32_bf16 v[160:163], a[104:107], v[144:147], v[160:163]\n"
" s_waitcnt lgkmcnt(3)\n"
" v_mfma_f32_16x16x32_bf16 v[164:167], a[96:99], v[148:151], v[164:167]\n"
" ds_read_b128 v[140:143], v231 offset:18432\n"
" v_mfma_f32_16x16x32_bf16 v[168:171], a[100:103], v[148:151], v[168:171]\n"
" s_waitcnt lgkmcnt(3)\n"
" v_mfma_f32_16x16x32_bf16 v[152:155], a[108:111], v[128:131], v[152:155]\n"
" ds_read_b128 v[144:147], v225 offset:19456\n"
" v_mfma_f32_16x16x32_bf16 v[156:159], a[112:115], v[128:131], v[156:159]\n"
" v_mfma_f32_16x16x32_bf16 v[160:163], a[116:119], v[128:131], v[160:163]\n"
" s_waitcnt lgkmcnt(3)\n"
" v_mfma_f32_16x16x32_bf16 v[164:167], a[108:111], v[132:135], v[164:167]\n"
" ds_read_b128 v[148:151], v231 offset:19456\n"
" v_mfma_f32_16x16x32_bf16 v[168:171], a[112:115], v[132:135], v[168:171]\n"
" s_waitcnt lgkmcnt(3)\n"
" v_mfma_f32_16x16x32_bf16 v[152:155], a[120:123], v[136:139], v[152:155]\n"
" ds_read_b128 v[128:131], v225 offset:24576\n"
" v_mfma_f32_16x16x32_bf16 v[156:159], a[124:127], v[136:139], v[156:159]\n"
" v_mfma_f32_16x16x32_bf16 v[160:163], a[128:131], v[136:139], v[160:163]\n"
" s_waitcnt lgkmcnt(3)\n"
" v_mfma_f32_16x16x32_bf16 v[164:167], a[120:123], v[140:143], v[164:167]\n"
" ds_read_b128 v[132:135], v231 offset:24576\n"
" v_mfma_f32_16x16x32_bf16 v[168:171], a[124:127], v[140:143], v[168:171]\n"
" s_waitcnt lgkmcnt(3)\n"
" v_mfma_f32_16x16x32_bf16 v[152:155], a[132:135], v[144:147], v[152:155]\n"
" ds_read_b128 v[136:139], v225 offset:25600\n"
" v_mfma_f32_16x16x32_bf16 v[156:159], a[136:139], v[144:147], v[156:159]\n"
" v_mfma_f32_16x16x32_bf16 v[160:163], a[140:143], v[144:147], v[160:163]\n"
" s_waitcnt lgkmcnt(3)\n"
" v_mfma_f32_16x16x32_bf16 v[164:167], a[132:135], v[148:151], v[164:167]\n"
" ds_read_b128 v[140:143], v231 offset:25600\n"
" v_mfma_f32_16x16x32_bf16 v[168:171], a[136:139], v[148:151], v[168:171]\n"
" s_waitcnt lgkmcnt(3)\n"
" v_mfma_f32_16x16x32_bf16 v[152:155], a[144:147], v[128:131], v[152:155]\n"
" ds_read_b128 v[144:147], v225 offset:26624\n"
" v_mfma_f32_16x16x32_bf16 v[156:159], a[148:151], v[128:131], v[156:159]\n"
" v_mfma_f32_16x16x32_bf16 v[160:163], a[152:155], v[128:131], v[160:163]\n"
" s_waitcnt lgkmcnt(3)\n"
" v_mfma_f32_16x16x32_bf16 v[164:167], a[144:147], v[132:135], v[164:167]\n"
" ds_read_b128 v[148:151], v231 offset:26624\n"
" v_mfma_f32_16x16x32_bf16 v[168:171], a[148:151], v[132:135], v[168:171]\n"
" s_waitcnt lgkmcnt(3)\n"
" v_mfma_f32_16x16x32_bf16 v[152:155], a[156:159], v[136:139], v[152:155]\n"
" s_waitcnt vmcnt(0)\n"
" v_mfma_f32_16x16x32_bf16 v[156:159], a[160:163], v[136:139], v[156:159]\n"
" s_barrier\n"
" v_mfma_f32_16x16x32_bf16 v[160:163], a[164:167], v[136:139], v[160:163]\n"
" ds_read_b128 v[128:131], v225 offset:27648\n"
" s_waitcnt lgkmcnt(3)\n"
" v_mfma_f32_16x16x32_bf16 v[164:167], a[156:159], v[140:143], v[164:167]\n"
" ds_read_b128 v[132:135], v231 offset:27648\n"
" buffer_load_dword v235, v226, s[48:51], 0 offen sc1\n"
" s_cmp_lg_u32 s94, 1\n"
" s_cbranch_scc1 .Lwo_nof_%=\n"
" v_mov_b32 v216, 1\n"
" s_mov_b64 exec, 1\n"
" ds_add_rtn_u32 v217, v230, v216\n"
" s_mov_b64 exec, -1\n"
" s_waitcnt lgkmcnt(0)\n"
" v_readfirstlane_b32 s76, v217\n"
" s_nop 0\n"
" s_and_b32 s76, s76, 3\n"
" s_cmp_lg_u32 s76, 3\n"
" s_cbranch_scc1 .Lwo_lf1_%=\n"
" v_mov_b32 v216, s95\n"
" buffer_store_dword v216, v227, s[48:51], 0 offen sc1\n"
".Lwo_lf1_%=:\n"
" s_nop 3\n"
".Lwo_nof_%=:\n"
" v_mfma_f32_16x16x32_bf16 v[168:171], a[160:163], v[140:143], v[168:171]\n"
" s_waitcnt lgkmcnt(3)\n"
" v_mfma_f32_16x16x32_bf16 v[152:155], a[168:171], v[144:147], v[152:155]\n"
" ds_read_b128 v[136:139], v225 offset:32768\n"
" v_mfma_f32_16x16x32_bf16 v[156:159], a[172:175], v[144:147], v[156:159]\n"
" v_mfma_f32_16x16x32_bf16 v[160:163], a[176:179], v[144:147], v[160:163]\n"
" s_waitcnt lgkmcnt(3)\n"
" v_mfma_f32_16x16x32_bf16 v[164:167], a[168:171], v[148:151], v[164:167]\n"
" ds_read_b128 v[140:143], v231 offset:32768\n"
" v_mfma_f32_16x16x32_bf16 v[168:171], a[172:175], v[148:151], v[168:171]\n"
" s_waitcnt lgkmcnt(3)\n"
" v_mfma_f32_16x16x32_bf16 v[152:155], a[180:183], v[128:131], v[152:155]\n"
" ds_read_b128 v[144:147], v225 offset:33792\n"
" v_mfma_f32_16x16x32_bf16 v[156:159], a[184:187], v[128:131], v[156:159]\n"
" v_mfma_f32_16x16x32_bf16 v[160:163], a[188:191], v[128:131], v[160:163]\n"
" s_waitcnt lgkmcnt(3)\n"
" v_mfma_f32_16x16x32_bf16 v[164:167], a[180:183], v[132:135], v[164:167]\n"
" ds_read_b128 v[148:151], v231 offset:33792\n"
" v_mfma_f32_16x16x32_bf16 v[168:171], a[184:187], v[132:135], v[168:171]\n"
" s_waitcnt lgkmcnt(3)\n"
" v_mfma_f32_16x16x32_bf16 v[152:155], a[192:195], v[136:139], v[152:155]\n"
" ds_read_b128 v[128:131], v225 offset:34816\n"
" v_mfma_f32_16x16x32_bf16 v[156:159], a[196:199], v[136:139], v[156:159]\n"
" v_mfma_f32_16x16x32_bf16 v[160:163], a[200:203], v[136:139], v[160:163]\n"
" s_waitcnt lgkmcnt(3)\n"
" v_mfma_f32_16x16x32_bf16 v[164:167], a[192:195], v[140:143], v[164:167]\n"
" ds_read_b128 v[132:135], v231 offset:34816\n"
" v_mfma_f32_16x16x32_bf16 v[168:171], a[196:199], v[140:143], v[168:171]\n"
" s_waitcnt lgkmcnt(3)\n"
" v_mfma_f32_16x16x32_bf16 v[152:155], a[204:207], v[144:147], v[152:155]\n"
" ds_read_b128 v[136:139], v225 offset:35840\n"
" v_mfma_f32_16x16x32_bf16 v[156:159], a[208:211], v[144:147], v[156:159]\n"
" v_mfma_f32_16x16x32_bf16 v[160:163], a[212:215], v[144:147], v[160:163]\n"
" s_waitcnt lgkmcnt(3)\n"
" v_mfma_f32_16x16x32_bf16 v[164:167], a[204:207], v[148:151], v[164:167]\n"
" ds_read_b128 v[140:143], v231 offset:35840\n"
" v_mfma_f32_16x16x32_bf16 v[168:171], a[208:211], v[148:151], v[168:171]\n"
" s_waitcnt lgkmcnt(3)\n"
" v_mfma_f32_16x16x32_bf16 v[152:155], a[216:219], v[128:131], v[152:155]\n"
" ds_read_b128 v[144:147], v225 offset:40960\n"
" v_mfma_f32_16x16x32_bf16 v[156:159], a[220:223], v[128:131], v[156:159]\n"
" v_mfma_f32_16x16x32_bf16 v[160:163], a[224:227], v[128:131], v[160:163]\n"
" s_waitcnt lgkmcnt(3)\n"
" v_mfma_f32_16x16x32_bf16 v[164:167], a[216:219], v[132:135], v[164:167]\n"
" ds_read_b128 v[148:151], v231 offset:40960\n"
" v_mfma_f32_16x16x32_bf16 v[168:171], a[220:223], v[132:135], v[168:171]\n"
" s_waitcnt lgkmcnt(3)\n"
" v_mfma_f32_16x16x32_bf16 v[152:155], a[228:231], v[136:139], v[152:155]\n"
" ds_read_b128 v[128:131], v225 offset:41984\n"
" v_mfma_f32_16x16x32_bf16 v[156:159], a[232:235], v[136:139], v[156:159]\n"
" v_mfma_f32_16x16x32_bf16 v[160:163], a[236:239], v[136:139], v[160:163]\n"
" s_waitcnt lgkmcnt(3)\n"
" v_mfma_f32_16x16x32_bf16 v[164:167], a[228:231], v[140:143], v[164:167]\n"
" ds_read_b128 v[132:135], v231 offset:41984\n"
" s_waitcnt vmcnt(0)\n"
" v_cmp_gt_i32 vcc, s78, v235\n"
" s_nop 0\n"
" s_cmp_eq_u64 vcc, 0\n"
" s_cselect_b32 s94, 1, 0\n"
" s_cselect_b32 s38, 134217728, 0\n"
" v_mfma_f32_16x16x32_bf16 v[168:171], a[232:235], v[140:143], v[168:171]\n"
" s_waitcnt lgkmcnt(3)\n"
" v_mfma_f32_16x16x32_bf16 v[152:155], a[240:243], v[144:147], v[152:155]\n"
" ds_read_b128 v[136:139], v225 offset:43008\n"
" v_mfma_f32_16x16x32_bf16 v[156:159], a[244:247], v[144:147], v[156:159]\n"
" s_add_i32 m0, s77, 0\n"
" s_add_i32 s76, s92, 0\n"
" buffer_load_dwordx4 v224, s[36:39], s76 offen sc1 lds\n"
" v_mfma_f32_16x16x32_bf16 v[160:163], a[248:251], v[144:147], v[160:163]\n"
" s_waitcnt lgkmcnt(3)\n"
" v_mfma_f32_16x16x32_bf16 v[164:167], a[240:243], v[148:151], v[164:167]\n"
" ds_read_b128 v[140:143], v231 offset:43008\n"
" v_mfma_f32_16x16x32_bf16 v[168:171], a[244:247], v[148:151], v[168:171]\n"
" s_waitcnt lgkmcnt(3)\n"
" v_mfma_f32_16x16x32_bf16 v[152:155], a[252:255], v[128:131], v[152:155]\n"
" ds_read_b128 v[144:147], v225 offset:44032\n"
" v_mfma_f32_16x16x32_bf16 v[156:159], v[0:3], v[128:131], v[156:159]\n"
" s_add_i32 m0, s77, 4096\n"
" s_add_i32 s76, s92, 4096\n"
" buffer_load_dwordx4 v224, s[36:39], s76 offen sc1 lds\n"
" v_mfma_f32_16x16x32_bf16 v[160:163], v[4:7], v[128:131], v[160:163]\n"
" s_waitcnt lgkmcnt(3)\n"
" v_mfma_f32_16x16x32_bf16 v[164:167], a[252:255], v[132:135], v[164:167]\n"
" ds_read_b128 v[148:151], v231 offset:44032\n"
" v_mfma_f32_16x16x32_bf16 v[168:171], v[0:3], v[132:135], v[168:171]\n"
" s_waitcnt lgkmcnt(3)\n"
" v_mfma_f32_16x16x32_bf16 v[152:155], v[8:11], v[136:139], v[152:155]\n"
" ds_read_b128 v[128:131], v225 offset:49152\n"
" v_mfma_f32_16x16x32_bf16 v[156:159], v[12:15], v[136:139], v[156:159]\n"
" s_add_i32 m0, s77, 8192\n"
" s_add_i32 s76, s92, 16384\n"
" buffer_load_dwordx4 v224, s[36:39], s76 offen sc1 lds\n"
" v_mfma_f32_16x16x32_bf16 v[160:163], v[16:19], v[136:139], v[160:163]\n"
" s_waitcnt lgkmcnt(3)\n"
" v_mfma_f32_16x16x32_bf16 v[164:167], v[8:11], v[140:143], v[164:167]\n"
" ds_read_b128 v[132:135], v231 offset:49152\n"
" v_mfma_f32_16x16x32_bf16 v[168:171], v[12:15], v[140:143], v[168:171]\n"
" s_waitcnt lgkmcnt(3)\n"
" v_mfma_f32_16x16x32_bf16 v[152:155], v[20:23], v[144:147], v[152:155]\n"
" ds_read_b128 v[136:139], v225 offset:50176\n"
" v_mfma_f32_16x16x32_bf16 v[156:159], v[24:27], v[144:147], v[156:159]\n"
" s_add_i32 m0, s77, 12288\n"
" s_add_i32 s76, s92, 20480\n"
" buffer_load_dwordx4 v224, s[36:39], s76 offen sc1 lds\n"
" v_mfma_f32_16x16x32_bf16 v[160:163], v[28:31], v[144:147], v[160:163]\n"
" s_waitcnt lgkmcnt(3)\n"
" v_mfma_f32_16x16x32_bf16 v[164:167], v[20:23], v[148:151], v[164:167]\n"
" ds_read_b128 v[140:143], v231 offset:50176\n"
" v_mfma_f32_16x16x32_bf16 v[168:171], v[24:27], v[148:151], v[168:171]\n"
" s_waitcnt lgkmcnt(3)\n"
" v_mfma_f32_16x16x32_bf16 v[152:155], v[32:35], v[128:131], v[152:155]\n"
" ds_read_b128 v[144:147], v225 offset:51200\n"
" v_mfma_f32_16x16x32_bf16 v[156:159], v[36:39], v[128:131], v[156:159]\n"
" s_add_i32 m0, s77, 16384\n"
" s_add_i32 s76, s92, 32768\n"
" buffer_load_dwordx4 v224, s[36:39], s76 offen sc1 lds\n"
" v_mfma_f32_16x16x32_bf16 v[160:163], v[40:43], v[128:131], v[160:163]\n"
" s_waitcnt lgkmcnt(3)\n"
" v_mfma_f32_16x16x32_bf16 v[164:167], v[32:35], v[132:135], v[164:167]\n"
" ds_read_b128 v[148:151], v231 offset:51200\n"
" v_mfma_f32_16x16x32_bf16 v[168:171], v[36:39], v[132:135], v[168:171]\n"
" s_waitcnt lgkmcnt(3)\n"
" v_mfma_f32_16x16x32_bf16 v[152:155], v[44:47], v[136:139], v[152:155]\n"
" ds_read_b128 v[128:131], v225 offset:52224\n"
" v_mfma_f32_16x16x32_bf16 v[156:159], v[48:51], v[136:139], v[156:159]\n"
" s_add_i32 m0, s77, 20480\n"
" s_add_i32 s76, s92, 36864\n"
" buffer_load_dwordx4 v224, s[36:39], s76 offen sc1 lds\n"
" v_mfma_f32_16x16x32_bf16 v[160:163], v[52:55], v[136:139], v[160:163]\n"
" s_waitcnt lgkmcnt(3)\n"
" v_mfma_f32_16x16x32_bf16 v[164:167], v[44:47], v[140:143], v[164:167]\n"
" ds_read_b128 v[132:135], v231 offset:52224\n"
" v_mfma_f32_16x16x32_bf16 v[168:171], v[48:51], v[140:143], v[168:171]\n"
" s_waitcnt lgkmcnt(3)\n"
" v_mfma_f32_16x16x32_bf16 v[152:155], v[56:59], v[144:147], v[152:155]\n"
" ds_read_b128 v[136:139], v225 offset:57344\n"
" v_mfma_f32_16x16x32_bf16 v[156:159], v[60:63], v[144:147], v[156:159]\n"
" s_add_i32 m0, s77, 24576\n"
" s_add_i32 s76, s92, 49152\n"
" buffer_load_dwordx4 v224, s[36:39], s76 offen sc1 lds\n"
" v_mfma_f32_16x16x32_bf16 v[160:163], v[64:67], v[144:147], v[160:163]\n"
" s_waitcnt lgkmcnt(3)\n"
" v_mfma_f32_16x16x32_bf16 v[164:167], v[56:59], v[148:151], v[164:167]\n"
" ds_read_b128 v[140:143], v231 offset:57344\n"
" v_mfma_f32_16x16x32_bf16 v[168:171], v[60:63], v[148:151], v[168:171]\n"
" s_waitcnt lgkmcnt(3)\n"
" v_mfma_f32_16x16x32_bf16 v[152:155], v[68:71], v[128:131], v[152:155]\n"
" ds_read_b128 v[144:147], v225 offset:58368\n"
" v_mfma_f32_16x16x32_bf16 v[156:159], v[72:75], v[128:131], v[156:159]\n"
" s_add_i32 m0, s77, 28672\n"
" s_add_i32 s76, s92, 53248\n"
" buffer_load_dwordx4 v224, s[36:39], s76 offen sc1 lds\n"
" v_mfma_f32_16x16x32_bf16 v[160:163], v[76:79], v[128:131], v[160:163]\n"
" s_waitcnt lgkmcnt(3)\n"
" v_mfma_f32_16x16x32_bf16 v[164:167], v[68:71], v[132:135], v[164:167]\n"
" ds_read_b128 v[148:151], v231 offset:58368\n"
" v_mfma_f32_16x16x32_bf16 v[168:171], v[72:75], v[132:135], v[168:171]\n"
" s_waitcnt lgkmcnt(3)\n"
" v_mfma_f32_16x16x32_bf16 v[152:155], v[80:83], v[136:139], v[152:155]\n"
" ds_read_b128 v[128:131], v225 offset:59392\n"
" v_mfma_f32_16x16x32_bf16 v[156:159], v[84:87], v[136:139], v[156:159]\n"
" s_add_i32 m0, s77, 32768\n"
" s_add_i32 s76, s92, 65536\n"
" buffer_load_dwordx4 v224, s[36:39], s76 offen sc1 lds\n"
" v_mfma_f32_16x16x32_bf16 v[160:163], v[88:91], v[136:139], v[160:163]\n"
" s_add_i32 m0, s77, 49152\n"
" s_add_i32 s76, s92, 98304\n"
" buffer_load_dwordx4 v224, s[36:39], s76 offen sc1 lds\n"
" s_waitcnt lgkmcnt(3)\n"
" v_mfma_f32_16x16x32_bf16 v[164:167], v[80:83], v[140:143], v[164:167]\n"
" ds_read_b128 v[132:135], v231 offset:59392\n"
" v_mfma_f32_16x16x32_bf16 v[168:171], v[84:87], v[140:143], v[168:171]\n"
" s_waitcnt lgkmcnt(3)\n"
" v_mfma_f32_16x16x32_bf16 v[152:155], v[92:95], v[144:147], v[152:155]\n"
" ds_read_b128 v[136:139], v225 offset:60416\n"
" v_mfma_f32_16x16x32_bf16 v[156:159], v[96:99], v[144:147], v[156:159]\n"
" s_add_i32 m0, s77, 36864\n"
" s_add_i32 s76, s92, 69632\n"
" buffer_load_dwordx4 v224, s[36:39], s76 offen sc1 lds\n"
" v_mfma_f32_16x16x32_bf16 v[160:163], v[100:103], v[144:147], v[160:163]\n"
" s_add_i32 m0, s77, 53248\n"
" s_add_i32 s76, s92, 102400\n"
" buffer_load_dwordx4 v224, s[36:39], s76 offen sc1 lds\n"
" s_waitcnt lgkmcnt(3)\n"
" v_mfma_f32_16x16x32_bf16 v[164:167], v[92:95], v[148:151], v[164:167]\n"
" ds_read_b128 v[140:143], v231 offset:60416\n"
" v_mfma_f32_16x16x32_bf16 v[168:171], v[96:99], v[148:151], v[168:171]\n"
" s_waitcnt lgkmcnt(3)\n"
" v_mfma_f32_16x16x32_bf16 v[152:155], v[104:107], v[128:131], v[152:155]\n"
" v_mfma_f32_16x16x32_bf16 v[156:159], v[108:111], v[128:131], v[156:159]\n"
" s_add_i32 m0, s77, 40960\n"
" s_add_i32 s76, s92, 81920\n"
" buffer_load_dwordx4 v224, s[36:39], s76 offen sc1 lds\n"
" v_mfma_f32_16x16x32_bf16 v[160:163], v[112:115], v[128:131], v[160:163]\n"
" s_add_i32 m0, s77, 57344\n"
" s_add_i32 s76, s92, 114688\n"
" buffer_load_dwordx4 v224, s[36:39], s76 offen sc1 lds\n"
" s_waitcnt lgkmcnt(2)\n"
" v_mfma_f32_16x16x32_bf16 v[164:167], v[104:107], v[132:135], v[164:167]\n"
" v_mfma_f32_16x16x32_bf16 v[168:171], v[108:111], v[132:135], v[168:171]\n"
" s_waitcnt lgkmcnt(1)\n"
" v_mfma_f32_16x16x32_bf16 v[152:155], v[116:119], v[136:139], v[152:155]\n"
" v_mfma_f32_16x16x32_bf16 v[156:159], v[120:123], v[136:139], v[156:159]\n"
" s_add_i32 m0, s77, 45056\n"
" s_add_i32 s76, s92, 86016\n"
" buffer_load_dwordx4 v224, s[36:39], s76 offen sc1 lds\n"
" v_mfma_f32_16x16x32_bf16 v[160:163], v[124:127], v[136:139], v[160:163]\n"
" s_add_i32 m0, s77, 61440\n"
" s_add_i32 s76, s92, 118784\n"
" buffer_load_dwordx4 v224, s[36:39], s76 offen sc1 lds\n"
" s_waitcnt lgkmcnt(0)\n"
" v_mfma_f32_16x16x32_bf16 v[164:167], v[116:119], v[140:143], v[164:167]\n"
" buffer_load_dword v235, v226, s[48:51], 0 offen sc1\n"
" v_mfma_f32_16x16x32_bf16 v[168:171], v[120:123], v[140:143], v[168:171]\n"
" v_xor_b32 v225, 0x10000, v225\n"
" v_xor_b32 v231, 0x10000, v231\n"
" v_cvt_pk_bf16_f32 v176, v152, v153\n"
" v_cvt_pk_bf16_f32 v177, v154, v155\n"
" v_cvt_pk_bf16_f32 v178, v156, v157\n"
" v_cvt_pk_bf16_f32 v179, v158, v159\n"
" v_cvt_pk_bf16_f32 v180, v160, v161\n"
" v_cvt_pk_bf16_f32 v181, v162, v163\n"
" buffer_store_dwordx4 v[176:179], v228, s[40:43], s93 offen sc1\n"
" buffer_store_dwordx2 v[180:181], v229, s[40:43], s93 offen sc1\n"
" v_cvt_pk_bf16_f32 v184, v164, v165\n"
" v_cvt_pk_bf16_f32 v185, v166, v167\n"
" v_cvt_pk_bf16_f32 v186, v168, v169\n"
" v_cvt_pk_bf16_f32 v187, v170, v171\n"
" buffer_store_dwordx4 v[184:187], v232, s[40:43], s93 offen sc1\n"
" s_cmp_lg_u32 s94, 0\n"
" s_cbranch_scc1 .Lwo_nx_%=\n"
" s_waitcnt vmcnt(3)\n"
" v_cmp_gt_i32 vcc, s78, v235\n"
" s_nop 0\n"
" s_cbranch_vccnz .Lwo_nx_%=\n"
" s_mov_b32 s38, 134217728\n"
" s_mov_b32 s94, 2\n"
" s_add_i32 m0, s77, 0\n"
" s_add_i32 s76, s92, 0\n"
" buffer_load_dwordx4 v224, s[36:39], s76 offen sc1 lds\n"
" s_add_i32 m0, s77, 4096\n"
" s_add_i32 s76, s92, 4096\n"
" buffer_load_dwordx4 v224, s[36:39], s76 offen sc1 lds\n"
" s_add_i32 m0, s77, 8192\n"
" s_add_i32 s76, s92, 16384\n"
" buffer_load_dwordx4 v224, s[36:39], s76 offen sc1 lds\n"
" s_add_i32 m0, s77, 12288\n"
" s_add_i32 s76, s92, 20480\n"
" buffer_load_dwordx4 v224, s[36:39], s76 offen sc1 lds\n"
" s_add_i32 m0, s77, 16384\n"
" s_add_i32 s76, s92, 32768\n"
" buffer_load_dwordx4 v224, s[36:39], s76 offen sc1 lds\n"
" s_add_i32 m0, s77, 20480\n"
" s_add_i32 s76, s92, 36864\n"
" buffer_load_dwordx4 v224, s[36:39], s76 offen sc1 lds\n"
" s_add_i32 m0, s77, 24576\n"
" s_add_i32 s76, s92, 49152\n"
" buffer_load_dwordx4 v224, s[36:39], s76 offen sc1 lds\n"
" s_add_i32 m0, s77, 28672\n"
" s_add_i32 s76, s92, 53248\n"
" buffer_load_dwordx4 v224, s[36:39], s76 offen sc1 lds\n"
" s_add_i32 m0, s77, 32768\n"
" s_add_i32 s76, s92, 65536\n"
" buffer_load_dwordx4 v224, s[36:39], s76 offen sc1 lds\n"
" s_add_i32 m0, s77, 36864\n"
" s_add_i32 s76, s92, 69632\n"
" buffer_load_dwordx4 v224, s[36:39], s76 offen sc1 lds\n"
" s_add_i32 m0, s77, 40960\n"
" s_add_i32 s76, s92, 81920\n"
" buffer_load_dwordx4 v224, s[36:39], s76 offen sc1 lds\n"
" s_add_i32 m0, s77, 45056\n"
" s_add_i32 s76, s92, 86016\n"
" buffer_load_dwordx4 v224, s[36:39], s76 offen sc1 lds\n"
" s_add_i32 m0, s77, 49152\n"
" s_add_i32 s76, s92, 98304\n"
" buffer_load_dwordx4 v224, s[36:39], s76 offen sc1 lds\n"
" s_add_i32 m0, s77, 53248\n"
" s_add_i32 s76, s92, 102400\n"
" buffer_load_dwordx4 v224, s[36:39], s76 offen sc1 lds\n"
" s_add_i32 m0, s77, 57344\n"
" s_add_i32 s76, s92, 114688\n"
" buffer_load_dwordx4 v224, s[36:39], s76 offen sc1 lds\n"
" s_add_i32 m0, s77, 61440\n"
" s_add_i32 s76, s92, 118784\n"
" buffer_load_dwordx4 v224, s[36:39], s76 offen sc1 lds\n"
" s_waitcnt vmcnt(16)\n"
" v_mov_b32 v216, 1\n"
" s_mov_b64 exec, 1\n"
" ds_add_rtn_u32 v217, v230, v216\n"
" s_mov_b64 exec, -1\n"
" s_waitcnt lgkmcnt(0)\n"
" v_readfirstlane_b32 s76, v217\n"
" s_nop 0\n"
" s_and_b32 s76, s76, 3\n"
" s_cmp_lg_u32 s76, 3\n"
" s_cbranch_scc1 .Lwo_lf3_%=\n"
" v_mov_b32 v216, s68\n"
" buffer_store_dword v216, v227, s[48:51], 0 offen sc1\n"
".Lwo_lf3_%=:\n"
" s_nop 3\n"
".Lwo_nx_%=:\n"
" s_mov_b32 s95, s68\n"
" s_add_i32 s68, s68, 1\n"
" s_cmp_lt_u32 s68, 64\n"
" s_cbranch_scc1 .Lwo_loop_%=\n"
" s_waitcnt vmcnt(0)\n"
" v_mov_b32 v216, 1\n"
" s_mov_b64 exec, 1\n"
" ds_add_rtn_u32 v217, v230, v216\n"
" s_mov_b64 exec, -1\n"
" s_waitcnt lgkmcnt(0)\n"
" v_readfirstlane_b32 s76, v217\n"
" s_nop 0\n"
" s_and_b32 s76, s76, 3\n"
" s_cmp_lg_u32 s76, 3\n"
" s_cbranch_scc1 .Lwo_lf2_%=\n"
" v_mov_b32 v216, s95\n"
" buffer_store_dword v216, v227, s[48:51], 0 offen sc1\n"
".Lwo_lf2_%=:\n"
" s_nop 3\n"
" s_waitcnt vmcnt(0)\n"
      : "+{v[224:239]}"(c0), "+{v[240:255]}"(c1), "+{s[36:39]}"(sI), "+{s[48:51]}"(sP)
      : "{s[40:43]}"(sO), "{s[52:55]}"(s0), "{s84}"(uni((int)OFF_O + (l * RS * 32 + gr * 8) * CELL)), "{s85}"(uni((int)OFF_TP + l * RS * (2 << 20))), "{s69}"(w), "{s[16:17]}"(p.tag), "{s[96:97]}"(dbgp(p, w))
      : "memory", "vcc", "scc", "m0", V_LOW, A_ALL, S_BULK, "s68");
  __builtin_amdgcn_endpgm();
}
__device__ __forceinline__ void ffn_role(const Params& p, int w, int id, int xc) {
  const int lane = lane_id(), tid = w * 64 + lane;
  const int l = id < 16 ? (id >> 2) & 1 : (id - 16) / (FW - 8), ic = id < 16 ? (id >> 3) * 4 + (id & 3) : 8 + (id - 16) % (FW - 8), g = lane >> 4, r = lane & 15;
  u8* ws = p.ws;
  i32x16 c0, c1;
#pragma unroll
  for (int i = 0; i < 16; ++i) { c0[i] = 0; c1[i] = 0; }
  c0[0] = tid * 16; c0[1] = lane * 16; c0[2] = (r >> 2) * CELL + CO + g * 64 + (r & 3) * 16;
  c0[3] = pg(PG_X1 + xc * 32 + (l * 8 + (lane & 7)) * 2); c0[4] = lane < 4 ? pg(PG_FFN + lane * 32 + l * 16 + ic) : OOB;
  c0[5] = LIMG + w * 20480 + lane * 16;
  c0[6] = ((r >> 2) * NPF + ic) * CELL + CO + w * 256 + g * 64 + (r & 3) * 16; c0[11] = pol_word(lane);
#pragma unroll
  for (int t = 0; t < 3; ++t) { const int ft = 3 * w + t; c0[7 + t] = 40960 + (ft >> 1) * 1024 + lane * 16 + (ft & 1) * 8; }
#pragma unroll
  for (int i = 0; i < 3; ++i) { const int q = (w * 3 + i) * 1024 + lane * 16; c1[i] = (q / 384) * 3456 + q % 384; c1[3 + i] = (4 * g + ((lane >> 2) & 3)) * 384 + (3 * w + i) * 32 + (lane & 3) * 8; }
  c1[6] = (16 * w + g) * 1280 + ((r ^ (g << 1)) << 4); c1[7] = (16 * w + 4 + g) * 1280 + ((r ^ ((4 + g) << 1)) << 4);
  c1[8] = trbase<256>(2 * w, lane); c1[9] = trbase<256>(2 * w + 1, lane);
  v4u sI = rawrsrc(ws, WSB), sO = rawrsrc(ws, WSB);
  v4u sP = rawrsrc(ws + OFF_PROG, PGB);
  const v4u s0 = rawrsrc(p.w1 + ic * 384, 640 * 3456 - ic * 384), s1 = rawrsrc(p.w2 + (size_t)ic * 192 * 1280, 192 * 1280);
  asm volatile(
" s_nop 4\n"
" s_mov_b32 s90, 0\n"
" s_lshl_b32 s77, s69, 12\n"
" s_add_i32 m0, s77, 65536\n"
" s_mov_b32 s76, 0\n"
" buffer_load_dwordx4 v246, s[56:59], s76 offen lds\n"
" s_add_i32 m0, s77, 66560\n"
" s_mov_b32 s76, 0\n"
" buffer_load_dwordx4 v247, s[56:59], s76 offen lds\n"
" s_add_i32 m0, s77, 67584\n"
" s_mov_b32 s76, 10240\n"
" buffer_load_dwordx4 v246, s[56:59], s76 offen lds\n"
" s_add_i32 m0, s77, 68608\n"
" s_mov_b32 s76, 10240\n"
" buffer_load_dwordx4 v247, s[56:59], s76 offen lds\n"
" s_lshl_b32 s77, s69, 12\n"
" s_add_i32 m0, s77, 81920\n"
" s_mov_b32 s76, 256\n"
" buffer_load_dwordx4 v246, s[56:59], s76 offen lds\n"
" s_add_i32 m0, s77, 82944\n"
" s_mov_b32 s76, 256\n"
" buffer_load_dwordx4 v247, s[56:59], s76 offen lds\n"
" s_add_i32 m0, s77, 83968\n"
" s_mov_b32 s76, 10496\n"
" buffer_load_dwordx4 v246, s[56:59], s76 offen lds\n"
" s_add_i32 m0, s77, 84992\n"
" s_mov_b32 s76, 10496\n"
" buffer_load_dwordx4 v247, s[56:59], s76 offen lds\n"
" s_lshl_b32 s77, s69, 12\n"
" s_add_i32 m0, s77, 98304\n"
" s_mov_b32 s76, 512\n"
" buffer_load_dwordx4 v246, s[56:59], s76 offen lds\n"
" s_add_i32 m0, s77, 99328\n"
" s_mov_b32 s76, 512\n"
" buffer_load_dwordx4 v247, s[56:59], s76 offen lds\n"
" s_add_i32 m0, s77, 100352\n"
" s_mov_b32 s76, 10752\n"
" buffer_load_dwordx4 v246, s[56:59], s76 offen lds\n"
" s_add_i32 m0, s77, 101376\n"
" s_mov_b32 s76, 10752\n"
" buffer_load_dwordx4 v247, s[56:59], s76 offen lds\n"
" s_lshl_b32 s77, s69, 12\n"
" s_add_i32 m0, s77, 114688\n"
" s_mov_b32 s76, 768\n"
" buffer_load_dwordx4 v246, s[56:59], s76 offen lds\n"
" s_add_i32 m0, s77, 115712\n"
" s_mov_b32 s76, 768\n"
" buffer_load_dwordx4 v247, s[56:59], s76 offen lds\n"
" s_add_i32 m0, s77, 116736\n"
" s_mov_b32 s76, 11008\n"
" buffer_load_dwordx4 v246, s[56:59], s76 offen lds\n"
" s_add_i32 m0, s77, 117760\n"
" s_mov_b32 s76, 11008\n"
" buffer_load_dwordx4 v247, s[56:59], s76 offen lds\n"
" s_lshl_b32 s77, s69, 12\n"
" s_add_i32 m0, s77, 131072\n"
" s_mov_b32 s76, 1024\n"
" buffer_load_dwordx4 v246, s[56:59], s76 offen lds\n"
" s_add_i32 m0, s77, 132096\n"
" s_mov_b32 s76, 1024\n"
" buffer_load_dwordx4 v247, s[56:59], s76 offen lds\n"
" s_add_i32 m0, s77, 133120\n"
" s_mov_b32 s76, 11264\n"
" buffer_load_dwordx4 v246, s[56:59], s76 offen lds\n"
" s_add_i32 m0, s77, 134144\n"
" s_mov_b32 s76, 11264\n"
" buffer_load_dwordx4 v247, s[56:59], s76 offen lds\n"
" s_lshl_b32 s77, s69, 12\n"
" s_add_i32 m0, s77, 0\n"
" s_mov_b32 s76, 81920\n"
" buffer_load_dwordx4 v246, s[56:59], s76 offen lds\n"
" s_add_i32 m0, s77, 1024\n"
" s_mov_b32 s76, 81920\n"
" buffer_load_dwordx4 v247, s[56:59], s76 offen lds\n"
" s_add_i32 m0, s77, 2048\n"
" s_mov_b32 s76, 92160\n"
" buffer_load_dwordx4 v246, s[56:59], s76 offen lds\n"
" s_add_i32 m0, s77, 3072\n"
" s_mov_b32 s76, 92160\n"
" buffer_load_dwordx4 v247, s[56:59], s76 offen lds\n"
" s_lshl_b32 s77, s69, 12\n"
" s_add_i32 m0, s77, 16384\n"
" s_mov_b32 s76, 82176\n"
" buffer_load_dwordx4 v246, s[56:59], s76 offen lds\n"
" s_add_i32 m0, s77, 17408\n"
" s_mov_b32 s76, 82176\n"
" buffer_load_dwordx4 v247, s[56:59], s76 offen lds\n"
" s_add_i32 m0, s77, 18432\n"
" s_mov_b32 s76, 92416\n"
" buffer_load_dwordx4 v246, s[56:59], s76 offen lds\n"
" s_add_i32 m0, s77, 19456\n"
" s_mov_b32 s76, 92416\n"
" buffer_load_dwordx4 v247, s[56:59], s76 offen lds\n"
" s_lshl_b32 s77, s69, 12\n"
" s_add_i32 m0, s77, 32768\n"
" s_mov_b32 s76, 82432\n"
" buffer_load_dwordx4 v246, s[56:59], s76 offen lds\n"
" s_add_i32 m0, s77, 33792\n"
" s_mov_b32 s76, 82432\n"
" buffer_load_dwordx4 v247, s[56:59], s76 offen lds\n"
" s_add_i32 m0, s77, 34816\n"
" s_mov_b32 s76, 92672\n"
" buffer_load_dwordx4 v246, s[56:59], s76 offen lds\n"
" s_add_i32 m0, s77, 35840\n"
" s_mov_b32 s76, 92672\n"
" buffer_load_dwordx4 v247, s[56:59], s76 offen lds\n"
" s_lshl_b32 s77, s69, 12\n"
" s_add_i32 m0, s77, 49152\n"
" s_mov_b32 s76, 82688\n"
" buffer_load_dwordx4 v246, s[56:59], s76 offen lds\n"
" s_add_i32 m0, s77, 50176\n"
" s_mov_b32 s76, 82688\n"
" buffer_load_dwordx4 v247, s[56:59], s76 offen lds\n"
" s_add_i32 m0, s77, 51200\n"
" s_mov_b32 s76, 92928\n"
" buffer_load_dwordx4 v246, s[56:59], s76 offen lds\n"
" s_add_i32 m0, s77, 52224\n"
" s_mov_b32 s76, 92928\n"
" buffer_load_dwordx4 v247, s[56:59], s76 offen lds\n"
" s_lshl_b32 s77, s69, 12\n"
" s_add_i32 m0, s77, 147456\n"
" s_mov_b32 s76, 82944\n"
" buffer_load_dwordx4 v246, s[56:59], s76 offen lds\n"
" s_add_i32 m0, s77, 148480\n"
" s_mov_b32 s76, 82944\n"
" buffer_load_dwordx4 v247, s[56:59], s76 offen lds\n"
" s_add_i32 m0, s77, 149504\n"
" s_mov_b32 s76, 93184\n"
" buffer_load_dwordx4 v246, s[56:59], s76 offen lds\n"
" s_add_i32 m0, s77, 150528\n"
" s_mov_b32 s76, 93184\n"
" buffer_load_dwordx4 v247, s[56:59], s76 offen lds\n"
" s_waitcnt lgkmcnt(0)\n"
" s_waitcnt vmcnt(36)\n"
" s_barrier\n"
" v_add_u32 v216, 65536, v248\n"
" v_add_u32 v217, 65536, v249\n"
" ds_read_b64_tr_b16 a[160:161], v216 offset:0\n"
" ds_read_b64_tr_b16 a[162:163], v216 offset:4096\n"
" ds_read_b64_tr_b16 a[164:165], v216 offset:8192\n"
" ds_read_b64_tr_b16 a[166:167], v216 offset:12288\n"
" ds_read_b64_tr_b16 a[184:185], v217 offset:0\n"
" ds_read_b64_tr_b16 a[186:187], v217 offset:4096\n"
" ds_read_b64_tr_b16 a[188:189], v217 offset:8192\n"
" ds_read_b64_tr_b16 a[190:191], v217 offset:12288\n"
" s_waitcnt lgkmcnt(0)\n"
" s_waitcnt vmcnt(32)\n"
" s_barrier\n"
" s_lshl_b32 s77, s69, 12\n"
" s_add_i32 m0, s77, 65536\n"
" s_mov_b32 s76, 163840\n"
" buffer_load_dwordx4 v246, s[56:59], s76 offen lds\n"
" s_add_i32 m0, s77, 66560\n"
" s_mov_b32 s76, 163840\n"
" buffer_load_dwordx4 v247, s[56:59], s76 offen lds\n"
" s_add_i32 m0, s77, 67584\n"
" s_mov_b32 s76, 174080\n"
" buffer_load_dwordx4 v246, s[56:59], s76 offen lds\n"
" s_add_i32 m0, s77, 68608\n"
" s_mov_b32 s76, 174080\n"
" buffer_load_dwordx4 v247, s[56:59], s76 offen lds\n"
" v_add_u32 v216, 81920, v248\n"
" v_add_u32 v217, 81920, v249\n"
" ds_read_b64_tr_b16 a[208:209], v216 offset:0\n"
" ds_read_b64_tr_b16 a[210:211], v216 offset:4096\n"
" ds_read_b64_tr_b16 a[212:213], v216 offset:8192\n"
" ds_read_b64_tr_b16 a[214:215], v216 offset:12288\n"
" ds_read_b64_tr_b16 a[232:233], v217 offset:0\n"
" ds_read_b64_tr_b16 a[234:235], v217 offset:4096\n"
" ds_read_b64_tr_b16 a[236:237], v217 offset:8192\n"
" ds_read_b64_tr_b16 a[238:239], v217 offset:12288\n"
" s_waitcnt lgkmcnt(0)\n"
" s_waitcnt vmcnt(32)\n"
" s_barrier\n"
" s_lshl_b32 s77, s69, 12\n"
" s_add_i32 m0, s77, 81920\n"
" s_mov_b32 s76, 164096\n"
" buffer_load_dwordx4 v246, s[56:59], s76 offen lds\n"
" s_add_i32 m0, s77, 82944\n"
" s_mov_b32 s76, 164096\n"
" buffer_load_dwordx4 v247, s[56:59], s76 offen lds\n"
" s_add_i32 m0, s77, 83968\n"
" s_mov_b32 s76, 174336\n"
" buffer_load_dwordx4 v246, s[56:59], s76 offen lds\n"
" s_add_i32 m0, s77, 84992\n"
" s_mov_b32 s76, 174336\n"
" buffer_load_dwordx4 v247, s[56:59], s76 offen lds\n"
" v_add_u32 v216, 98304, v248\n"
" v_add_u32 v217, 98304, v249\n"
" ds_read_b64_tr_b16 v[0:1], v216 offset:0\n"
" ds_read_b64_tr_b16 v[2:3], v216 offset:4096\n"
" ds_read_b64_tr_b16 v[4:5], v216 offset:8192\n"
" ds_read_b64_tr_b16 v[6:7], v216 offset:12288\n"
" ds_read_b64_tr_b16 v[24:25], v217 offset:0\n"
" ds_read_b64_tr_b16 v[26:27], v217 offset:4096\n"
" ds_read_b64_tr_b16 v[28:29], v217 offset:8192\n"
" ds_read_b64_tr_b16 v[30:31], v217 offset:12288\n"
" s_waitcnt lgkmcnt(0)\n"
" s_waitcnt vmcnt(32)\n"
" s_barrier\n"
" s_lshl_b32 s77, s69, 12\n"
" s_add_i32 m0, s77, 98304\n"
" s_mov_b32 s76, 164352\n"
" buffer_load_dwordx4 v246, s[56:59], s76 offen lds\n"
" s_add_i32 m0, s77, 99328\n"
" s_mov_b32 s76, 164352\n"
" buffer_load_dwordx4 v247, s[56:59], s76 offen lds\n"
" s_add_i32 m0, s77, 100352\n"
" s_mov_b32 s76, 174592\n"
" buffer_load_dwordx4 v246, s[56:59], s76 offen lds\n"
" s_add_i32 m0, s77, 101376\n"
" s_mov_b32 s76, 174592\n"
" buffer_load_dwordx4 v247, s[56:59], s76 offen lds\n"
" v_add_u32 v216, 114688, v248\n"
" v_add_u32 v217, 114688, v249\n"
" ds_read_b64_tr_b16 v[48:49], v216 offset:0\n"
" ds_read_b64_tr_b16 v[50:51], v216 offset:4096\n"
" ds_read_b64_tr_b16 v[52:53], v216 offset:8192\n"
" ds_read_b64_tr_b16 v[54:55], v216 offset:12288\n"
" ds_read_b64_tr_b16 v[72:73], v217 offset:0\n"
" ds_read_b64_tr_b16 v[74:75], v217 offset:4096\n"
" ds_read_b64_tr_b16 v[76:77], v217 offset:8192\n"
" ds_read_b64_tr_b16 v[78:79], v217 offset:12288\n"
" s_waitcnt lgkmcnt(0)\n"
" s_waitcnt vmcnt(32)\n"
" s_barrier\n"
" s_lshl_b32 s77, s69, 12\n"
" s_add_i32 m0, s77, 114688\n"
" s_mov_b32 s76, 164608\n"
" buffer_load_dwordx4 v246, s[56:59], s76 offen lds\n"
" s_add_i32 m0, s77, 115712\n"
" s_mov_b32 s76, 164608\n"
" buffer_load_dwordx4 v247, s[56:59], s76 offen lds\n"
" s_add_i32 m0, s77, 116736\n"
" s_mov_b32 s76, 174848\n"
" buffer_load_dwordx4 v246, s[56:59], s76 offen lds\n"
" s_add_i32 m0, s77, 117760\n"
" s_mov_b32 s76, 174848\n"
" buffer_load_dwordx4 v247, s[56:59], s76 offen lds\n"
" v_add_u32 v216, 131072, v248\n"
" v_add_u32 v217, 131072, v249\n"
" ds_read_b64_tr_b16 v[96:97], v216 offset:0\n"
" ds_read_b64_tr_b16 v[98:99], v216 offset:4096\n"
" ds_read_b64_tr_b16 v[100:101], v216 offset:8192\n"
" ds_read_b64_tr_b16 v[102:103], v216 offset:12288\n"
" ds_read_b64_tr_b16 v[120:121], v217 offset:0\n"
" ds_read_b64_tr_b16 v[122:123], v217 offset:4096\n"
" ds_read_b64_tr_b16 v[124:125], v217 offset:8192\n"
" ds_read_b64_tr_b16 v[126:127], v217 offset:12288\n"
" s_waitcnt lgkmcnt(0)\n"
" s_waitcnt vmcnt(32)\n"
" s_barrier\n"
" s_lshl_b32 s77, s69, 12\n"
" s_add_i32 m0, s77, 131072\n"
" s_mov_b32 s76, 164864\n"
" buffer_load_dwordx4 v246, s[56:59], s76 offen lds\n"
" s_add_i32 m0, s77, 132096\n"
" s_mov_b32 s76, 164864\n"
" buffer_load_dwordx4 v247, s[56:59], s76 offen lds\n"
" s_add_i32 m0, s77, 133120\n"
" s_mov_b32 s76, 175104\n"
" buffer_load_dwordx4 v246, s[56:59], s76 offen lds\n"
" s_add_i32 m0, s77, 134144\n"
" s_mov_b32 s76, 175104\n"
" buffer_load_dwordx4 v247, s[56:59], s76 offen lds\n"
" ds_read_b64_tr_b16 a[168:169], v248 offset:0\n"
" ds_read_b64_tr_b16 a[170:171], v248 offset:4096\n"
" ds_read_b64_tr_b16 a[172:173], v248 offset:8192\n"
" ds_read_b64_tr_b16 a[174:175], v248 offset:12288\n"
" ds_read_b64_tr_b16 a[192:193], v249 offset:0\n"
" ds_read_b64_tr_b16 a[194:195], v249 offset:4096\n"
" ds_read_b64_tr_b16 a[196:197], v249 offset:8192\n"
" ds_read_b64_tr_b16 a[198:199], v249 offset:12288\n"
" s_waitcnt lgkmcnt(0)\n"
" s_waitcnt vmcnt(32)\n"
" s_barrier\n"
" s_mul_i32 s77, s69, 3072\n"
" s_add_i32 m0, s77, 0\n"
" s_mov_b32 s76, 0\n"
" buffer_load_dwordx4 v240, s[52:55], s76 offen lds\n"
" s_add_i32 m0, s77, 1024\n"
" s_mov_b32 s76, 0\n"
" buffer_load_dwordx4 v241, s[52:55], s76 offen lds\n"
" s_add_i32 m0, s77, 2048\n"
" s_mov_b32 s76, 0\n"
" buffer_load_dwordx4 v242, s[52:55], s76 offen lds\n"
" ds_read_b64_tr_b16 a[216:217], v248 offset:16384\n"
" ds_read_b64_tr_b16 a[218:219], v248 offset:20480\n"
" ds_read_b64_tr_b16 a[220:221], v248 offset:24576\n"
" ds_read_b64_tr_b16 a[222:223], v248 offset:28672\n"
" ds_read_b64_tr_b16 a[240:241], v249 offset:16384\n"
" ds_read_b64_tr_b16 a[242:243], v249 offset:20480\n"
" ds_read_b64_tr_b16 a[244:245], v249 offset:24576\n"
" ds_read_b64_tr_b16 a[246:247], v249 offset:28672\n"
" s_waitcnt lgkmcnt(0)\n"
" s_waitcnt vmcnt(31)\n"
" s_barrier\n"
" s_mul_i32 s77, s69, 3072\n"
" s_add_i32 m0, s77, 16384\n"
" s_mov_b32 s76, 110592\n"
" buffer_load_dwordx4 v240, s[52:55], s76 offen lds\n"
" s_add_i32 m0, s77, 17408\n"
" s_mov_b32 s76, 110592\n"
" buffer_load_dwordx4 v241, s[52:55], s76 offen lds\n"
" s_add_i32 m0, s77, 18432\n"
" s_mov_b32 s76, 110592\n"
" buffer_load_dwordx4 v242, s[52:55], s76 offen lds\n"
" ds_read_b64_tr_b16 v[8:9], v248 offset:32768\n"
" ds_read_b64_tr_b16 v[10:11], v248 offset:36864\n"
" ds_read_b64_tr_b16 v[12:13], v248 offset:40960\n"
" ds_read_b64_tr_b16 v[14:15], v248 offset:45056\n"
" ds_read_b64_tr_b16 v[32:33], v249 offset:32768\n"
" ds_read_b64_tr_b16 v[34:35], v249 offset:36864\n"
" ds_read_b64_tr_b16 v[36:37], v249 offset:40960\n"
" ds_read_b64_tr_b16 v[38:39], v249 offset:45056\n"
" s_waitcnt lgkmcnt(0)\n"
" s_waitcnt vmcnt(30)\n"
" s_barrier\n"
" s_mul_i32 s77, s69, 3072\n"
" s_add_i32 m0, s77, 32768\n"
" s_mov_b32 s76, 221184\n"
" buffer_load_dwordx4 v240, s[52:55], s76 offen lds\n"
" s_add_i32 m0, s77, 33792\n"
" s_mov_b32 s76, 221184\n"
" buffer_load_dwordx4 v241, s[52:55], s76 offen lds\n"
" s_add_i32 m0, s77, 34816\n"
" s_mov_b32 s76, 221184\n"
" buffer_load_dwordx4 v242, s[52:55], s76 offen lds\n"
" v_add_u32 v216, 49152, v248\n"
" v_add_u32 v217, 49152, v249\n"
" ds_read_b64_tr_b16 v[56:57], v216 offset:0\n"
" ds_read_b64_tr_b16 v[58:59], v216 offset:4096\n"
" ds_read_b64_tr_b16 v[60:61], v216 offset:8192\n"
" ds_read_b64_tr_b16 v[62:63], v216 offset:12288\n"
" ds_read_b64_tr_b16 v[80:81], v217 offset:0\n"
" ds_read_b64_tr_b16 v[82:83], v217 offset:4096\n"
" ds_read_b64_tr_b16 v[84:85], v217 offset:8192\n"
" ds_read_b64_tr_b16 v[86:87], v217 offset:12288\n"
" s_waitcnt lgkmcnt(0)\n"
" s_waitcnt vmcnt(29)\n"
" s_barrier\n"
" s_mul_i32 s77, s69, 3072\n"
" s_add_i32 m0, s77, 49152\n"
" s_mov_b32 s76, 331776\n"
" buffer_load_dwordx4 v240, s[52:55], s76 offen lds\n"
" s_add_i32 m0, s77, 50176\n"
" s_mov_b32 s76, 331776\n"
" buffer_load_dwordx4 v241, s[52:55], s76 offen lds\n"
" s_add_i32 m0, s77, 51200\n"
" s_mov_b32 s76, 331776\n"
" buffer_load_dwordx4 v242, s[52:55], s76 offen lds\n"
" v_add_u32 v216, 147456, v248\n"
" v_add_u32 v217, 147456, v249\n"
" ds_read_b64_tr_b16 v[104:105], v216 offset:0\n"
" ds_read_b64_tr_b16 v[106:107], v216 offset:4096\n"
" ds_read_b64_tr_b16 v[108:109], v216 offset:8192\n"
" ds_read_b64_tr_b16 v[110:111], v216 offset:12288\n"
" ds_read_b64_tr_b16 v[128:129], v217 offset:0\n"
" ds_read_b64_tr_b16 v[130:131], v217 offset:4096\n"
" ds_read_b64_tr_b16 v[132:133], v217 offset:8192\n"
" ds_read_b64_tr_b16 v[134:135], v217 offset:12288\n"
" s_waitcnt lgkmcnt(0)\n"
" s_waitcnt vmcnt(28)\n"
" s_barrier\n"
" s_mul_i32 s77, s69, 3072\n"
" s_add_i32 m0, s77, 147456\n"
" s_mov_b32 s76, 442368\n"
" buffer_load_dwordx4 v240, s[52:55], s76 offen lds\n"
" s_add_i32 m0, s77, 148480\n"
" s_mov_b32 s76, 442368\n"
" buffer_load_dwordx4 v241, s[52:55], s76 offen lds\n"
" s_add_i32 m0, s77, 149504\n"
" s_mov_b32 s76, 442368\n"
" buffer_load_dwordx4 v242, s[52:55], s76 offen lds\n"
" v_add_u32 v216, 65536, v248\n"
" v_add_u32 v217, 65536, v249\n"
" ds_read_b64_tr_b16 a[176:177], v216 offset:0\n"
" ds_read_b64_tr_b16 a[178:179], v216 offset:4096\n"
" ds_read_b64_tr_b16 a[180:181], v216 offset:8192\n"
" ds_read_b64_tr_b16 a[182:183], v216 offset:12288\n"
" ds_read_b64_tr_b16 a[200:201], v217 offset:0\n"
" ds_read_b64_tr_b16 a[202:203], v217 offset:4096\n"
" ds_read_b64_tr_b16 a[204:205], v217 offset:8192\n"
" ds_read_b64_tr_b16 a[206:207], v217 offset:12288\n"
" s_waitcnt lgkmcnt(0)\n"
" s_waitcnt vmcnt(27)\n"
" s_barrier\n"
" v_add_u32 v216, 81920, v248\n"
" v_add_u32 v217, 81920, v249\n"
" ds_read_b64_tr_b16 a[224:225], v216 offset:0\n"
" ds_read_b64_tr_b16 a[226:227], v216 offset:4096\n"
" ds_read_b64_tr_b16 a[228:229], v216 offset:8192\n"
" ds_read_b64_tr_b16 a[230:231], v216 offset:12288\n"
" ds_read_b64_tr_b16 a[248:249], v217 offset:0\n"
" ds_read_b64_tr_b16 a[250:251], v217 offset:4096\n"
" ds_read_b64_tr_b16 a[252:253], v217 offset:8192\n"
" ds_read_b64_tr_b16 a[254:255], v217 offset:12288\n"
" s_waitcnt lgkmcnt(0)\n"
" s_waitcnt vmcnt(23)\n"
" s_barrier\n"
" v_add_u32 v216, 98304, v248\n"
" v_add_u32 v217, 98304, v249\n"
" ds_read_b64_tr_b16 v[16:17], v216 offset:0\n"
" ds_read_b64_tr_b16 v[18:19], v216 offset:4096\n"
" ds_read_b64_tr_b16 v[20:21], v216 offset:8192\n"
" ds_read_b64_tr_b16 v[22:23], v216 offset:12288\n"
" ds_read_b64_tr_b16 v[40:41], v217 offset:0\n"
" ds_read_b64_tr_b16 v[42:43], v217 offset:4096\n"
" ds_read_b64_tr_b16 v[44:45], v217 offset:8192\n"
" ds_read_b64_tr_b16 v[46:47], v217 offset:12288\n"
" s_waitcnt lgkmcnt(0)\n"
" s_waitcnt vmcnt(19)\n"
" s_barrier\n"
" v_add_u32 v216, 114688, v248\n"
" v_add_u32 v217, 114688, v249\n"
" ds_read_b64_tr_b16 v[64:65], v216 offset:0\n"
" ds_read_b64_tr_b16 v[66:67], v216 offset:4096\n"
" ds_read_b64_tr_b16 v[68:69], v216 offset:8192\n"
" ds_read_b64_tr_b16 v[70:71], v216 offset:12288\n"
" ds_read_b64_tr_b16 v[88:89], v217 offset:0\n"
" ds_read_b64_tr_b16 v[90:91], v217 offset:4096\n"
" ds_read_b64_tr_b16 v[92:93], v217 offset:8192\n"
" ds_read_b64_tr_b16 v[94:95], v217 offset:12288\n"
" s_waitcnt lgkmcnt(0)\n"
" s_waitcnt vmcnt(15)\n"
" s_barrier\n"
" v_add_u32 v216, 131072, v248\n"
" v_add_u32 v217, 131072, v249\n"
" ds_read_b64_tr_b16 v[112:113], v216 offset:0\n"
" ds_read_b64_tr_b16 v[114:115], v216 offset:4096\n"
" ds_read_b64_tr_b16 v[116:117], v216 offset:8192\n"
" ds_read_b64_tr_b16 v[118:119], v216 offset:12288\n"
" ds_read_b64_tr_b16 v[136:137], v217 offset:0\n"
" ds_read_b64_tr_b16 v[138:139], v217 offset:4096\n"
" ds_read_b64_tr_b16 v[140:141], v217 offset:8192\n"
" ds_read_b64_tr_b16 v[142:143], v217 offset:12288\n"
" s_waitcnt lgkmcnt(0)\n"
" s_waitcnt vmcnt(12)\n"
" s_barrier\n"
" ds_read_b64_tr_b16 a[0:1], v243 offset:0\n"
" ds_read_b64_tr_b16 a[2:3], v243 offset:6144\n"
" ds_read_b64_tr_b16 a[4:5], v244 offset:0\n"
" ds_read_b64_tr_b16 a[6:7], v244 offset:6144\n"
" ds_read_b64_tr_b16 v[144:145], v245 offset:0\n"
" ds_read_b64_tr_b16 v[146:147], v245 offset:6144\n"
" s_waitcnt lgkmcnt(0)\n"
" ds_write_b128 v229, v[144:147] offset:0\n"
" s_waitcnt lgkmcnt(0)\n"
" s_waitcnt vmcnt(9)\n"
" s_barrier\n"
" s_mul_i32 s77, s69, 3072\n"
" s_add_i32 m0, s77, 0\n"
" s_mov_b32 s76, 552960\n"
" buffer_load_dwordx4 v240, s[52:55], s76 offen lds\n"
" s_add_i32 m0, s77, 1024\n"
" s_mov_b32 s76, 552960\n"
" buffer_load_dwordx4 v241, s[52:55], s76 offen lds\n"
" s_add_i32 m0, s77, 2048\n"
" s_mov_b32 s76, 552960\n"
" buffer_load_dwordx4 v242, s[52:55], s76 offen lds\n"
" ds_read_b64_tr_b16 a[8:9], v243 offset:16384\n"
" ds_read_b64_tr_b16 a[10:11], v243 offset:22528\n"
" ds_read_b64_tr_b16 a[12:13], v244 offset:16384\n"
" ds_read_b64_tr_b16 a[14:15], v244 offset:22528\n"
" ds_read_b64_tr_b16 v[144:145], v245 offset:16384\n"
" ds_read_b64_tr_b16 v[146:147], v245 offset:22528\n"
" s_waitcnt lgkmcnt(0)\n"
" ds_write_b128 v229, v[144:147] offset:1024\n"
" s_waitcnt lgkmcnt(0)\n"
" s_waitcnt vmcnt(9)\n"
" s_barrier\n"
" s_mul_i32 s77, s69, 3072\n"
" s_add_i32 m0, s77, 16384\n"
" s_mov_b32 s76, 663552\n"
" buffer_load_dwordx4 v240, s[52:55], s76 offen lds\n"
" s_add_i32 m0, s77, 17408\n"
" s_mov_b32 s76, 663552\n"
" buffer_load_dwordx4 v241, s[52:55], s76 offen lds\n"
" s_add_i32 m0, s77, 18432\n"
" s_mov_b32 s76, 663552\n"
" buffer_load_dwordx4 v242, s[52:55], s76 offen lds\n"
" ds_read_b64_tr_b16 a[16:17], v243 offset:32768\n"
" ds_read_b64_tr_b16 a[18:19], v243 offset:38912\n"
" ds_read_b64_tr_b16 a[20:21], v244 offset:32768\n"
" ds_read_b64_tr_b16 a[22:23], v244 offset:38912\n"
" ds_read_b64_tr_b16 v[144:145], v245 offset:32768\n"
" ds_read_b64_tr_b16 v[146:147], v245 offset:38912\n"
" s_waitcnt lgkmcnt(0)\n"
" ds_write_b128 v229, v[144:147] offset:2048\n"
" s_waitcnt lgkmcnt(0)\n"
" s_waitcnt vmcnt(9)\n"
" s_barrier\n"
" s_mul_i32 s77, s69, 3072\n"
" s_add_i32 m0, s77, 32768\n"
" s_mov_b32 s76, 774144\n"
" buffer_load_dwordx4 v240, s[52:55], s76 offen lds\n"
" s_add_i32 m0, s77, 33792\n"
" s_mov_b32 s76, 774144\n"
" buffer_load_dwordx4 v241, s[52:55], s76 offen lds\n"
" s_add_i32 m0, s77, 34816\n"
" s_mov_b32 s76, 774144\n"
" buffer_load_dwordx4 v242, s[52:55], s76 offen lds\n"
" v_add_u32 v216, 49152, v243\n"
" v_add_u32 v217, 49152, v244\n"
" v_add_u32 v218, 49152, v245\n"
" ds_read_b64_tr_b16 a[24:25], v216 offset:0\n"
" ds_read_b64_tr_b16 a[26:27], v216 offset:6144\n"
" ds_read_b64_tr_b16 a[28:29], v217 offset:0\n"
" ds_read_b64_tr_b16 a[30:31], v217 offset:6144\n"
" ds_read_b64_tr_b16 v[144:145], v218 offset:0\n"
" ds_read_b64_tr_b16 v[146:147], v218 offset:6144\n"
" s_waitcnt lgkmcnt(0)\n"
" ds_write_b128 v229, v[144:147] offset:3072\n"
" s_waitcnt lgkmcnt(0)\n"
" s_waitcnt vmcnt(9)\n"
" s_barrier\n"
" s_mul_i32 s77, s69, 3072\n"
" s_add_i32 m0, s77, 49152\n"
" s_mov_b32 s76, 884736\n"
" buffer_load_dwordx4 v240, s[52:55], s76 offen lds\n"
" s_add_i32 m0, s77, 50176\n"
" s_mov_b32 s76, 884736\n"
" buffer_load_dwordx4 v241, s[52:55], s76 offen lds\n"
" s_add_i32 m0, s77, 51200\n"
" s_mov_b32 s76, 884736\n"
" buffer_load_dwordx4 v242, s[52:55], s76 offen lds\n"
" v_add_u32 v216, 147456, v243\n"
" v_add_u32 v217, 147456, v244\n"
" v_add_u32 v218, 147456, v245\n"
" ds_read_b64_tr_b16 a[32:33], v216 offset:0\n"
" ds_read_b64_tr_b16 a[34:35], v216 offset:6144\n"
" ds_read_b64_tr_b16 a[36:37], v217 offset:0\n"
" ds_read_b64_tr_b16 a[38:39], v217 offset:6144\n"
" ds_read_b64_tr_b16 v[144:145], v218 offset:0\n"
" ds_read_b64_tr_b16 v[146:147], v218 offset:6144\n"
" s_waitcnt lgkmcnt(0)\n"
" ds_write_b128 v229, v[144:147] offset:4096\n"
" s_waitcnt lgkmcnt(0)\n"
" s_waitcnt vmcnt(9)\n"
" s_barrier\n"
" s_mul_i32 s77, s69, 3072\n"
" s_add_i32 m0, s77, 147456\n"
" s_mov_b32 s76, 995328\n"
" buffer_load_dwordx4 v240, s[52:55], s76 offen lds\n"
" s_add_i32 m0, s77, 148480\n"
" s_mov_b32 s76, 995328\n"
" buffer_load_dwordx4 v241, s[52:55], s76 offen lds\n"
" s_add_i32 m0, s77, 149504\n"
" s_mov_b32 s76, 995328\n"
" buffer_load_dwordx4 v242, s[52:55], s76 offen lds\n"
" ds_read_b64_tr_b16 a[40:41], v243 offset:0\n"
" ds_read_b64_tr_b16 a[42:43], v243 offset:6144\n"
" ds_read_b64_tr_b16 a[44:45], v244 offset:0\n"
" ds_read_b64_tr_b16 a[46:47], v244 offset:6144\n"
" ds_read_b64_tr_b16 v[144:145], v245 offset:0\n"
" ds_read_b64_tr_b16 v[146:147], v245 offset:6144\n"
" s_waitcnt lgkmcnt(0)\n"
" ds_write_b128 v229, v[144:147] offset:5120\n"
" s_waitcnt lgkmcnt(0)\n"
" s_waitcnt vmcnt(9)\n"
" s_barrier\n"
" s_mul_i32 s77, s69, 3072\n"
" s_add_i32 m0, s77, 0\n"
" s_mov_b32 s76, 1105920\n"
" buffer_load_dwordx4 v240, s[52:55], s76 offen lds\n"
" s_add_i32 m0, s77, 1024\n"
" s_mov_b32 s76, 1105920\n"
" buffer_load_dwordx4 v241, s[52:55], s76 offen lds\n"
" s_add_i32 m0, s77, 2048\n"
" s_mov_b32 s76, 1105920\n"
" buffer_load_dwordx4 v242, s[52:55], s76 offen lds\n"
" ds_read_b64_tr_b16 a[48:49], v243 offset:16384\n"
" ds_read_b64_tr_b16 a[50:51], v243 offset:22528\n"
" ds_read_b64_tr_b16 a[52:53], v244 offset:16384\n"
" ds_read_b64_tr_b16 a[54:55], v244 offset:22528\n"
" ds_read_b64_tr_b16 v[144:145], v245 offset:16384\n"
" ds_read_b64_tr_b16 v[146:147], v245 offset:22528\n"
" s_waitcnt lgkmcnt(0)\n"
" ds_write_b128 v229, v[144:147] offset:6144\n"
" s_waitcnt lgkmcnt(0)\n"
" s_waitcnt vmcnt(9)\n"
" s_barrier\n"
" s_mul_i32 s77, s69, 3072\n"
" s_add_i32 m0, s77, 16384\n"
" s_mov_b32 s76, 1216512\n"
" buffer_load_dwordx4 v240, s[52:55], s76 offen lds\n"
" s_add_i32 m0, s77, 17408\n"
" s_mov_b32 s76, 1216512\n"
" buffer_load_dwordx4 v241, s[52:55], s76 offen lds\n"
" s_add_i32 m0, s77, 18432\n"
" s_mov_b32 s76, 1216512\n"
" buffer_load_dwordx4 v242, s[52:55], s76 offen lds\n"
" ds_read_b64_tr_b16 a[56:57], v243 offset:32768\n"
" ds_read_b64_tr_b16 a[58:59], v243 offset:38912\n"
" ds_read_b64_tr_b16 a[60:61], v244 offset:32768\n"
" ds_read_b64_tr_b16 a[62:63], v244 offset:38912\n"
" ds_read_b64_tr_b16 v[144:145], v245 offset:32768\n"
" ds_read_b64_tr_b16 v[146:147], v245 offset:38912\n"
" s_waitcnt lgkmcnt(0)\n"
" ds_write_b128 v229, v[144:147] offset:7168\n"
" s_waitcnt lgkmcnt(0)\n"
" s_waitcnt vmcnt(9)\n"
" s_barrier\n"
" s_mul_i32 s77, s69, 3072\n"
" s_add_i32 m0, s77, 32768\n"
" s_mov_b32 s76, 1327104\n"
" buffer_load_dwordx4 v240, s[52:55], s76 offen lds\n"
" s_add_i32 m0, s77, 33792\n"
" s_mov_b32 s76, 1327104\n"
" buffer_load_dwordx4 v241, s[52:55], s76 offen lds\n"
" s_add_i32 m0, s77, 34816\n"
" s_mov_b32 s76, 1327104\n"
" buffer_load_dwordx4 v242, s[52:55], s76 offen lds\n"
" v_add_u32 v216, 49152, v243\n"
" v_add_u32 v217, 49152, v244\n"
" v_add_u32 v218, 49152, v245\n"
" ds_read_b64_tr_b16 a[64:65], v216 offset:0\n"
" ds_read_b64_tr_b16 a[66:67], v216 offset:6144\n"
" ds_read_b64_tr_b16 a[68:69], v217 offset:0\n"
" ds_read_b64_tr_b16 a[70:71], v217 offset:6144\n"
" ds_read_b64_tr_b16 v[144:145], v218 offset:0\n"
" ds_read_b64_tr_b16 v[146:147], v218 offset:6144\n"
" s_waitcnt lgkmcnt(0)\n"
" ds_write_b128 v229, v[144:147] offset:8192\n"
" s_waitcnt lgkmcnt(0)\n"
" s_waitcnt vmcnt(9)\n"
" s_barrier\n"
" s_mul_i32 s77, s69, 3072\n"
" s_add_i32 m0, s77, 49152\n"
" s_mov_b32 s76, 1437696\n"
" buffer_load_dwordx4 v240, s[52:55], s76 offen lds\n"
" s_add_i32 m0, s77, 50176\n"
" s_mov_b32 s76, 1437696\n"
" buffer_load_dwordx4 v241, s[52:55], s76 offen lds\n"
" s_add_i32 m0, s77, 51200\n"
" s_mov_b32 s76, 1437696\n"
" buffer_load_dwordx4 v242, s[52:55], s76 offen lds\n"
" v_add_u32 v216, 147456, v243\n"
" v_add_u32 v217, 147456, v244\n"
" v_add_u32 v218, 147456, v245\n"
" ds_read_b64_tr_b16 a[72:73], v216 offset:0\n"
" ds_read_b64_tr_b16 a[74:75], v216 offset:6144\n"
" ds_read_b64_tr_b16 a[76:77], v217 offset:0\n"
" ds_read_b64_tr_b16 a[78:79], v217 offset:6144\n"
" ds_read_b64_tr_b16 v[144:145], v218 offset:0\n"
" ds_read_b64_tr_b16 v[146:147], v218 offset:6144\n"
" s_waitcnt lgkmcnt(0)\n"
" ds_write_b128 v229, v[144:147] offset:9216\n"
" s_waitcnt lgkmcnt(0)\n"
" s_waitcnt vmcnt(9)\n"
" s_barrier\n"
" s_mul_i32 s77, s69, 3072\n"
" s_add_i32 m0, s77, 147456\n"
" s_mov_b32 s76, 1548288\n"
" buffer_load_dwordx4 v240, s[52:55], s76 offen lds\n"
" s_add_i32 m0, s77, 148480\n"
" s_mov_b32 s76, 1548288\n"
" buffer_load_dwordx4 v241, s[52:55], s76 offen lds\n"
" s_add_i32 m0, s77, 149504\n"
" s_mov_b32 s76, 1548288\n"
" buffer_load_dwordx4 v242, s[52:55], s76 offen lds\n"
" ds_read_b64_tr_b16 a[80:81], v243 offset:0\n"
" ds_read_b64_tr_b16 a[82:83], v243 offset:6144\n"
" ds_read_b64_tr_b16 a[84:85], v244 offset:0\n"
" ds_read_b64_tr_b16 a[86:87], v244 offset:6144\n"
" ds_read_b64_tr_b16 v[144:145], v245 offset:0\n"
" ds_read_b64_tr_b16 v[146:147], v245 offset:6144\n"
" s_waitcnt lgkmcnt(0)\n"
" ds_write_b128 v229, v[144:147] offset:10240\n"
" s_waitcnt lgkmcnt(0)\n"
" s_waitcnt vmcnt(9)\n"
" s_barrier\n"
" s_mul_i32 s77, s69, 3072\n"
" s_add_i32 m0, s77, 0\n"
" s_mov_b32 s76, 1658880\n"
" buffer_load_dwordx4 v240, s[52:55], s76 offen lds\n"
" s_add_i32 m0, s77, 1024\n"
" s_mov_b32 s76, 1658880\n"
" buffer_load_dwordx4 v241, s[52:55], s76 offen lds\n"
" s_add_i32 m0, s77, 2048\n"
" s_mov_b32 s76, 1658880\n"
" buffer_load_dwordx4 v242, s[52:55], s76 offen lds\n"
" ds_read_b64_tr_b16 a[88:89], v243 offset:16384\n"
" ds_read_b64_tr_b16 a[90:91], v243 offset:22528\n"
" ds_read_b64_tr_b16 a[92:93], v244 offset:16384\n"
" ds_read_b64_tr_b16 a[94:95], v244 offset:22528\n"
" ds_read_b64_tr_b16 v[144:145], v245 offset:16384\n"
" ds_read_b64_tr_b16 v[146:147], v245 offset:22528\n"
" s_waitcnt lgkmcnt(0)\n"
" ds_write_b128 v229, v[144:147] offset:11264\n"
" s_waitcnt lgkmcnt(0)\n"
" s_waitcnt vmcnt(9)\n"
" s_barrier\n"
" s_mul_i32 s77, s69, 3072\n"
" s_add_i32 m0, s77, 16384\n"
" s_mov_b32 s76, 1769472\n"
" buffer_load_dwordx4 v240, s[52:55], s76 offen lds\n"
" s_add_i32 m0, s77, 17408\n"
" s_mov_b32 s76, 1769472\n"
" buffer_load_dwordx4 v241, s[52:55], s76 offen lds\n"
" s_add_i32 m0, s77, 18432\n"
" s_mov_b32 s76, 1769472\n"
" buffer_load_dwordx4 v242, s[52:55], s76 offen lds\n"
" ds_read_b64_tr_b16 a[96:97], v243 offset:32768\n"
" ds_read_b64_tr_b16 a[98:99], v243 offset:38912\n"
" ds_read_b64_tr_b16 a[100:101], v244 offset:32768\n"
" ds_read_b64_tr_b16 a[102:103], v244 offset:38912\n"
" ds_read_b64_tr_b16 v[144:145], v245 offset:32768\n"
" ds_read_b64_tr_b16 v[146:147], v245 offset:38912\n"
" s_waitcnt lgkmcnt(0)\n"
" ds_write_b128 v229, v[144:147] offset:12288\n"
" s_waitcnt lgkmcnt(0)\n"
" s_waitcnt vmcnt(9)\n"
" s_barrier\n"
" s_mul_i32 s77, s69, 3072\n"
" s_add_i32 m0, s77, 32768\n"
" s_mov_b32 s76, 1880064\n"
" buffer_load_dwordx4 v240, s[52:55], s76 offen lds\n"
" s_add_i32 m0, s77, 33792\n"
" s_mov_b32 s76, 1880064\n"
" buffer_load_dwordx4 v241, s[52:55], s76 offen lds\n"
" s_add_i32 m0, s77, 34816\n"
" s_mov_b32 s76, 1880064\n"
" buffer_load_dwordx4 v242, s[52:55], s76 offen lds\n"
" v_add_u32 v216, 49152, v243\n"
" v_add_u32 v217, 49152, v244\n"
" v_add_u32 v218, 49152, v245\n"
" ds_read_b64_tr_b16 a[104:105], v216 offset:0\n"
" ds_read_b64_tr_b16 a[106:107], v216 offset:6144\n"
" ds_read_b64_tr_b16 a[108:109], v217 offset:0\n"
" ds_read_b64_tr_b16 a[110:111], v217 offset:6144\n"
" ds_read_b64_tr_b16 v[144:145], v218 offset:0\n"
" ds_read_b64_tr_b16 v[146:147], v218 offset:6144\n"
" s_waitcnt lgkmcnt(0)\n"
" ds_write_b128 v229, v[144:147] offset:13312\n"
" s_waitcnt lgkmcnt(0)\n"
" s_waitcnt vmcnt(9)\n"
" s_barrier\n"
" s_mul_i32 s77, s69, 3072\n"
" s_add_i32 m0, s77, 49152\n"
" s_mov_b32 s76, 1990656\n"
" buffer_load_dwordx4 v240, s[52:55], s76 offen lds\n"
" s_add_i32 m0, s77, 50176\n"
" s_mov_b32 s76, 1990656\n"
" buffer_load_dwordx4 v241, s[52:55], s76 offen lds\n"
" s_add_i32 m0, s77, 51200\n"
" s_mov_b32 s76, 1990656\n"
" buffer_load_dwordx4 v242, s[52:55], s76 offen lds\n"
" v_add_u32 v216, 147456, v243\n"
" v_add_u32 v217, 147456, v244\n"
" v_add_u32 v218, 147456, v245\n"
" ds_read_b64_tr_b16 a[112:113], v216 offset:0\n"
" ds_read_b64_tr_b16 a[114:115], v216 offset:6144\n"
" ds_read_b64_tr_b16 a[116:117], v217 offset:0\n"
" ds_read_b64_tr_b16 a[118:119], v217 offset:6144\n"
" ds_read_b64_tr_b16 v[144:145], v218 offset:0\n"
" ds_read_b64_tr_b16 v[146:147], v218 offset:6144\n"
" s_waitcnt lgkmcnt(0)\n"
" ds_write_b128 v229, v[144:147] offset:14336\n"
" s_waitcnt lgkmcnt(0)\n"
" s_waitcnt vmcnt(9)\n"
" s_barrier\n"
" s_mul_i32 s77, s69, 3072\n"
" s_add_i32 m0, s77, 147456\n"
" s_mov_b32 s76, 2101248\n"
" buffer_load_dwordx4 v240, s[52:55], s76 offen lds\n"
" s_add_i32 m0, s77, 148480\n"
" s_mov_b32 s76, 2101248\n"
" buffer_load_dwordx4 v241, s[52:55], s76 offen lds\n"
" s_add_i32 m0, s77, 149504\n"
" s_mov_b32 s76, 2101248\n"
" buffer_load_dwordx4 v242, s[52:55], s76 offen lds\n"
" ds_read_b64_tr_b16 a[120:121], v243 offset:0\n"
" ds_read_b64_tr_b16 a[122:123], v243 offset:6144\n"
" ds_read_b64_tr_b16 a[124:125], v244 offset:0\n"
" ds_read_b64_tr_b16 a[126:127], v244 offset:6144\n"
" ds_read_b64_tr_b16 v[144:145], v245 offset:0\n"
" ds_read_b64_tr_b16 v[146:147], v245 offset:6144\n"
" s_waitcnt lgkmcnt(0)\n"
" ds_write_b128 v229, v[144:147] offset:15360\n"
" s_waitcnt lgkmcnt(0)\n"
" s_waitcnt vmcnt(9)\n"
" s_barrier\n"
" ds_read_b64_tr_b16 a[128:129], v243 offset:16384\n"
" ds_read_b64_tr_b16 a[130:131], v243 offset:22528\n"
" ds_read_b64_tr_b16 a[132:133], v244 offset:16384\n"
" ds_read_b64_tr_b16 a[134:135], v244 offset:22528\n"
" ds_read_b64_tr_b16 v[144:145], v245 offset:16384\n"
" ds_read_b64_tr_b16 v[146:147], v245 offset:22528\n"
" s_waitcnt lgkmcnt(0)\n"
" ds_write_b128 v229, v[144:147] offset:16384\n"
" s_waitcnt lgkmcnt(0)\n"
" s_waitcnt vmcnt(6)\n"
" s_barrier\n"
" ds_read_b64_tr_b16 a[136:137], v243 offset:32768\n"
" ds_read_b64_tr_b16 a[138:139], v243 offset:38912\n"
" ds_read_b64_tr_b16 a[140:141], v244 offset:32768\n"
" ds_read_b64_tr_b16 a[142:143], v244 offset:38912\n"
" ds_read_b64_tr_b16 v[144:145], v245 offset:32768\n"
" ds_read_b64_tr_b16 v[146:147], v245 offset:38912\n"
" s_waitcnt lgkmcnt(0)\n"
" ds_write_b128 v229, v[144:147] offset:17408\n"
" s_waitcnt lgkmcnt(0)\n"
" s_waitcnt vmcnt(3)\n"
" s_barrier\n"
" v_add_u32 v216, 49152, v243\n"
" v_add_u32 v217, 49152, v244\n"
" v_add_u32 v218, 49152, v245\n"
" ds_read_b64_tr_b16 a[144:145], v216 offset:0\n"
" ds_read_b64_tr_b16 a[146:147], v216 offset:6144\n"
" ds_read_b64_tr_b16 a[148:149], v217 offset:0\n"
" ds_read_b64_tr_b16 a[150:151], v217 offset:6144\n"
" ds_read_b64_tr_b16 v[144:145], v218 offset:0\n"
" ds_read_b64_tr_b16 v[146:147], v218 offset:6144\n"
" s_waitcnt lgkmcnt(0)\n"
" ds_write_b128 v229, v[144:147] offset:18432\n"
" s_waitcnt lgkmcnt(0)\n"
" s_waitcnt vmcnt(0)\n"
" s_barrier\n"
" v_add_u32 v216, 147456, v243\n"
" v_add_u32 v217, 147456, v244\n"
" v_add_u32 v218, 147456, v245\n"
" ds_read_b64_tr_b16 a[152:153], v216 offset:0\n"
" ds_read_b64_tr_b16 a[154:155], v216 offset:6144\n"
" ds_read_b64_tr_b16 a[156:157], v217 offset:0\n"
" ds_read_b64_tr_b16 a[158:159], v217 offset:6144\n"
" ds_read_b64_tr_b16 v[144:145], v218 offset:0\n"
" ds_read_b64_tr_b16 v[146:147], v218 offset:6144\n"
" s_waitcnt lgkmcnt(0)\n"
" ds_write_b128 v229, v[144:147] offset:19456\n"
" s_waitcnt lgkmcnt(0)\n"
" s_waitcnt vmcnt(0)\n"
" s_barrier\n"
" s_lshl_b32 s77, s69, 10\n"
" v_mov_b32 v240, 61440\n"
" v_mov_b32 v216, 0\n"
" ds_write_b32 v240, v216\n"
" s_waitcnt lgkmcnt(0)\n"
" s_barrier\n"
" s_mov_b32 s95, -1\n"
" s_mov_b32 s68, 0\n"
" s_mov_b32 s94, 0\n"
" s_mov_b32 s91, 0\n"
".Lff_pol_%=:\n"
" buffer_load_dwordx2 v[220:221], v235, s[48:51], 0 offen sc1\n"
" s_waitcnt vmcnt(0)\n"
" v_or_b32 v222, 1, v220\n"
" v_mov_b32 v223, v221\n"
" v_cmp_ne_u64 vcc, s[16:17], v[222:223]\n"
" s_nop 0\n"
" s_cbranch_vccz .Lff_polok_%=\n"
" s_add_i32 s91, s91, 1\n"
" s_or_b32 s91, s91, s90\n"
" s_cmp_ge_u32 s91, 65536\n"
" s_cselect_b32 s90, 65536, 0\n"
" s_cbranch_scc1 .Lff_polok_%=\n"
" s_sleep 4\n"
" s_branch .Lff_pol_%=\n"
".Lff_polok_%=:\n"
" v_and_b32 v220, 1, v220\n"
" v_cmp_ne_u32_e64 s[86:87], 1, v220\n"
" s_nop 1\n"
" s_bitcmp1_b32 s86, 0\n"
" s_cselect_b32 s76, 8192, 0\n"
" s_add_u32 s48, s48, s76\n"
".Lff_loop_%=:\n"
" s_cmp_lg_u32 s94, 0\n"
" s_cbranch_scc1 .Lff_have_%=\n"
" s_mov_b32 s91, 0\n"
" buffer_load_dword v235, v227, s[48:51], 0 offen sc1\n"
" s_waitcnt vmcnt(0)\n"
" v_mov_b32 v242, 1\n"
" s_mov_b64 exec, 1\n"
" ds_add_rtn_u32 v243, v240, v242\n"
" s_mov_b64 exec, -1\n"
" s_waitcnt lgkmcnt(0)\n"
" v_readfirstlane_b32 s76, v243\n"
" s_nop 0\n"
" s_and_b32 s76, s76, 3\n"
" s_cmp_lg_u32 s76, 3\n"
" s_cbranch_scc1 .Lff_lf0_%=\n"
" v_mov_b32 v242, s95\n"
" buffer_store_dword v242, v228, s[48:51], 0 offen sc1\n"
".Lff_lf0_%=:\n"
" s_nop 3\n"
".Lff_in_%=:\n"
" v_cmp_gt_i32 vcc, s68, v235\n"
" s_nop 0\n"
" s_cbranch_vccz .Lff_inok_%=\n"
" s_add_i32 s91, s91, 1\n"
" s_or_b32 s91, s91, s90\n"
" s_cmp_ge_u32 s91, 65536\n"
" s_cselect_b32 s90, 65536, 0\n"
" s_cbranch_scc1 .Lff_inok_%=\n"
" s_sleep 2\n"
" buffer_load_dword v235, v227, s[48:51], 0 offen sc1\n"
" s_waitcnt vmcnt(0)\n"
" s_branch .Lff_in_%=\n"
".Lff_inok_%=:\n"
" s_mov_b32 s38, 134217728\n"
" s_and_b32 s92, s68, 15\n"
" s_lshl_b32 s92, s92, 17\n"
" s_add_i32 s92, s92, s84\n"
" s_lshr_b32 s76, s92, 21\n"
" s_bitcmp1_b64 s[86:87], s76\n"
" s_cselect_b32 s76, 8192, 0\n"
" s_add_i32 s92, s92, s76\n"
" s_lshl_b32 s79, s69, 8\n"
" s_add_i32 s92, s92, s79\n"
" s_add_i32 m0, s77, 0\n"
" s_add_i32 s76, s92, 0\n"
" buffer_load_dwordx4 v226, s[36:39], s76 offen sc1 lds\n"
" s_add_i32 m0, s77, 4096\n"
" s_add_i32 s76, s92, 1024\n"
" buffer_load_dwordx4 v226, s[36:39], s76 offen sc1 lds\n"
" s_add_i32 m0, s77, 8192\n"
" s_add_i32 s76, s92, 2048\n"
" buffer_load_dwordx4 v226, s[36:39], s76 offen sc1 lds\n"
" s_add_i32 m0, s77, 12288\n"
" s_add_i32 s76, s92, 3072\n"
" buffer_load_dwordx4 v226, s[36:39], s76 offen sc1 lds\n"
" s_add_i32 m0, s77, 16384\n"
" s_add_i32 s76, s92, 4096\n"
" buffer_load_dwordx4 v226, s[36:39], s76 offen sc1 lds\n"
" s_add_i32 m0, s77, 20480\n"
" s_add_i32 s76, s92, 65536\n"
" buffer_load_dwordx4 v226, s[36:39], s76 offen sc1 lds\n"
" s_add_i32 m0, s77, 24576\n"
" s_add_i32 s76, s92, 66560\n"
" buffer_load_dwordx4 v226, s[36:39], s76 offen sc1 lds\n"
" s_add_i32 m0, s77, 28672\n"
" s_add_i32 s76, s92, 67584\n"
" buffer_load_dwordx4 v226, s[36:39], s76 offen sc1 lds\n"
" s_add_i32 m0, s77, 32768\n"
" s_add_i32 s76, s92, 68608\n"
" buffer_load_dwordx4 v226, s[36:39], s76 offen sc1 lds\n"
" s_add_i32 m0, s77, 36864\n"
" s_add_i32 s76, s92, 69632\n"
" buffer_load_dwordx4 v226, s[36:39], s76 offen sc1 lds\n"
".Lff_have_%=:\n"
" s_waitcnt vmcnt(9)\n"
".Lff_go_%=:\n"
" v_xor_b32 v227, 0x80, v227\n"
" s_and_b32 s93, s68, 15\n"
" s_lshl_b32 s93, s93, 21\n"
" s_add_i32 s93, s93, s85\n"
" s_lshr_b32 s76, s93, 21\n"
" s_bitcmp1_b64 s[86:87], s76\n"
" s_cselect_b32 s76, 8192, 0\n"
" s_add_i32 s93, s93, s76\n"
" s_add_i32 s78, s68, 1\n"
" s_and_b32 s92, s78, 15\n"
" s_lshl_b32 s92, s92, 17\n"
" s_add_i32 s92, s92, s84\n"
" s_lshr_b32 s76, s92, 21\n"
" s_bitcmp1_b64 s[86:87], s76\n"
" s_cselect_b32 s76, 8192, 0\n"
" s_add_i32 s92, s92, s76\n"
" s_lshl_b32 s79, s69, 8\n"
" s_add_i32 s92, s92, s79\n"
" ds_read_b128 v[160:163], v229 offset:0\n"
" ds_read_b128 v[164:167], v229 offset:1024\n"
" ds_read_b128 v[168:171], v229 offset:2048\n"
" s_barrier\n"
" ds_read_b128 v[144:147], v225 offset:0\n"
" ds_read_b128 v[148:151], v225 offset:1024\n"
" ds_read_b128 v[152:155], v225 offset:2048\n"
" s_waitcnt lgkmcnt(2)\n"
" v_mfma_f32_16x16x32_bf16 v[176:179], a[0:3], v[144:147], 0\n"
" ds_read_b128 v[156:159], v225 offset:3072\n"
" v_mfma_f32_16x16x32_bf16 v[180:183], a[4:7], v[144:147], 0\n"
" ds_read_b128 v[172:175], v229 offset:3072\n"
" v_mfma_f32_16x16x32_bf16 v[184:187], v[160:163], v[144:147], 0\n"
" s_waitcnt vmcnt(7)\n"
" s_barrier\n"
" s_waitcnt lgkmcnt(3)\n"
" v_mfma_f32_16x16x32_bf16 v[176:179], a[8:11], v[148:151], v[176:179]\n"
" ds_read_b128 v[144:147], v225 offset:4096\n"
" v_mfma_f32_16x16x32_bf16 v[180:183], a[12:15], v[148:151], v[180:183]\n"
" ds_read_b128 v[160:163], v229 offset:4096\n"
" v_mfma_f32_16x16x32_bf16 v[184:187], v[164:167], v[148:151], v[184:187]\n"
" s_waitcnt lgkmcnt(4)\n"
" v_mfma_f32_16x16x32_bf16 v[176:179], a[16:19], v[152:155], v[176:179]\n"
" ds_read_b128 v[148:151], v225 offset:5120\n"
" v_mfma_f32_16x16x32_bf16 v[180:183], a[20:23], v[152:155], v[180:183]\n"
" ds_read_b128 v[164:167], v229 offset:5120\n"
" v_mfma_f32_16x16x32_bf16 v[184:187], v[168:171], v[152:155], v[184:187]\n"
" s_waitcnt lgkmcnt(5)\n"
" v_mfma_f32_16x16x32_bf16 v[176:179], a[24:27], v[156:159], v[176:179]\n"
" ds_read_b128 v[152:155], v225 offset:6144\n"
" v_mfma_f32_16x16x32_bf16 v[180:183], a[28:31], v[156:159], v[180:183]\n"
" ds_read_b128 v[168:171], v229 offset:6144\n"
" s_waitcnt lgkmcnt(6)\n"
" v_mfma_f32_16x16x32_bf16 v[184:187], v[172:175], v[156:159], v[184:187]\n"
" s_waitcnt lgkmcnt(5)\n"
" v_mfma_f32_16x16x32_bf16 v[176:179], a[32:35], v[144:147], v[176:179]\n"
" ds_read_b128 v[156:159], v225 offset:7168\n"
" v_mfma_f32_16x16x32_bf16 v[180:183], a[36:39], v[144:147], v[180:183]\n"
" ds_read_b128 v[172:175], v229 offset:7168\n"
" s_waitcnt lgkmcnt(6)\n"
" v_mfma_f32_16x16x32_bf16 v[184:187], v[160:163], v[144:147], v[184:187]\n"
" s_waitcnt lgkmcnt(5)\n"
" v_mfma_f32_16x16x32_bf16 v[176:179], a[40:43], v[148:151], v[176:179]\n"
" ds_read_b128 v[144:147], v225 offset:8192\n"
" v_mfma_f32_16x16x32_bf16 v[180:183], a[44:47], v[148:151], v[180:183]\n"
" ds_read_b128 v[160:163], v229 offset:8192\n"
" s_waitcnt lgkmcnt(6)\n"
" v_mfma_f32_16x16x32_bf16 v[184:187], v[164:167], v[148:151], v[184:187]\n"
" s_waitcnt lgkmcnt(5)\n"
" v_mfma_f32_16x16x32_bf16 v[176:179], a[48:51], v[152:155], v[176:179]\n"
" ds_read_b128 v[148:151], v225 offset:9216\n"
" v_mfma_f32_16x16x32_bf16 v[180:183], a[52:55], v[152:155], v[180:183]\n"
" ds_read_b128 v[164:167], v229 offset:9216\n"
" s_waitcnt lgkmcnt(6)\n"
" v_mfma_f32_16x16x32_bf16 v[184:187], v[168:171], v[152:155], v[184:187]\n"
" s_waitcnt lgkmcnt(5)\n"
" v_mfma_f32_16x16x32_bf16 v[176:179], a[56:59], v[156:159], v[176:179]\n"
" ds_read_b128 v[152:155], v225 offset:10240\n"
" v_mfma_f32_16x16x32_bf16 v[180:183], a[60:63], v[156:159], v[180:183]\n"
" ds_read_b128 v[168:171], v229 offset:10240\n"
" s_waitcnt lgkmcnt(6)\n"
" v_mfma_f32_16x16x32_bf16 v[184:187], v[172:175], v[156:159], v[184:187]\n"
" s_waitcnt vmcnt(4)\n"
" s_barrier\n"
" s_waitcnt lgkmcnt(5)\n"
" v_mfma_f32_16x16x32_bf16 v[176:179], a[64:67], v[144:147], v[176:179]\n"
" ds_read_b128 v[156:159], v225 offset:11264\n"
" v_mfma_f32_16x16x32_bf16 v[180:183], a[68:71], v[144:147], v[180:183]\n"
" ds_read_b128 v[172:175], v229 offset:11264\n"
" s_waitcnt lgkmcnt(6)\n"
" v_mfma_f32_16x16x32_bf16 v[184:187], v[160:163], v[144:147], v[184:187]\n"
" s_waitcnt lgkmcnt(5)\n"
" v_mfma_f32_16x16x32_bf16 v[176:179], a[72:75], v[148:151], v[176:179]\n"
" ds_read_b128 v[144:147], v225 offset:12288\n"
" v_mfma_f32_16x16x32_bf16 v[180:183], a[76:79], v[148:151], v[180:183]\n"
" ds_read_b128 v[160:163], v229 offset:12288\n"
" s_waitcnt lgkmcnt(6)\n"
" v_mfma_f32_16x16x32_bf16 v[184:187], v[164:167], v[148:151], v[184:187]\n"
" s_waitcnt lgkmcnt(5)\n"
" v_mfma_f32_16x16x32_bf16 v[176:179], a[80:83], v[152:155], v[176:179]\n"
" ds_read_b128 v[148:151], v225 offset:13312\n"
" v_mfma_f32_16x16x32_bf16 v[180:183], a[84:87], v[152:155], v[180:183]\n"
" ds_read_b128 v[164:167], v229 offset:13312\n"
" s_waitcnt lgkmcnt(6)\n"
" v_mfma_f32_16x16x32_bf16 v[184:187], v[168:171], v[152:155], v[184:187]\n"
" s_waitcnt lgkmcnt(5)\n"
" v_mfma_f32_16x16x32_bf16 v[176:179], a[88:91], v[156:159], v[176:179]\n"
" ds_read_b128 v[152:155], v225 offset:14336\n"
" v_mfma_f32_16x16x32_bf16 v[180:183], a[92:95], v[156:159], v[180:183]\n"
" ds_read_b128 v[168:171], v229 offset:14336\n"
" s_waitcnt lgkmcnt(6)\n"
" v_mfma_f32_16x16x32_bf16 v[184:187], v[172:175], v[156:159], v[184:187]\n"
" s_waitcnt lgkmcnt(5)\n"
" v_mfma_f32_16x16x32_bf16 v[176:179], a[96:99], v[144:147], v[176:179]\n"
" ds_read_b128 v[156:159], v225 offset:15360\n"
" v_mfma_f32_16x16x32_bf16 v[180:183], a[100:103], v[144:147], v[180:183]\n"
" ds_read_b128 v[172:175], v229 offset:15360\n"
" s_waitcnt lgkmcnt(6)\n"
" v_mfma_f32_16x16x32_bf16 v[184:187], v[160:163], v[144:147], v[184:187]\n"
" s_waitcnt lgkmcnt(5)\n"
" v_mfma_f32_16x16x32_bf16 v[176:179], a[104:107], v[148:151], v[176:179]\n"
" ds_read_b128 v[144:147], v225 offset:16384\n"
" v_mfma_f32_16x16x32_bf16 v[180:183], a[108:111], v[148:151], v[180:183]\n"
" ds_read_b128 v[160:163], v229 offset:16384\n"
" s_waitcnt lgkmcnt(6)\n"
" v_mfma_f32_16x16x32_bf16 v[184:187], v[164:167], v[148:151], v[184:187]\n"
" s_waitcnt lgkmcnt(5)\n"
" v_mfma_f32_16x16x32_bf16 v[176:179], a[112:115], v[152:155], v[176:179]\n"
" ds_read_b128 v[148:151], v225 offset:17408\n"
" v_mfma_f32_16x16x32_bf16 v[180:183], a[116:119], v[152:155], v[180:183]\n"
" ds_read_b128 v[164:167], v229 offset:17408\n"
" s_waitcnt lgkmcnt(6)\n"
" v_mfma_f32_16x16x32_bf16 v[184:187], v[168:171], v[152:155], v[184:187]\n"
" s_waitcnt lgkmcnt(5)\n"
" v_mfma_f32_16x16x32_bf16 v[176:179], a[120:123], v[156:159], v[176:179]\n"
" ds_read_b128 v[152:155], v225 offset:18432\n"
" v_mfma_f32_16x16x32_bf16 v[180:183], a[124:127], v[156:159], v[180:183]\n"
" ds_read_b128 v[168:171], v229 offset:18432\n"
" s_waitcnt lgkmcnt(6)\n"
" v_mfma_f32_16x16x32_bf16 v[184:187], v[172:175], v[156:159], v[184:187]\n"
" s_waitcnt lgkmcnt(5)\n"
" v_mfma_f32_16x16x32_bf16 v[176:179], a[128:131], v[144:147], v[176:179]\n"
" ds_read_b128 v[156:159], v225 offset:19456\n"
" v_mfma_f32_16x16x32_bf16 v[180:183], a[132:135], v[144:147], v[180:183]\n"
" ds_read_b128 v[172:175], v229 offset:19456\n"
" s_waitcnt lgkmcnt(6)\n"
" v_mfma_f32_16x16x32_bf16 v[184:187], v[160:163], v[144:147], v[184:187]\n"
" s_waitcnt lgkmcnt(5)\n"
" v_mfma_f32_16x16x32_bf16 v[176:179], a[136:139], v[148:151], v[176:179]\n"
" ds_read_b128 v[144:147], v225 offset:20480\n"
" v_mfma_f32_16x16x32_bf16 v[180:183], a[140:143], v[148:151], v[180:183]\n"
" ds_read_b128 v[160:163], v229 offset:0\n"
" s_waitcnt lgkmcnt(6)\n"
" v_mfma_f32_16x16x32_bf16 v[184:187], v[164:167], v[148:151], v[184:187]\n"
" s_waitcnt vmcnt(0)\n"
" s_barrier\n"
" s_waitcnt lgkmcnt(5)\n"
" v_mfma_f32_16x16x32_bf16 v[176:179], a[144:147], v[152:155], v[176:179]\n"
" ds_read_b128 v[148:151], v225 offset:21504\n"
" v_mfma_f32_16x16x32_bf16 v[180:183], a[148:151], v[152:155], v[180:183]\n"
" ds_read_b128 v[164:167], v229 offset:1024\n"
" s_waitcnt lgkmcnt(6)\n"
" v_mfma_f32_16x16x32_bf16 v[184:187], v[168:171], v[152:155], v[184:187]\n"
" s_waitcnt lgkmcnt(5)\n"
" v_mfma_f32_16x16x32_bf16 v[176:179], a[152:155], v[156:159], v[176:179]\n"
" ds_read_b128 v[152:155], v225 offset:22528\n"
" v_mfma_f32_16x16x32_bf16 v[180:183], a[156:159], v[156:159], v[180:183]\n"
" ds_read_b128 v[168:171], v229 offset:2048\n"
" s_waitcnt lgkmcnt(6)\n"
" v_mfma_f32_16x16x32_bf16 v[184:187], v[172:175], v[156:159], v[184:187]\n"
" s_cmp_lg_u32 s94, 1\n"
" s_cbranch_scc1 .Lff_nof_%=\n"
" v_mov_b32 v242, 1\n"
" s_mov_b64 exec, 1\n"
" ds_add_rtn_u32 v243, v240, v242\n"
" s_mov_b64 exec, -1\n"
" s_waitcnt lgkmcnt(0)\n"
" v_readfirstlane_b32 s76, v243\n"
" s_nop 0\n"
" s_and_b32 s76, s76, 3\n"
" s_cmp_lg_u32 s76, 3\n"
" s_cbranch_scc1 .Lff_lf1_%=\n"
" v_mov_b32 v242, s95\n"
" buffer_store_dword v242, v228, s[48:51], 0 offen sc1\n"
".Lff_lf1_%=:\n"
" s_nop 3\n"
".Lff_nof_%=:\n"
" s_waitcnt lgkmcnt(5)\n"
" v_mfma_f32_16x16x32_bf16 v[188:191], a[0:3], v[144:147], 0\n"
" ds_read_b128 v[156:159], v225 offset:23552\n"
" v_mfma_f32_16x16x32_bf16 v[192:195], a[4:7], v[144:147], 0\n"
" ds_read_b128 v[172:175], v229 offset:3072\n"
" s_waitcnt lgkmcnt(6)\n"
" v_mfma_f32_16x16x32_bf16 v[196:199], v[160:163], v[144:147], 0\n"
" s_waitcnt lgkmcnt(5)\n"
" v_mfma_f32_16x16x32_bf16 v[188:191], a[8:11], v[148:151], v[188:191]\n"
" ds_read_b128 v[144:147], v225 offset:24576\n"
" v_mfma_f32_16x16x32_bf16 v[192:195], a[12:15], v[148:151], v[192:195]\n"
" ds_read_b128 v[160:163], v229 offset:4096\n"
" s_waitcnt lgkmcnt(6)\n"
" v_mfma_f32_16x16x32_bf16 v[196:199], v[164:167], v[148:151], v[196:199]\n"
" s_waitcnt lgkmcnt(5)\n"
" v_mfma_f32_16x16x32_bf16 v[188:191], a[16:19], v[152:155], v[188:191]\n"
" ds_read_b128 v[148:151], v225 offset:25600\n"
" v_mfma_f32_16x16x32_bf16 v[192:195], a[20:23], v[152:155], v[192:195]\n"
" ds_read_b128 v[164:167], v229 offset:5120\n"
" s_waitcnt lgkmcnt(6)\n"
" v_mfma_f32_16x16x32_bf16 v[196:199], v[168:171], v[152:155], v[196:199]\n"
" v_cvt_pk_bf16_f32 v236, v176, v177\n"
" v_cvt_pk_bf16_f32 v237, v178, v179\n"
" s_waitcnt lgkmcnt(5)\n"
" v_mfma_f32_16x16x32_bf16 v[188:191], a[24:27], v[156:159], v[188:191]\n"
" ds_read_b128 v[152:155], v225 offset:26624\n"
" v_mfma_f32_16x16x32_bf16 v[192:195], a[28:31], v[156:159], v[192:195]\n"
" ds_read_b128 v[168:171], v229 offset:6144\n"
" s_waitcnt lgkmcnt(6)\n"
" v_mfma_f32_16x16x32_bf16 v[196:199], v[172:175], v[156:159], v[196:199]\n"
" v_pk_max_i16 v236, v236, 0\n"
" v_pk_max_i16 v237, v237, 0\n"
" s_waitcnt lgkmcnt(5)\n"
" v_mfma_f32_16x16x32_bf16 v[188:191], a[32:35], v[144:147], v[188:191]\n"
" ds_read_b128 v[156:159], v225 offset:27648\n"
" v_mfma_f32_16x16x32_bf16 v[192:195], a[36:39], v[144:147], v[192:195]\n"
" ds_read_b128 v[172:175], v229 offset:7168\n"
" s_waitcnt lgkmcnt(6)\n"
" v_mfma_f32_16x16x32_bf16 v[196:199], v[160:163], v[144:147], v[196:199]\n"
" ds_write_b64 v231, v[236:237] offset:0\n"
" s_waitcnt lgkmcnt(6)\n"
" v_mfma_f32_16x16x32_bf16 v[188:191], a[40:43], v[148:151], v[188:191]\n"
" ds_read_b128 v[144:147], v225 offset:28672\n"
" v_mfma_f32_16x16x32_bf16 v[192:195], a[44:47], v[148:151], v[192:195]\n"
" ds_read_b128 v[160:163], v229 offset:8192\n"
" s_waitcnt lgkmcnt(7)\n"
" v_mfma_f32_16x16x32_bf16 v[196:199], v[164:167], v[148:151], v[196:199]\n"
" v_cvt_pk_bf16_f32 v238, v180, v181\n"
" v_cvt_pk_bf16_f32 v239, v182, v183\n"
" s_waitcnt lgkmcnt(6)\n"
" v_mfma_f32_16x16x32_bf16 v[188:191], a[48:51], v[152:155], v[188:191]\n"
" ds_read_b128 v[148:151], v225 offset:29696\n"
" v_mfma_f32_16x16x32_bf16 v[192:195], a[52:55], v[152:155], v[192:195]\n"
" ds_read_b128 v[164:167], v229 offset:9216\n"
" s_waitcnt lgkmcnt(7)\n"
" v_mfma_f32_16x16x32_bf16 v[196:199], v[168:171], v[152:155], v[196:199]\n"
" v_pk_max_i16 v238, v238, 0\n"
" v_pk_max_i16 v239, v239, 0\n"
" s_waitcnt lgkmcnt(6)\n"
" v_mfma_f32_16x16x32_bf16 v[188:191], a[56:59], v[156:159], v[188:191]\n"
" ds_read_b128 v[152:155], v225 offset:30720\n"
" v_mfma_f32_16x16x32_bf16 v[192:195], a[60:63], v[156:159], v[192:195]\n"
" ds_read_b128 v[168:171], v229 offset:10240\n"
" s_waitcnt lgkmcnt(7)\n"
" v_mfma_f32_16x16x32_bf16 v[196:199], v[172:175], v[156:159], v[196:199]\n"
" ds_write_b64 v232, v[238:239] offset:0\n"
" s_waitcnt lgkmcnt(6)\n"
" v_mfma_f32_16x16x32_bf16 v[188:191], a[64:67], v[144:147], v[188:191]\n"
" ds_read_b128 v[156:159], v225 offset:31744\n"
" v_mfma_f32_16x16x32_bf16 v[192:195], a[68:71], v[144:147], v[192:195]\n"
" ds_read_b128 v[172:175], v229 offset:11264\n"
" s_waitcnt lgkmcnt(7)\n"
" v_mfma_f32_16x16x32_bf16 v[196:199], v[160:163], v[144:147], v[196:199]\n"
" v_cvt_pk_bf16_f32 v236, v184, v185\n"
" v_cvt_pk_bf16_f32 v237, v186, v187\n"
" s_waitcnt lgkmcnt(6)\n"
" v_mfma_f32_16x16x32_bf16 v[188:191], a[72:75], v[148:151], v[188:191]\n"
" ds_read_b128 v[144:147], v225 offset:32768\n"
" v_mfma_f32_16x16x32_bf16 v[192:195], a[76:79], v[148:151], v[192:195]\n"
" ds_read_b128 v[160:163], v229 offset:12288\n"
" s_waitcnt lgkmcnt(7)\n"
" v_mfma_f32_16x16x32_bf16 v[196:199], v[164:167], v[148:151], v[196:199]\n"
" v_pk_max_i16 v236, v236, 0\n"
" v_pk_max_i16 v237, v237, 0\n"
" s_waitcnt lgkmcnt(6)\n"
" v_mfma_f32_16x16x32_bf16 v[188:191], a[80:83], v[152:155], v[188:191]\n"
" ds_read_b128 v[148:151], v225 offset:33792\n"
" v_mfma_f32_16x16x32_bf16 v[192:195], a[84:87], v[152:155], v[192:195]\n"
" ds_read_b128 v[164:167], v229 offset:13312\n"
" s_waitcnt lgkmcnt(7)\n"
" v_mfma_f32_16x16x32_bf16 v[196:199], v[168:171], v[152:155], v[196:199]\n"
" ds_write_b64 v233, v[236:237] offset:0\n"
" s_waitcnt lgkmcnt(6)\n"
" v_mfma_f32_16x16x32_bf16 v[188:191], a[88:91], v[156:159], v[188:191]\n"
" ds_read_b128 v[152:155], v225 offset:34816\n"
" v_mfma_f32_16x16x32_bf16 v[192:195], a[92:95], v[156:159], v[192:195]\n"
" ds_read_b128 v[168:171], v229 offset:14336\n"
" s_waitcnt lgkmcnt(7)\n"
" v_mfma_f32_16x16x32_bf16 v[196:199], v[172:175], v[156:159], v[196:199]\n"
" s_waitcnt lgkmcnt(6)\n"
" v_mfma_f32_16x16x32_bf16 v[188:191], a[96:99], v[144:147], v[188:191]\n"
" ds_read_b128 v[156:159], v225 offset:35840\n"
" v_mfma_f32_16x16x32_bf16 v[192:195], a[100:103], v[144:147], v[192:195]\n"
" ds_read_b128 v[172:175], v229 offset:15360\n"
" s_waitcnt lgkmcnt(7)\n"
" v_mfma_f32_16x16x32_bf16 v[196:199], v[160:163], v[144:147], v[196:199]\n"
" s_waitcnt lgkmcnt(4)\n"
" s_barrier\n"
" v_mfma_f32_16x16x32_bf16 v[188:191], a[104:107], v[148:151], v[188:191]\n"
" ds_read_b128 v[144:147], v225 offset:36864\n"
" v_mfma_f32_16x16x32_bf16 v[192:195], a[108:111], v[148:151], v[192:195]\n"
" ds_read_b128 v[160:163], v229 offset:16384\n"
" v_mfma_f32_16x16x32_bf16 v[196:199], v[164:167], v[148:151], v[196:199]\n"
" ds_read_b128 v[200:203], v225 offset:40960\n"
" s_waitcnt lgkmcnt(6)\n"
" v_mfma_f32_16x16x32_bf16 v[188:191], a[112:115], v[152:155], v[188:191]\n"
" ds_read_b128 v[148:151], v225 offset:37888\n"
" v_mfma_f32_16x16x32_bf16 v[192:195], a[116:119], v[152:155], v[192:195]\n"
" ds_read_b128 v[164:167], v229 offset:17408\n"
" s_waitcnt lgkmcnt(7)\n"
" v_mfma_f32_16x16x32_bf16 v[196:199], v[168:171], v[152:155], v[196:199]\n"
" ds_read_b128 v[204:207], v225 offset:41984\n"
" s_waitcnt lgkmcnt(7)\n"
" v_mfma_f32_16x16x32_bf16 v[188:191], a[120:123], v[156:159], v[188:191]\n"
" ds_read_b128 v[152:155], v225 offset:38912\n"
" v_mfma_f32_16x16x32_bf16 v[192:195], a[124:127], v[156:159], v[192:195]\n"
" ds_read_b128 v[168:171], v229 offset:18432\n"
" s_waitcnt lgkmcnt(8)\n"
" v_mfma_f32_16x16x32_bf16 v[196:199], v[172:175], v[156:159], v[196:199]\n"
" ds_read_b128 v[208:211], v225 offset:43008\n"
" s_waitcnt lgkmcnt(8)\n"
" v_mfma_f32_16x16x32_bf16 v[188:191], a[128:131], v[144:147], v[188:191]\n"
" ds_read_b128 v[156:159], v225 offset:39936\n"
" v_mfma_f32_16x16x32_bf16 v[192:195], a[132:135], v[144:147], v[192:195]\n"
" ds_read_b128 v[172:175], v229 offset:19456\n"
" s_waitcnt lgkmcnt(9)\n"
" v_mfma_f32_16x16x32_bf16 v[196:199], v[160:163], v[144:147], v[196:199]\n"
" ds_read_b128 v[212:215], v225 offset:44032\n"
" s_waitcnt lgkmcnt(8)\n"
" v_mfma_f32_16x16x32_bf16 v[188:191], a[136:139], v[148:151], v[188:191]\n"
" v_mfma_f32_16x16x32_bf16 v[192:195], a[140:143], v[148:151], v[192:195]\n"
" s_waitcnt lgkmcnt(7)\n"
" v_mfma_f32_16x16x32_bf16 v[196:199], v[164:167], v[148:151], v[196:199]\n"
" ds_read_b128 v[216:219], v225 offset:45056\n"
" s_waitcnt lgkmcnt(6)\n"
" v_mfma_f32_16x16x32_bf16 v[188:191], a[144:147], v[152:155], v[188:191]\n"
" v_mfma_f32_16x16x32_bf16 v[192:195], a[148:151], v[152:155], v[192:195]\n"
" s_waitcnt lgkmcnt(5)\n"
" v_mfma_f32_16x16x32_bf16 v[196:199], v[168:171], v[152:155], v[196:199]\n"
" ds_read_b128 v[220:223], v225 offset:46080\n"
" s_waitcnt lgkmcnt(4)\n"
" v_mfma_f32_16x16x32_bf16 v[188:191], a[152:155], v[156:159], v[188:191]\n"
" v_mfma_f32_16x16x32_bf16 v[192:195], a[156:159], v[156:159], v[192:195]\n"
" s_waitcnt lgkmcnt(3)\n"
" v_mfma_f32_16x16x32_bf16 v[196:199], v[172:175], v[156:159], v[196:199]\n"
" v_mfma_f32_16x16x32_bf16 v[168:171], a[160:163], v[200:203], 0\n"
" v_mfma_f32_16x16x32_bf16 v[172:175], a[184:187], v[200:203], 0\n"
" v_mfma_f32_16x16x32_bf16 v[168:171], a[164:167], v[204:207], v[168:171]\n"
" v_mfma_f32_16x16x32_bf16 v[172:175], a[188:191], v[204:207], v[172:175]\n"
" s_nop 0\n"
" v_cvt_pk_bf16_f32 v236, v188, v189\n"
" v_cvt_pk_bf16_f32 v237, v190, v191\n"
" v_mfma_f32_16x16x32_bf16 v[168:171], a[168:171], v[208:211], v[168:171]\n"
" v_pk_max_i16 v236, v236, 0\n"
" v_pk_max_i16 v237, v237, 0\n"
" v_mfma_f32_16x16x32_bf16 v[172:175], a[192:195], v[208:211], v[172:175]\n"
" ds_write_b64 v231, v[236:237] offset:6144\n"
" s_waitcnt lgkmcnt(3)\n"
" v_mfma_f32_16x16x32_bf16 v[168:171], a[172:175], v[212:215], v[168:171]\n"
" v_cvt_pk_bf16_f32 v238, v192, v193\n"
" v_cvt_pk_bf16_f32 v239, v194, v195\n"
" v_mfma_f32_16x16x32_bf16 v[172:175], a[196:199], v[212:215], v[172:175]\n"
" v_pk_max_i16 v238, v238, 0\n"
" v_pk_max_i16 v239, v239, 0\n"
" s_waitcnt lgkmcnt(2)\n"
" v_mfma_f32_16x16x32_bf16 v[168:171], a[176:179], v[216:219], v[168:171]\n"
" ds_write_b64 v232, v[238:239] offset:6144\n"
" v_mfma_f32_16x16x32_bf16 v[172:175], a[200:203], v[216:219], v[172:175]\n"
" v_cvt_pk_bf16_f32 v236, v196, v197\n"
" v_cvt_pk_bf16_f32 v237, v198, v199\n"
" s_waitcnt lgkmcnt(2)\n"
" v_mfma_f32_16x16x32_bf16 v[168:171], a[180:183], v[220:223], v[168:171]\n"
" v_pk_max_i16 v236, v236, 0\n"
" v_pk_max_i16 v237, v237, 0\n"
" v_mfma_f32_16x16x32_bf16 v[172:175], a[204:207], v[220:223], v[172:175]\n"
" ds_write_b64 v233, v[236:237] offset:6144\n"
" v_mfma_f32_16x16x32_bf16 v[176:179], a[208:211], v[200:203], 0\n"
" v_mfma_f32_16x16x32_bf16 v[180:183], a[232:235], v[200:203], 0\n"
" v_mfma_f32_16x16x32_bf16 v[176:179], a[212:215], v[204:207], v[176:179]\n"
" v_mfma_f32_16x16x32_bf16 v[180:183], a[236:239], v[204:207], v[180:183]\n"
" s_waitcnt lgkmcnt(0)\n"
" s_barrier\n"
" v_mfma_f32_16x16x32_bf16 v[176:179], a[216:219], v[208:211], v[176:179]\n"
" v_mfma_f32_16x16x32_bf16 v[180:183], a[240:243], v[208:211], v[180:183]\n"
" v_mfma_f32_16x16x32_bf16 v[176:179], a[220:223], v[212:215], v[176:179]\n"
" v_cvt_pk_bf16_f32 v184, v168, v169\n"
" v_cvt_pk_bf16_f32 v185, v170, v171\n"
" v_mfma_f32_16x16x32_bf16 v[180:183], a[244:247], v[212:215], v[180:183]\n"
" s_add_i32 s79, s93, 0\n"
" v_cvt_pk_bf16_f32 v186, v172, v173\n"
" v_cvt_pk_bf16_f32 v187, v174, v175\n"
" v_mfma_f32_16x16x32_bf16 v[176:179], a[224:227], v[216:219], v[176:179]\n"
" buffer_store_dwordx4 v[184:187], v230, s[40:43], s79 offen sc1\n"
" v_mfma_f32_16x16x32_bf16 v[180:183], a[248:251], v[216:219], v[180:183]\n"
" ds_read_b128 v[144:147], v225 offset:47104\n"
" v_mfma_f32_16x16x32_bf16 v[176:179], a[228:231], v[220:223], v[176:179]\n"
" ds_read_b128 v[148:151], v225 offset:48128\n"
" v_mfma_f32_16x16x32_bf16 v[180:183], a[252:255], v[220:223], v[180:183]\n"
" ds_read_b128 v[152:155], v225 offset:49152\n"
" v_mfma_f32_16x16x32_bf16 v[168:171], v[0:3], v[200:203], 0\n"
" ds_read_b128 v[156:159], v225 offset:50176\n"
" v_mfma_f32_16x16x32_bf16 v[172:175], v[24:27], v[200:203], 0\n"
" ds_read_b128 v[160:163], v225 offset:51200\n"
" v_mfma_f32_16x16x32_bf16 v[168:171], v[4:7], v[204:207], v[168:171]\n"
" ds_read_b128 v[164:167], v225 offset:52224\n"
" v_mfma_f32_16x16x32_bf16 v[172:175], v[28:31], v[204:207], v[172:175]\n"
" v_mfma_f32_16x16x32_bf16 v[168:171], v[8:11], v[208:211], v[168:171]\n"
" v_mfma_f32_16x16x32_bf16 v[172:175], v[32:35], v[208:211], v[172:175]\n"
" v_mfma_f32_16x16x32_bf16 v[168:171], v[12:15], v[212:215], v[168:171]\n"
" v_cvt_pk_bf16_f32 v184, v176, v177\n"
" v_cvt_pk_bf16_f32 v185, v178, v179\n"
" v_mfma_f32_16x16x32_bf16 v[172:175], v[36:39], v[212:215], v[172:175]\n"
" s_add_i32 s79, s93, 1024\n"
" v_cvt_pk_bf16_f32 v186, v180, v181\n"
" v_cvt_pk_bf16_f32 v187, v182, v183\n"
" v_mfma_f32_16x16x32_bf16 v[168:171], v[16:19], v[216:219], v[168:171]\n"
" buffer_store_dwordx4 v[184:187], v230, s[40:43], s79 offen sc1\n"
" v_mfma_f32_16x16x32_bf16 v[172:175], v[40:43], v[216:219], v[172:175]\n"
" v_mfma_f32_16x16x32_bf16 v[168:171], v[20:23], v[220:223], v[168:171]\n"
" v_mfma_f32_16x16x32_bf16 v[172:175], v[44:47], v[220:223], v[172:175]\n"
" v_mfma_f32_16x16x32_bf16 v[176:179], v[48:51], v[200:203], 0\n"
" v_mfma_f32_16x16x32_bf16 v[180:183], v[72:75], v[200:203], 0\n"
" v_mfma_f32_16x16x32_bf16 v[176:179], v[52:55], v[204:207], v[176:179]\n"
" buffer_load_dword v235, v227, s[48:51], 0 offen sc1\n"
" v_mfma_f32_16x16x32_bf16 v[180:183], v[76:79], v[204:207], v[180:183]\n"
" v_mfma_f32_16x16x32_bf16 v[176:179], v[56:59], v[208:211], v[176:179]\n"
" v_mfma_f32_16x16x32_bf16 v[180:183], v[80:83], v[208:211], v[180:183]\n"
" v_mfma_f32_16x16x32_bf16 v[176:179], v[60:63], v[212:215], v[176:179]\n"
" v_cvt_pk_bf16_f32 v184, v168, v169\n"
" v_cvt_pk_bf16_f32 v185, v170, v171\n"
" v_mfma_f32_16x16x32_bf16 v[180:183], v[84:87], v[212:215], v[180:183]\n"
" s_add_i32 s79, s93, 2048\n"
" v_cvt_pk_bf16_f32 v186, v172, v173\n"
" v_cvt_pk_bf16_f32 v187, v174, v175\n"
" v_mfma_f32_16x16x32_bf16 v[176:179], v[64:67], v[216:219], v[176:179]\n"
" buffer_store_dwordx4 v[184:187], v230, s[40:43], s79 offen sc1\n"
" v_mfma_f32_16x16x32_bf16 v[180:183], v[88:91], v[216:219], v[180:183]\n"
" v_mfma_f32_16x16x32_bf16 v[176:179], v[68:71], v[220:223], v[176:179]\n"
" v_mfma_f32_16x16x32_bf16 v[180:183], v[92:95], v[220:223], v[180:183]\n"
" v_mfma_f32_16x16x32_bf16 v[168:171], v[96:99], v[200:203], 0\n"
" v_mfma_f32_16x16x32_bf16 v[172:175], v[120:123], v[200:203], 0\n"
" v_mfma_f32_16x16x32_bf16 v[168:171], v[100:103], v[204:207], v[168:171]\n"
" v_mfma_f32_16x16x32_bf16 v[172:175], v[124:127], v[204:207], v[172:175]\n"
" v_mfma_f32_16x16x32_bf16 v[168:171], v[104:107], v[208:211], v[168:171]\n"
" v_mfma_f32_16x16x32_bf16 v[172:175], v[128:131], v[208:211], v[172:175]\n"
" v_mfma_f32_16x16x32_bf16 v[168:171], v[108:111], v[212:215], v[168:171]\n"
" v_cvt_pk_bf16_f32 v184, v176, v177\n"
" v_cvt_pk_bf16_f32 v185, v178, v179\n"
" v_mfma_f32_16x16x32_bf16 v[172:175], v[132:135], v[212:215], v[172:175]\n"
" s_add_i32 s79, s93, 3072\n"
" v_cvt_pk_bf16_f32 v186, v180, v181\n"
" v_cvt_pk_bf16_f32 v187, v182, v183\n"
" v_mfma_f32_16x16x32_bf16 v[168:171], v[112:115], v[216:219], v[168:171]\n"
" buffer_store_dwordx4 v[184:187], v230, s[40:43], s79 offen sc1\n"
" v_mfma_f32_16x16x32_bf16 v[172:175], v[136:139], v[216:219], v[172:175]\n"
" v_mfma_f32_16x16x32_bf16 v[168:171], v[116:119], v[220:223], v[168:171]\n"
" v_mfma_f32_16x16x32_bf16 v[172:175], v[140:143], v[220:223], v[172:175]\n"
" s_waitcnt lgkmcnt(5)\n"
" v_mfma_f32_16x16x32_bf16 v[176:179], a[160:163], v[144:147], 0\n"
" v_mfma_f32_16x16x32_bf16 v[180:183], a[184:187], v[144:147], 0\n"
" s_waitcnt lgkmcnt(4)\n"
" v_mfma_f32_16x16x32_bf16 v[176:179], a[164:167], v[148:151], v[176:179]\n"
" v_mfma_f32_16x16x32_bf16 v[180:183], a[188:191], v[148:151], v[180:183]\n"
" s_waitcnt lgkmcnt(3)\n"
" v_mfma_f32_16x16x32_bf16 v[176:179], a[168:171], v[152:155], v[176:179]\n"
" v_mfma_f32_16x16x32_bf16 v[180:183], a[192:195], v[152:155], v[180:183]\n"
" s_waitcnt lgkmcnt(2)\n"
" v_mfma_f32_16x16x32_bf16 v[176:179], a[172:175], v[156:159], v[176:179]\n"
" v_cvt_pk_bf16_f32 v184, v168, v169\n"
" v_cvt_pk_bf16_f32 v185, v170, v171\n"
" v_mfma_f32_16x16x32_bf16 v[180:183], a[196:199], v[156:159], v[180:183]\n"
" s_add_i32 s79, s93, 4096\n"
" v_cvt_pk_bf16_f32 v186, v172, v173\n"
" v_cvt_pk_bf16_f32 v187, v174, v175\n"
" s_waitcnt lgkmcnt(1)\n"
" v_mfma_f32_16x16x32_bf16 v[176:179], a[176:179], v[160:163], v[176:179]\n"
" buffer_store_dwordx4 v[184:187], v230, s[40:43], s79 offen sc1\n"
" v_mfma_f32_16x16x32_bf16 v[180:183], a[200:203], v[160:163], v[180:183]\n"
" s_waitcnt lgkmcnt(0)\n"
" v_mfma_f32_16x16x32_bf16 v[176:179], a[180:183], v[164:167], v[176:179]\n"
" v_mfma_f32_16x16x32_bf16 v[180:183], a[204:207], v[164:167], v[180:183]\n"
" v_mfma_f32_16x16x32_bf16 v[168:171], a[208:211], v[144:147], 0\n"
" s_waitcnt vmcnt(3)\n"
" v_cmp_gt_i32 vcc, s78, v235\n"
" v_mfma_f32_16x16x32_bf16 v[172:175], a[232:235], v[144:147], 0\n"
" s_cmp_eq_u64 vcc, 0\n"
" s_cselect_b32 s94, 1, 0\n"
" s_cselect_b32 s38, 134217728, 0\n"
" v_mfma_f32_16x16x32_bf16 v[168:171], a[212:215], v[148:151], v[168:171]\n"
" s_add_i32 m0, s77, 0\n"
" s_add_i32 s76, s92, 0\n"
" buffer_load_dwordx4 v226, s[36:39], s76 offen sc1 lds\n"
" v_mfma_f32_16x16x32_bf16 v[172:175], a[236:239], v[148:151], v[172:175]\n"
" v_mfma_f32_16x16x32_bf16 v[168:171], a[216:219], v[152:155], v[168:171]\n"
" s_add_i32 m0, s77, 4096\n"
" s_add_i32 s76, s92, 1024\n"
" buffer_load_dwordx4 v226, s[36:39], s76 offen sc1 lds\n"
" v_mfma_f32_16x16x32_bf16 v[172:175], a[240:243], v[152:155], v[172:175]\n"
" v_mfma_f32_16x16x32_bf16 v[168:171], a[220:223], v[156:159], v[168:171]\n"
" v_cvt_pk_bf16_f32 v184, v176, v177\n"
" v_cvt_pk_bf16_f32 v185, v178, v179\n"
" v_mfma_f32_16x16x32_bf16 v[172:175], a[244:247], v[156:159], v[172:175]\n"
" s_add_i32 s79, s93, 655360\n"
" v_cvt_pk_bf16_f32 v186, v180, v181\n"
" v_cvt_pk_bf16_f32 v187, v182, v183\n"
" v_mfma_f32_16x16x32_bf16 v[168:171], a[224:227], v[160:163], v[168:171]\n"
" buffer_store_dwordx4 v[184:187], v230, s[40:43], s79 offen sc1\n"
" v_mfma_f32_16x16x32_bf16 v[172:175], a[248:251], v[160:163], v[172:175]\n"
" v_mfma_f32_16x16x32_bf16 v[168:171], a[228:231], v[164:167], v[168:171]\n"
" s_add_i32 m0, s77, 8192\n"
" s_add_i32 s76, s92, 2048\n"
" buffer_load_dwordx4 v226, s[36:39], s76 offen sc1 lds\n"
" v_mfma_f32_16x16x32_bf16 v[172:175], a[252:255], v[164:167], v[172:175]\n"
" v_mfma_f32_16x16x32_bf16 v[176:179], v[0:3], v[144:147], 0\n"
" v_mfma_f32_16x16x32_bf16 v[180:183], v[24:27], v[144:147], 0\n"
" v_mfma_f32_16x16x32_bf16 v[176:179], v[4:7], v[148:151], v[176:179]\n"
" s_add_i32 m0, s77, 12288\n"
" s_add_i32 s76, s92, 3072\n"
" buffer_load_dwordx4 v226, s[36:39], s76 offen sc1 lds\n"
" v_mfma_f32_16x16x32_bf16 v[180:183], v[28:31], v[148:151], v[180:183]\n"
" v_mfma_f32_16x16x32_bf16 v[176:179], v[8:11], v[152:155], v[176:179]\n"
" s_add_i32 m0, s77, 16384\n"
" s_add_i32 s76, s92, 4096\n"
" buffer_load_dwordx4 v226, s[36:39], s76 offen sc1 lds\n"
" v_mfma_f32_16x16x32_bf16 v[180:183], v[32:35], v[152:155], v[180:183]\n"
" v_mfma_f32_16x16x32_bf16 v[176:179], v[12:15], v[156:159], v[176:179]\n"
" v_cvt_pk_bf16_f32 v184, v168, v169\n"
" v_cvt_pk_bf16_f32 v185, v170, v171\n"
" v_mfma_f32_16x16x32_bf16 v[180:183], v[36:39], v[156:159], v[180:183]\n"
" s_add_i32 s79, s93, 656384\n"
" v_cvt_pk_bf16_f32 v186, v172, v173\n"
" v_cvt_pk_bf16_f32 v187, v174, v175\n"
" v_mfma_f32_16x16x32_bf16 v[176:179], v[16:19], v[160:163], v[176:179]\n"
" buffer_store_dwordx4 v[184:187], v230, s[40:43], s79 offen sc1\n"
" v_mfma_f32_16x16x32_bf16 v[180:183], v[40:43], v[160:163], v[180:183]\n"
" v_mfma_f32_16x16x32_bf16 v[176:179], v[20:23], v[164:167], v[176:179]\n"
" s_add_i32 m0, s77, 20480\n"
" s_add_i32 s76, s92, 65536\n"
" buffer_load_dwordx4 v226, s[36:39], s76 offen sc1 lds\n"
" v_mfma_f32_16x16x32_bf16 v[180:183], v[44:47], v[164:167], v[180:183]\n"
" v_mfma_f32_16x16x32_bf16 v[168:171], v[48:51], v[144:147], 0\n"
" v_mfma_f32_16x16x32_bf16 v[172:175], v[72:75], v[144:147], 0\n"
" v_mfma_f32_16x16x32_bf16 v[168:171], v[52:55], v[148:151], v[168:171]\n"
" s_add_i32 m0, s77, 24576\n"
" s_add_i32 s76, s92, 66560\n"
" buffer_load_dwordx4 v226, s[36:39], s76 offen sc1 lds\n"
" v_mfma_f32_16x16x32_bf16 v[172:175], v[76:79], v[148:151], v[172:175]\n"
" v_mfma_f32_16x16x32_bf16 v[168:171], v[56:59], v[152:155], v[168:171]\n"
" s_add_i32 m0, s77, 28672\n"
" s_add_i32 s76, s92, 67584\n"
" buffer_load_dwordx4 v226, s[36:39], s76 offen sc1 lds\n"
" v_mfma_f32_16x16x32_bf16 v[172:175], v[80:83], v[152:155], v[172:175]\n"
" v_mfma_f32_16x16x32_bf16 v[168:171], v[60:63], v[156:159], v[168:171]\n"
" v_cvt_pk_bf16_f32 v184, v176, v177\n"
" v_cvt_pk_bf16_f32 v185, v178, v179\n"
" v_mfma_f32_16x16x32_bf16 v[172:175], v[84:87], v[156:159], v[172:175]\n"
" s_add_i32 s79, s93, 657408\n"
" v_cvt_pk_bf16_f32 v186, v180, v181\n"
" v_cvt_pk_bf16_f32 v187, v182, v183\n"
" v_mfma_f32_16x16x32_bf16 v[168:171], v[64:67], v[160:163], v[168:171]\n"
" buffer_store_dwordx4 v[184:187], v230, s[40:43], s79 offen sc1\n"
" v_mfma_f32_16x16x32_bf16 v[172:175], v[88:91], v[160:163], v[172:175]\n"
" v_mfma_f32_16x16x32_bf16 v[168:171], v[68:71], v[164:167], v[168:171]\n"
" s_add_i32 m0, s77, 32768\n"
" s_add_i32 s76, s92, 68608\n"
" buffer_load_dwordx4 v226, s[36:39], s76 offen sc1 lds\n"
" v_mfma_f32_16x16x32_bf16 v[172:175], v[92:95], v[164:167], v[172:175]\n"
" v_mfma_f32_16x16x32_bf16 v[176:179], v[96:99], v[144:147], 0\n"
" v_mfma_f32_16x16x32_bf16 v[180:183], v[120:123], v[144:147], 0\n"
" v_mfma_f32_16x16x32_bf16 v[176:179], v[100:103], v[148:151], v[176:179]\n"
" s_add_i32 m0, s77, 36864\n"
" s_add_i32 s76, s92, 69632\n"
" buffer_load_dwordx4 v226, s[36:39], s76 offen sc1 lds\n"
" v_mfma_f32_16x16x32_bf16 v[180:183], v[124:127], v[148:151], v[180:183]\n"
" v_mfma_f32_16x16x32_bf16 v[176:179], v[104:107], v[152:155], v[176:179]\n"
" v_mfma_f32_16x16x32_bf16 v[180:183], v[128:131], v[152:155], v[180:183]\n"
" buffer_load_dword v235, v227, s[48:51], 0 offen sc1\n"
" v_mfma_f32_16x16x32_bf16 v[176:179], v[108:111], v[156:159], v[176:179]\n"
" v_cvt_pk_bf16_f32 v184, v168, v169\n"
" v_cvt_pk_bf16_f32 v185, v170, v171\n"
" v_mfma_f32_16x16x32_bf16 v[180:183], v[132:135], v[156:159], v[180:183]\n"
" s_add_i32 s79, s93, 658432\n"
" v_cvt_pk_bf16_f32 v186, v172, v173\n"
" v_cvt_pk_bf16_f32 v187, v174, v175\n"
" v_mfma_f32_16x16x32_bf16 v[176:179], v[112:115], v[160:163], v[176:179]\n"
" buffer_store_dwordx4 v[184:187], v230, s[40:43], s79 offen sc1\n"
" v_mfma_f32_16x16x32_bf16 v[180:183], v[136:139], v[160:163], v[180:183]\n"
" v_mfma_f32_16x16x32_bf16 v[176:179], v[116:119], v[164:167], v[176:179]\n"
" v_mfma_f32_16x16x32_bf16 v[180:183], v[140:143], v[164:167], v[180:183]\n"
" s_nop 6\n"
" v_cvt_pk_bf16_f32 v184, v176, v177\n"
" v_cvt_pk_bf16_f32 v185, v178, v179\n"
" s_add_i32 s79, s93, 659456\n"
" v_cvt_pk_bf16_f32 v186, v180, v181\n"
" v_cvt_pk_bf16_f32 v187, v182, v183\n"
" buffer_store_dwordx4 v[184:187], v230, s[40:43], s79 offen sc1\n"
" s_cmp_lg_u32 s94, 0\n"
" s_cbranch_scc1 .Lff_nx_%=\n"
" s_waitcnt vmcnt(2)\n"
" v_cmp_gt_i32 vcc, s78, v235\n"
" s_nop 0\n"
" s_cbranch_vccnz .Lff_nx_%=\n"
" s_mov_b32 s38, 134217728\n"
" s_mov_b32 s94, 2\n"
" s_add_i32 m0, s77, 0\n"
" s_add_i32 s76, s92, 0\n"
" buffer_load_dwordx4 v226, s[36:39], s76 offen sc1 lds\n"
" s_add_i32 m0, s77, 4096\n"
" s_add_i32 s76, s92, 1024\n"
" buffer_load_dwordx4 v226, s[36:39], s76 offen sc1 lds\n"
" s_add_i32 m0, s77, 8192\n"
" s_add_i32 s76, s92, 2048\n"
" buffer_load_dwordx4 v226, s[36:39], s76 offen sc1 lds\n"
" s_add_i32 m0, s77, 12288\n"
" s_add_i32 s76, s92, 3072\n"
" buffer_load_dwordx4 v226, s[36:39], s76 offen sc1 lds\n"
" s_add_i32 m0, s77, 16384\n"
" s_add_i32 s76, s92, 4096\n"
" buffer_load_dwordx4 v226, s[36:39], s76 offen sc1 lds\n"
" s_add_i32 m0, s77, 20480\n"
" s_add_i32 s76, s92, 65536\n"
" buffer_load_dwordx4 v226, s[36:39], s76 offen sc1 lds\n"
" s_add_i32 m0, s77, 24576\n"
" s_add_i32 s76, s92, 66560\n"
" buffer_load_dwordx4 v226, s[36:39], s76 offen sc1 lds\n"
" s_add_i32 m0, s77, 28672\n"
" s_add_i32 s76, s92, 67584\n"
" buffer_load_dwordx4 v226, s[36:39], s76 offen sc1 lds\n"
" s_add_i32 m0, s77, 32768\n"
" s_add_i32 s76, s92, 68608\n"
" buffer_load_dwordx4 v226, s[36:39], s76 offen sc1 lds\n"
" s_add_i32 m0, s77, 36864\n"
" s_add_i32 s76, s92, 69632\n"
" buffer_load_dwordx4 v226, s[36:39], s76 offen sc1 lds\n"
" s_waitcnt vmcnt(10)\n"
" v_mov_b32 v242, 1\n"
" s_mov_b64 exec, 1\n"
" ds_add_rtn_u32 v243, v240, v242\n"
" s_mov_b64 exec, -1\n"
" s_waitcnt lgkmcnt(0)\n"
" v_readfirstlane_b32 s76, v243\n"
" s_nop 0\n"
" s_and_b32 s76, s76, 3\n"
" s_cmp_lg_u32 s76, 3\n"
" s_cbranch_scc1 .Lff_lf3_%=\n"
" v_mov_b32 v242, s68\n"
" buffer_store_dword v242, v228, s[48:51], 0 offen sc1\n"
".Lff_lf3_%=:\n"
" s_nop 3\n"
".Lff_nx_%=:\n"
" s_mov_b32 s95, s68\n"
" s_add_i32 s68, s68, 1\n"
" s_cmp_lt_u32 s68, 64\n"
" s_cbranch_scc1 .Lff_loop_%=\n"
" s_waitcnt vmcnt(0)\n"
" v_mov_b32 v242, 1\n"
" s_mov_b64 exec, 1\n"
" ds_add_rtn_u32 v243, v240, v242\n"
" s_mov_b64 exec, -1\n"
" s_waitcnt lgkmcnt(0)\n"
" v_readfirstlane_b32 s76, v243\n"
" s_nop 0\n"
" s_and_b32 s76, s76, 3\n"
" s_cmp_lg_u32 s76, 3\n"
" s_cbranch_scc1 .Lff_lf2_%=\n"
" v_mov_b32 v242, s95\n"
" buffer_store_dword v242, v228, s[48:51], 0 offen sc1\n"
".Lff_lf2_%=:\n"
" s_nop 3\n"
" s_waitcnt vmcnt(0)\n"
      : "+{v[224:239]}"(c0), "+{v[240:255]}"(c1), "+{s[36:39]}"(sI), "+{s[48:51]}"(sP)
      : "{s[40:43]}"(sO), "{s[52:55]}"(s0), "{s[56:59]}"(s1), "{s84}"(uni((int)OFF_XF + l * RS * 8 * CELL)), "{s85}"(uni((int)OFF_TP + l * RS * (2 << 20))), "{s69}"(w), "{s[16:17]}"(p.tag), "{s[96:97]}"(dbgp(p, w))
      : "memory", "vcc", "scc", "m0", V_LOW, A_ALL, S_BULK, "s68");
  __builtin_amdgcn_endpgm();
}
__device__ __forceinline__ float bflo(unsigned u) { return __uint_as_float(u << 16); }
__device__ __forceinline__ float bfhi(unsigned u) { return __uint_as_float(u & 0xFFFF0000u); }
__device__ __forceinline__ float tok_sum(float x) {
  x += __builtin_bit_cast(float, __builtin_amdgcn_update_dpp(0, __builtin_bit_cast(int, x), 0x124, 0xf, 0xf, false));
  x += __builtin_bit_cast(float, __builtin_amdgcn_update_dpp(0, __builtin_bit_cast(int, x), 0x128, 0xf, 0xf, false));
  return xg_sum(x);
}
__device__ __forceinline__ v4u join16(v2u a, v2u b) {
  const auto x = __builtin_amdgcn_permlane16_swap(a.x, b.x, false, false), y = __builtin_amdgcn_permlane16_swap(a.y, b.y, false, false);
  return v4u{x[0], y[0], x[1], y[1]};
}
__device__ __forceinline__ int row_off(int lane, int quad) {
  const int g = lane >> 4, n = lane & 15;
  return (quad * 4 + (n & 3)) * 1280 + (g >> 1) * 64 + (g & 1) * 32 + (n >> 2) * 8;
}
__device__ __forceinline__ void ln0_load(const Params& p, int lane, int quad, int s, v2u* x) {
#pragma unroll
  for (int i = 0; i < 10; ++i) x[i] = *(const GAS v2u*)(p.x + (size_t)s * 40960 + row_off(lane, quad) + i * 128);
}
__device__ __forceinline__ void ln0_wave(const Params& p, int quad) {
  const int lane = lane_id(), g = lane >> 4, n = lane & 15;
  u8* ws = p.ws;
  const rsrc_t rP = mkrsrc(ws + OFF_PROG), rN = mkrsrc(ws + quad * CELL + CO);
  const int sof = (g & 1) * 512 + ((g >> 1) * 16 + n) * 16;
  v2u x0[10], x1[10];
  ln0_load(p, lane, quad, 0, x0); ln0_load(p, lane, quad, 1, x1);
  bool dead = false;
  v2u pw;
  for (int sp = 0;;) {
    asm volatile("" ::: "memory");
    pw = ld8(rP, pol_word(lane), 0);
    if (__all((pw.x | 1u) == (unsigned)p.tag && pw.y == (unsigned)(p.tag >> 32)) || give_up(sp, dead)) break;
    __builtin_amdgcn_s_sleep(4);
  }
  asm volatile("" ::: "memory");
  const unsigned long long polb = __ballot((pw.x & 1u) == 0u);
  const int fsh = (int)(polb & 1) << 13;
  st4(RS - 1, rP, lane < 4 ? pg(PG_FIN + lane * 8 + quad) : OOB, fsh);
  for (int s = 0; s < NSEG; ++s) {
    f32x4 v[10];
#pragma unroll
    for (int i = 0; i < 10; ++i) { const v2u x = x0[i]; v[i] = f32x4{bflo(x.x), bfhi(x.x), bflo(x.y), bfhi(x.y)}; }
    v2u xo[10];
#pragma unroll
    for (int i = 0; i < 10; ++i) { xo[i] = x0[i]; x0[i] = x1[i]; }
    SB();
    ln0_load(p, lane, quad, min(s + 2, NSEG - 1), x1);
    SB();
    f32x4 s4 = v[0];
#pragma unroll
    for (int i = 1; i < 10; ++i) s4 += v[i];
    const float mean = tok_sum((s4[0] + s4[1]) + (s4[2] + s4[3])) * (1.0f / 640.0f);
    f32x4 q4 = {0.f, 0.f, 0.f, 0.f};
#pragma unroll
    for (int i = 0; i < 10; ++i) { v[i] -= mean; q4 += v[i] * v[i]; }
    const float rstd = __builtin_amdgcn_rsqf(tok_sum((q4[0] + q4[1]) + (q4[2] + q4[3])) * (1.0f / 640.0f) + 1e-5f);
    int no = (int)OFF_XN + s * 8 * CELL;
    no += (int)((polb >> (no >> 21)) & 1) << 13;
    int xo_ = (int)OFF_XR + s * 8 * CELL;
    xo_ += (int)((polb >> (xo_ >> 21)) & 1) << 13;
#pragma unroll
    for (int i = 0; i < 5; ++i) st16(join16(pk4(v[2 * i] * rstd), pk4(v[2 * i + 1] * rstd)), rN, sof, no + i * 1024);
#pragma unroll
    for (int i = 0; i < 5; ++i) st16(join16(xo[2 * i], xo[2 * i + 1]), rN, sof, xo_ + i * 1024);
    if (!((LNMASK >> s) & 1)) continue;
    VM0();
    st4((unsigned)s, rP, lane == 0 ? pg(PG_LN0 + quad) : OOB, 8192 - fsh);
  }
}
template <int KIND>
__device__ __forceinline__ void red_role(const Params& p, int w, int l, int quad, int s_first, int s_step, int xc) {
  const int lane = lane_id(), g = lane >> 4, n = lane & 15;
  u8* ws = p.ws;
  i32x16 c0;
#pragma unroll
  for (int i = 0; i < 16; ++i) c0[i] = 0;
  c0[0] = (g & 1) * CELL + ((g >> 1) * 16 + n) * 16; c0[1] = (g & 1) ? OOB : c0[0]; c0[5] = pol_word(lane);
  c0[2] = KIND == 3 ? row_off(lane, quad) : (g & 1) * 512 + ((g >> 1) * 16 + n) * 16;
  c0[3] = pg(KIND == 1 ? PG_WO + xc * 32 + l * 16 + (lane & 15) : PG_FFN + xc * 32 + l * 16 + (lane % FW));
  c0[4] = lane < 4 ? pg(KIND == 1 ? PG_X1 + lane * 32 + (l * 8 + quad) * 2 + 1 : KIND == 2 ? PG_XN + lane * 16 + quad * 2 + 1 : PG_FIN + lane * 8 + quad) : OOB;
#pragma unroll
  for (int j = 0; j < 4; ++j) c0[8 + j] = (n >> 3) != (g >> 1) ? 0 : (n & 7) == 2 * j ? 0x3F80 : (n & 7) == 2 * j + 1 ? 0x3F800000 : 0;
  const v4u sS = rawrsrc(ws + (KIND == 1 ? 96 + quad * 4 : quad * NPF) * CELL + CO, WSB);
  const v4u sR = rawrsrc(ws + quad * CELL + CO, WSB);
  const v4u sX = KIND == 1 ? rawrsrc(ws + (quad * NPF + NPF - 1) * CELL + CO, WSB)
               : KIND == 2 ? sR : rawrsrc(p.out, 2048 * 1280);
  const v4u sN = KIND == 3 ? rawrsrc(ws + OFF_CTL, 128) : rawrsrc(ws + quad * CELL + CO, WSB);
  v4u sP = rawrsrc(ws + OFF_PROG, PGB);
  int seg = __builtin_amdgcn_readfirstlane(s_first);
#define RED_OPS : "+{v[224:239]}"(c0), "+{s68}"(seg), "+{s[48:51]}"(sP) \
      : "{s[36:39]}"(sS), "{s[40:43]}"(sX), "{s[44:47]}"(sR), "{s[52:55]}"(sN), "{s70}"(s_step), "{s98}"(0x3acccccd), "{s73}"(uni(KIND == 1 ? 63 - 32 * l : 31)), "{s88}"(uni((int)OFF_XR + (KIND == 1 ? l : 1) * 64 * 8 * CELL)), \
        "{s84}"(uni(KIND == 1 ? (int)OFF_XF + l * RS * 8 * CELL : (int)OFF_XN + 64 * 8 * CELL)), "{s85}"(uni((int)OFF_TP + l * RS * (2 << 20))), "{s[16:17]}"(p.tag), "{s[96:97]}"(dbgp(p, w)) \
      : "memory", "vcc", "scc", V_LOW, A_ALL, "s20", "s21", "s22", "s23", "s24", "s25", "s26", "s27", "s30", "s31", "s71", "s72", "s76", "s77", "s78", "s79", "s80", "s81", "s82", "s83", "s86", "s87", \
        "s90", "s91", "s92", "s93", "s94", "s95"
  if (KIND == 1) asm volatile(
" s_nop 4\n"
" s_mov_b32 s90, 0\n"
" s_mov_b32 s72, 0\n"
" s_mov_b32 s71, -1\n"
" s_mov_b32 s91, 0\n"
".Lr1_pol_%=:\n"
" buffer_load_dwordx2 v[220:221], v229, s[48:51], 0 offen sc1\n"
" s_waitcnt vmcnt(0)\n"
" v_or_b32 v222, 1, v220\n"
" v_mov_b32 v223, v221\n"
" v_cmp_ne_u64 vcc, s[16:17], v[222:223]\n"
" s_nop 0\n"
" s_cbranch_vccz .Lr1_polok_%=\n"
" s_add_i32 s91, s91, 1\n"
" s_or_b32 s91, s91, s90\n"
" s_cmp_ge_u32 s91, 65536\n"
" s_cselect_b32 s90, 65536, 0\n"
" s_cbranch_scc1 .Lr1_polok_%=\n"
" s_sleep 4\n"
" s_branch .Lr1_pol_%=\n"
".Lr1_polok_%=:\n"
" v_and_b32 v220, 1, v220\n"
" v_cmp_ne_u32_e64 s[86:87], 1, v220\n"
" s_nop 1\n"
" s_bitcmp1_b32 s86, 0\n"
" s_cselect_b32 s76, 8192, 0\n"
" s_add_u32 s48, s48, s76\n"
".Lr1_loop_%=:\n"
" s_cmp_lg_u32 s72, 0\n"
" s_cbranch_scc1 .Lr1_have_%=\n"
" s_mov_b32 s91, 0\n"
" buffer_load_dword v229, v227, s[48:51], 0 offen sc1\n"
" s_waitcnt vmcnt(0)\n"
" v_mov_b32 v66, s71\n"
" buffer_store_dword v66, v228, s[48:51], 0 offen sc1\n"
".Lr1_in_%=:\n"
" v_cmp_gt_i32 vcc, s68, v229\n"
" s_nop 0\n"
" s_cbranch_vccz .Lr1_inok_%=\n"
" s_add_i32 s91, s91, 1\n"
" s_or_b32 s91, s91, s90\n"
" s_cmp_ge_u32 s91, 65536\n"
" s_cselect_b32 s90, 65536, 0\n"
" s_cbranch_scc1 .Lr1_inok_%=\n"
" s_sleep 2\n"
" buffer_load_dword v229, v227, s[48:51], 0 offen sc1\n"
" s_waitcnt vmcnt(0)\n"
" s_branch .Lr1_in_%=\n"
".Lr1_inok_%=:\n"
" s_and_b32 s92, s68, 15\n"
" s_lshl_b32 s92, s92, 21\n"
" s_add_i32 s92, s92, s85\n"
" s_lshr_b32 s76, s92, 21\n"
" s_bitcmp1_b64 s[86:87], s76\n"
" s_cselect_b32 s76, 8192, 0\n"
" s_add_i32 s92, s92, s76\n"
" s_and_b32 s93, s68, s73\n"
" s_lshl_b32 s93, s93, 17\n"
" s_add_i32 s93, s93, s88\n"
" s_lshr_b32 s76, s93, 21\n"
" s_bitcmp1_b64 s[86:87], s76\n"
" s_cselect_b32 s76, 8192, 0\n"
" s_add_i32 s93, s93, s76\n"
" s_add_i32 s78, s92, 0\n"
" buffer_load_dwordx4 a[0:3], v224, s[36:39], s78 offen offset:0 sc1\n"
" s_add_i32 s79, s92, 32768\n"
" buffer_load_dwordx4 a[4:7], v224, s[36:39], s79 offen offset:0 sc1\n"
" buffer_load_dwordx4 a[8:11], v225, s[44:47], s93 offen offset:0 sc1\n"
" buffer_load_dwordx4 a[12:15], v224, s[36:39], s78 offen offset:512 sc1\n"
" buffer_load_dwordx4 a[16:19], v224, s[36:39], s79 offen offset:512 sc1\n"
" buffer_load_dwordx4 a[20:23], v225, s[44:47], s93 offen offset:512 sc1\n"
" buffer_load_dwordx4 a[24:27], v224, s[36:39], s78 offen offset:1024 sc1\n"
" buffer_load_dwordx4 a[28:31], v224, s[36:39], s79 offen offset:1024 sc1\n"
" buffer_load_dwordx4 a[32:35], v225, s[44:47], s93 offen offset:1024 sc1\n"
" buffer_load_dwordx4 a[36:39], v224, s[36:39], s78 offen offset:1536 sc1\n"
" buffer_load_dwordx4 a[40:43], v224, s[36:39], s79 offen offset:1536 sc1\n"
" buffer_load_dwordx4 a[44:47], v225, s[44:47], s93 offen offset:1536 sc1\n"
" buffer_load_dwordx4 a[48:51], v224, s[36:39], s78 offen offset:2048 sc1\n"
" buffer_load_dwordx4 a[52:55], v224, s[36:39], s79 offen offset:2048 sc1\n"
" buffer_load_dwordx4 a[56:59], v225, s[44:47], s93 offen offset:2048 sc1\n"
" buffer_load_dwordx4 a[60:63], v224, s[36:39], s78 offen offset:2560 sc1\n"
" buffer_load_dwordx4 a[64:67], v224, s[36:39], s79 offen offset:2560 sc1\n"
" buffer_load_dwordx4 a[68:71], v225, s[44:47], s93 offen offset:2560 sc1\n"
" buffer_load_dwordx4 a[72:75], v224, s[36:39], s78 offen offset:3072 sc1\n"
" buffer_load_dwordx4 a[76:79], v224, s[36:39], s79 offen offset:3072 sc1\n"
" buffer_load_dwordx4 a[80:83], v225, s[44:47], s93 offen offset:3072 sc1\n"
" buffer_load_dwordx4 a[84:87], v224, s[36:39], s78 offen offset:3584 sc1\n"
" buffer_load_dwordx4 a[88:91], v224, s[36:39], s79 offen offset:3584 sc1\n"
" buffer_load_dwordx4 a[92:95], v225, s[44:47], s93 offen offset:3584 sc1\n"
" s_add_i32 s78, s92, 4096\n"
" buffer_load_dwordx4 a[96:99], v224, s[36:39], s78 offen offset:0 sc1\n"
" s_add_i32 s79, s92, 36864\n"
" buffer_load_dwordx4 a[100:103], v224, s[36:39], s79 offen offset:0 sc1\n"
" s_add_i32 s93, s93, 4096\n"
" buffer_load_dwordx4 a[104:107], v225, s[44:47], s93 offen offset:0 sc1\n"
" buffer_load_dwordx4 a[108:111], v224, s[36:39], s78 offen offset:512 sc1\n"
" buffer_load_dwordx4 a[112:115], v224, s[36:39], s79 offen offset:512 sc1\n"
" buffer_load_dwordx4 a[116:119], v225, s[44:47], s93 offen offset:512 sc1\n"
".Lr1_have_%=:\n"
" s_mov_b32 s94, s92\n"
" s_and_b32 s95, s68, 15\n"
" s_lshl_b32 s95, s95, 17\n"
" s_add_i32 s95, s95, s84\n"
" s_lshr_b32 s76, s95, 21\n"
" s_bitcmp1_b64 s[86:87], s76\n"
" s_cselect_b32 s76, 8192, 0\n"
" s_add_i32 s95, s95, s76\n"
" s_waitcnt vmcnt(29)\n"
" v_mfma_f32_16x16x32_bf16 v[0:3], v[232:235], a[0:3], 0\n"
" s_waitcnt vmcnt(28)\n"
" v_mfma_f32_16x16x32_bf16 v[0:3], v[232:235], a[4:7], v[0:3]\n"
" s_waitcnt vmcnt(27)\n"
" v_mfma_f32_16x16x32_bf16 v[0:3], v[232:235], a[8:11], v[0:3]\n"
" s_waitcnt vmcnt(26)\n"
" v_mfma_f32_16x16x32_bf16 v[4:7], v[232:235], a[12:15], 0\n"
" s_waitcnt vmcnt(25)\n"
" v_mfma_f32_16x16x32_bf16 v[4:7], v[232:235], a[16:19], v[4:7]\n"
" s_waitcnt vmcnt(24)\n"
" v_mfma_f32_16x16x32_bf16 v[4:7], v[232:235], a[20:23], v[4:7]\n"
" s_nop 1\n"
" v_cvt_pk_bf16_f32 v40, v0, v1\n"
" v_cvt_pk_bf16_f32 v41, v2, v3\n"
" v_add_f32 v64, v0, v1\n"
" v_add_f32 v66, v2, v3\n"
" v_add_f32 v64, v64, v66\n"
" s_nop 1\n"
" v_add_f32_dpp v64, v64, v64 row_ror:4 row_mask:0xf bank_mask:0xf\n"
" s_nop 1\n"
" v_add_f32_dpp v64, v64, v64 row_ror:8 row_mask:0xf bank_mask:0xf\n"
" v_mov_b32 v66, v64\n"
" s_nop 1\n"
" v_permlane16_swap_b32 v64, v66\n"
" v_add_f32 v64, v64, v66\n"
" v_mov_b32 v66, v64\n"
" s_nop 1\n"
" v_permlane32_swap_b32 v64, v66\n"
" v_add_f32 v64, v64, v66\n"
" v_mul_f32 v64, 0xbc800000, v64\n"
" v_pk_add_f32 v[0:1], v[0:1], v[64:65] op_sel_hi:[1,0]\n"
" v_pk_add_f32 v[2:3], v[2:3], v[64:65] op_sel_hi:[1,0]\n"
" v_pk_add_f32 v[60:61], v[0:1], v[2:3]\n"
" v_pk_mul_f32 v[62:63], v[0:1], v[0:1]\n"
" v_pk_fma_f32 v[62:63], v[2:3], v[2:3], v[62:63]\n"
" s_waitcnt vmcnt(23)\n"
" v_mfma_f32_16x16x32_bf16 v[8:11], v[232:235], a[24:27], 0\n"
" s_waitcnt vmcnt(22)\n"
" v_mfma_f32_16x16x32_bf16 v[8:11], v[232:235], a[28:31], v[8:11]\n"
" s_waitcnt vmcnt(21)\n"
" v_mfma_f32_16x16x32_bf16 v[8:11], v[232:235], a[32:35], v[8:11]\n"
" v_cvt_pk_bf16_f32 v42, v4, v5\n"
" v_cvt_pk_bf16_f32 v43, v6, v7\n"
" v_pk_add_f32 v[4:5], v[4:5], v[64:65] op_sel_hi:[1,0]\n"
" v_pk_add_f32 v[6:7], v[6:7], v[64:65] op_sel_hi:[1,0]\n"
" v_pk_add_f32 v[60:61], v[60:61], v[4:5]\n"
" v_pk_fma_f32 v[62:63], v[4:5], v[4:5], v[62:63]\n"
" v_pk_add_f32 v[60:61], v[60:61], v[6:7]\n"
" v_pk_fma_f32 v[62:63], v[6:7], v[6:7], v[62:63]\n"
" v_permlane16_swap_b32 v40, v42\n"
" v_permlane16_swap_b32 v41, v43\n"
" s_waitcnt vmcnt(20)\n"
" v_mfma_f32_16x16x32_bf16 v[12:15], v[232:235], a[36:39], 0\n"
" s_waitcnt vmcnt(19)\n"
" v_mfma_f32_16x16x32_bf16 v[12:15], v[232:235], a[40:43], v[12:15]\n"
" s_waitcnt vmcnt(18)\n"
" v_mfma_f32_16x16x32_bf16 v[12:15], v[232:235], a[44:47], v[12:15]\n"
" v_cvt_pk_bf16_f32 v44, v8, v9\n"
" v_cvt_pk_bf16_f32 v45, v10, v11\n"
" v_pk_add_f32 v[8:9], v[8:9], v[64:65] op_sel_hi:[1,0]\n"
" v_pk_add_f32 v[10:11], v[10:11], v[64:65] op_sel_hi:[1,0]\n"
" v_pk_add_f32 v[60:61], v[60:61], v[8:9]\n"
" v_pk_fma_f32 v[62:63], v[8:9], v[8:9], v[62:63]\n"
" v_pk_add_f32 v[60:61], v[60:61], v[10:11]\n"
" v_pk_fma_f32 v[62:63], v[10:11], v[10:11], v[62:63]\n"
" s_waitcnt vmcnt(17)\n"
" v_mfma_f32_16x16x32_bf16 v[16:19], v[232:235], a[48:51], 0\n"
" s_waitcnt vmcnt(16)\n"
" v_mfma_f32_16x16x32_bf16 v[16:19], v[232:235], a[52:55], v[16:19]\n"
" s_waitcnt vmcnt(15)\n"
" v_mfma_f32_16x16x32_bf16 v[16:19], v[232:235], a[56:59], v[16:19]\n"
" v_cvt_pk_bf16_f32 v46, v12, v13\n"
" v_cvt_pk_bf16_f32 v47, v14, v15\n"
" v_pk_add_f32 v[12:13], v[12:13], v[64:65] op_sel_hi:[1,0]\n"
" v_pk_add_f32 v[14:15], v[14:15], v[64:65] op_sel_hi:[1,0]\n"
" v_pk_add_f32 v[60:61], v[60:61], v[12:13]\n"
" v_pk_fma_f32 v[62:63], v[12:13], v[12:13], v[62:63]\n"
" v_pk_add_f32 v[60:61], v[60:61], v[14:15]\n"
" v_pk_fma_f32 v[62:63], v[14:15], v[14:15], v[62:63]\n"
" v_permlane16_swap_b32 v44, v46\n"
" v_permlane16_swap_b32 v45, v47\n"
" s_waitcnt vmcnt(14)\n"
" v_mfma_f32_16x16x32_bf16 v[20:23], v[232:235], a[60:63], 0\n"
" s_waitcnt vmcnt(13)\n"
" v_mfma_f32_16x16x32_bf16 v[20:23], v[232:235], a[64:67], v[20:23]\n"
" s_waitcnt vmcnt(12)\n"
" v_mfma_f32_16x16x32_bf16 v[20:23], v[232:235], a[68:71], v[20:23]\n"
" v_cvt_pk_bf16_f32 v48, v16, v17\n"
" v_cvt_pk_bf16_f32 v49, v18, v19\n"
" v_pk_add_f32 v[16:17], v[16:17], v[64:65] op_sel_hi:[1,0]\n"
" v_pk_add_f32 v[18:19], v[18:19], v[64:65] op_sel_hi:[1,0]\n"
" v_pk_add_f32 v[60:61], v[60:61], v[16:17]\n"
" v_pk_fma_f32 v[62:63], v[16:17], v[16:17], v[62:63]\n"
" v_pk_add_f32 v[60:61], v[60:61], v[18:19]\n"
" v_pk_fma_f32 v[62:63], v[18:19], v[18:19], v[62:63]\n"
" s_waitcnt vmcnt(11)\n"
" v_mfma_f32_16x16x32_bf16 v[24:27], v[232:235], a[72:75], 0\n"
" s_waitcnt vmcnt(10)\n"
" v_mfma_f32_16x16x32_bf16 v[24:27], v[232:235], a[76:79], v[24:27]\n"
" s_waitcnt vmcnt(9)\n"
" v_mfma_f32_16x16x32_bf16 v[24:27], v[232:235], a[80:83], v[24:27]\n"
" v_cvt_pk_bf16_f32 v50, v20, v21\n"
" v_cvt_pk_bf16_f32 v51, v22, v23\n"
" v_pk_add_f32 v[20:21], v[20:21], v[64:65] op_sel_hi:[1,0]\n"
" v_pk_add_f32 v[22:23], v[22:23], v[64:65] op_sel_hi:[1,0]\n"
" v_pk_add_f32 v[60:61], v[60:61], v[20:21]\n"
" v_pk_fma_f32 v[62:63], v[20:21], v[20:21], v[62:63]\n"
" v_pk_add_f32 v[60:61], v[60:61], v[22:23]\n"
" v_pk_fma_f32 v[62:63], v[22:23], v[22:23], v[62:63]\n"
" v_permlane16_swap_b32 v48, v50\n"
" v_permlane16_swap_b32 v49, v51\n"
" s_waitcnt vmcnt(8)\n"
" v_mfma_f32_16x16x32_bf16 v[28:31], v[232:235], a[84:87], 0\n"
" s_waitcnt vmcnt(7)\n"
" v_mfma_f32_16x16x32_bf16 v[28:31], v[232:235], a[88:91], v[28:31]\n"
" s_waitcnt vmcnt(6)\n"
" v_mfma_f32_16x16x32_bf16 v[28:31], v[232:235], a[92:95], v[28:31]\n"
" v_cvt_pk_bf16_f32 v52, v24, v25\n"
" v_cvt_pk_bf16_f32 v53, v26, v27\n"
" v_pk_add_f32 v[24:25], v[24:25], v[64:65] op_sel_hi:[1,0]\n"
" v_pk_add_f32 v[26:27], v[26:27], v[64:65] op_sel_hi:[1,0]\n"
" v_pk_add_f32 v[60:61], v[60:61], v[24:25]\n"
" v_pk_fma_f32 v[62:63], v[24:25], v[24:25], v[62:63]\n"
" v_pk_add_f32 v[60:61], v[60:61], v[26:27]\n"
" v_pk_fma_f32 v[62:63], v[26:27], v[26:27], v[62:63]\n"
" s_waitcnt vmcnt(5)\n"
" v_mfma_f32_16x16x32_bf16 v[32:35], v[232:235], a[96:99], 0\n"
" s_waitcnt vmcnt(4)\n"
" v_mfma_f32_16x16x32_bf16 v[32:35], v[232:235], a[100:103], v[32:35]\n"
" s_waitcnt vmcnt(3)\n"
" v_mfma_f32_16x16x32_bf16 v[32:35], v[232:235], a[104:107], v[32:35]\n"
" v_cvt_pk_bf16_f32 v54, v28, v29\n"
" v_cvt_pk_bf16_f32 v55, v30, v31\n"
" v_pk_add_f32 v[28:29], v[28:29], v[64:65] op_sel_hi:[1,0]\n"
" v_pk_add_f32 v[30:31], v[30:31], v[64:65] op_sel_hi:[1,0]\n"
" v_pk_add_f32 v[60:61], v[60:61], v[28:29]\n"
" v_pk_fma_f32 v[62:63], v[28:29], v[28:29], v[62:63]\n"
" v_pk_add_f32 v[60:61], v[60:61], v[30:31]\n"
" v_pk_fma_f32 v[62:63], v[30:31], v[30:31], v[62:63]\n"
" v_permlane16_swap_b32 v52, v54\n"
" v_permlane16_swap_b32 v53, v55\n"
" s_waitcnt vmcnt(2)\n"
" v_mfma_f32_16x16x32_bf16 v[36:39], v[232:235], a[108:111], 0\n"
" s_waitcnt vmcnt(1)\n"
" v_mfma_f32_16x16x32_bf16 v[36:39], v[232:235], a[112:115], v[36:39]\n"
" s_waitcnt vmcnt(0)\n"
" v_mfma_f32_16x16x32_bf16 v[36:39], v[232:235], a[116:119], v[36:39]\n"
" v_cvt_pk_bf16_f32 v56, v32, v33\n"
" v_cvt_pk_bf16_f32 v57, v34, v35\n"
" v_pk_add_f32 v[32:33], v[32:33], v[64:65] op_sel_hi:[1,0]\n"
" v_pk_add_f32 v[34:35], v[34:35], v[64:65] op_sel_hi:[1,0]\n"
" v_pk_add_f32 v[60:61], v[60:61], v[32:33]\n"
" v_pk_fma_f32 v[62:63], v[32:33], v[32:33], v[62:63]\n"
" v_pk_add_f32 v[60:61], v[60:61], v[34:35]\n"
" v_pk_fma_f32 v[62:63], v[34:35], v[34:35], v[62:63]\n"
" s_add_i32 s77, s68, s70\n"
" buffer_load_dword v229, v227, s[48:51], 0 offen sc1\n"
" v_cvt_pk_bf16_f32 v58, v36, v37\n"
" v_cvt_pk_bf16_f32 v59, v38, v39\n"
" v_pk_add_f32 v[36:37], v[36:37], v[64:65] op_sel_hi:[1,0]\n"
" v_pk_add_f32 v[38:39], v[38:39], v[64:65] op_sel_hi:[1,0]\n"
" v_pk_add_f32 v[60:61], v[60:61], v[36:37]\n"
" v_pk_fma_f32 v[62:63], v[36:37], v[36:37], v[62:63]\n"
" v_pk_add_f32 v[60:61], v[60:61], v[38:39]\n"
" v_pk_fma_f32 v[62:63], v[38:39], v[38:39], v[62:63]\n"
" v_permlane16_swap_b32 v56, v58\n"
" v_permlane16_swap_b32 v57, v59\n"
" v_add_f32 v60, v60, v61\n"
" v_add_f32 v62, v62, v63\n"
" s_nop 0\n"
" v_add_f32_dpp v60, v60, v60 row_ror:4 row_mask:0xf bank_mask:0xf\n"
" v_add_f32_dpp v62, v62, v62 row_ror:4 row_mask:0xf bank_mask:0xf\n"
" s_nop 0\n"
" v_add_f32_dpp v60, v60, v60 row_ror:8 row_mask:0xf bank_mask:0xf\n"
" v_add_f32_dpp v62, v62, v62 row_ror:8 row_mask:0xf bank_mask:0xf\n"
" v_mov_b32 v66, v60\n"
" v_mov_b32 v67, v62\n"
" s_nop 0\n"
" v_permlane16_swap_b32 v60, v66\n"
" v_permlane16_swap_b32 v62, v67\n"
" v_add_f32 v60, v60, v66\n"
" v_add_f32 v62, v62, v67\n"
" v_mov_b32 v66, v60\n"
" v_mov_b32 v67, v62\n"
" s_nop 0\n"
" v_permlane32_swap_b32 v60, v66\n"
" v_permlane32_swap_b32 v62, v67\n"
" v_add_f32 v60, v60, v66\n"
" v_add_f32 v62, v62, v67\n"
" v_mul_f32 v68, 0x3acccccd, v60\n"
" v_mul_f32 v66, v68, v68\n"
" v_fma_f32 v66, v62, s98, -v66\n"
" v_max_f32 v66, 0, v66\n"
" v_add_f32 v66, 0x3727c5ac, v66\n"
" v_rsq_f32 v70, v66\n"
" s_add_i32 s76, s95, 4096\n"
" v_mul_f32 v68, v68, v70\n"
" v_pk_fma_f32 v[0:1], v[0:1], v[70:71], v[68:69] op_sel_hi:[1,0,0] neg_lo:[0,0,1] neg_hi:[0,0,1]\n"
" v_pk_fma_f32 v[2:3], v[2:3], v[70:71], v[68:69] op_sel_hi:[1,0,0] neg_lo:[0,0,1] neg_hi:[0,0,1]\n"
" v_pk_fma_f32 v[4:5], v[4:5], v[70:71], v[68:69] op_sel_hi:[1,0,0] neg_lo:[0,0,1] neg_hi:[0,0,1]\n"
" v_pk_fma_f32 v[6:7], v[6:7], v[70:71], v[68:69] op_sel_hi:[1,0,0] neg_lo:[0,0,1] neg_hi:[0,0,1]\n"
" v_cvt_pk_bf16_f32 v72, v0, v1\n"
" v_cvt_pk_bf16_f32 v74, v4, v5\n"
" v_cvt_pk_bf16_f32 v73, v2, v3\n"
" v_cvt_pk_bf16_f32 v75, v6, v7\n"
" v_permlane16_swap_b32 v72, v74\n"
" s_nop 0\n"
" v_permlane16_swap_b32 v73, v75\n"
" buffer_store_dwordx4 v[72:75], v226, s[52:55], s95 offen offset:0 sc1\n"
" v_pk_fma_f32 v[8:9], v[8:9], v[70:71], v[68:69] op_sel_hi:[1,0,0] neg_lo:[0,0,1] neg_hi:[0,0,1]\n"
" v_pk_fma_f32 v[10:11], v[10:11], v[70:71], v[68:69] op_sel_hi:[1,0,0] neg_lo:[0,0,1] neg_hi:[0,0,1]\n"
" v_pk_fma_f32 v[12:13], v[12:13], v[70:71], v[68:69] op_sel_hi:[1,0,0] neg_lo:[0,0,1] neg_hi:[0,0,1]\n"
" v_pk_fma_f32 v[14:15], v[14:15], v[70:71], v[68:69] op_sel_hi:[1,0,0] neg_lo:[0,0,1] neg_hi:[0,0,1]\n"
" v_cvt_pk_bf16_f32 v76, v8, v9\n"
" v_cvt_pk_bf16_f32 v78, v12, v13\n"
" v_cvt_pk_bf16_f32 v77, v10, v11\n"
" v_cvt_pk_bf16_f32 v79, v14, v15\n"
" v_permlane16_swap_b32 v76, v78\n"
" s_nop 0\n"
" v_permlane16_swap_b32 v77, v79\n"
" buffer_store_dwordx4 v[76:79], v226, s[52:55], s95 offen offset:1024 sc1\n"
" v_pk_fma_f32 v[16:17], v[16:17], v[70:71], v[68:69] op_sel_hi:[1,0,0] neg_lo:[0,0,1] neg_hi:[0,0,1]\n"
" v_pk_fma_f32 v[18:19], v[18:19], v[70:71], v[68:69] op_sel_hi:[1,0,0] neg_lo:[0,0,1] neg_hi:[0,0,1]\n"
" v_pk_fma_f32 v[20:21], v[20:21], v[70:71], v[68:69] op_sel_hi:[1,0,0] neg_lo:[0,0,1] neg_hi:[0,0,1]\n"
" v_pk_fma_f32 v[22:23], v[22:23], v[70:71], v[68:69] op_sel_hi:[1,0,0] neg_lo:[0,0,1] neg_hi:[0,0,1]\n"
" v_cvt_pk_bf16_f32 v72, v16, v17\n"
" v_cvt_pk_bf16_f32 v74, v20, v21\n"
" v_cvt_pk_bf16_f32 v73, v18, v19\n"
" v_cvt_pk_bf16_f32 v75, v22, v23\n"
" v_permlane16_swap_b32 v72, v74\n"
" s_nop 0\n"
" v_permlane16_swap_b32 v73, v75\n"
" buffer_store_dwordx4 v[72:75], v226, s[52:55], s95 offen offset:2048 sc1\n"
" v_pk_fma_f32 v[24:25], v[24:25], v[70:71], v[68:69] op_sel_hi:[1,0,0] neg_lo:[0,0,1] neg_hi:[0,0,1]\n"
" v_pk_fma_f32 v[26:27], v[26:27], v[70:71], v[68:69] op_sel_hi:[1,0,0] neg_lo:[0,0,1] neg_hi:[0,0,1]\n"
" v_pk_fma_f32 v[28:29], v[28:29], v[70:71], v[68:69] op_sel_hi:[1,0,0] neg_lo:[0,0,1] neg_hi:[0,0,1]\n"
" v_pk_fma_f32 v[30:31], v[30:31], v[70:71], v[68:69] op_sel_hi:[1,0,0] neg_lo:[0,0,1] neg_hi:[0,0,1]\n"
" v_cvt_pk_bf16_f32 v76, v24, v25\n"
" v_cvt_pk_bf16_f32 v78, v28, v29\n"
" v_cvt_pk_bf16_f32 v77, v26, v27\n"
" v_cvt_pk_bf16_f32 v79, v30, v31\n"
" v_permlane16_swap_b32 v76, v78\n"
" s_nop 0\n"
" v_permlane16_swap_b32 v77, v79\n"
" buffer_store_dwordx4 v[76:79], v226, s[52:55], s95 offen offset:3072 sc1\n"
" v_pk_fma_f32 v[32:33], v[32:33], v[70:71], v[68:69] op_sel_hi:[1,0,0] neg_lo:[0,0,1] neg_hi:[0,0,1]\n"
" v_pk_fma_f32 v[34:35], v[34:35], v[70:71], v[68:69] op_sel_hi:[1,0,0] neg_lo:[0,0,1] neg_hi:[0,0,1]\n"
" v_pk_fma_f32 v[36:37], v[36:37], v[70:71], v[68:69] op_sel_hi:[1,0,0] neg_lo:[0,0,1] neg_hi:[0,0,1]\n"
" v_pk_fma_f32 v[38:39], v[38:39], v[70:71], v[68:69] op_sel_hi:[1,0,0] neg_lo:[0,0,1] neg_hi:[0,0,1]\n"
" v_cvt_pk_bf16_f32 v72, v32, v33\n"
" v_cvt_pk_bf16_f32 v74, v36, v37\n"
" v_cvt_pk_bf16_f32 v73, v34, v35\n"
" v_cvt_pk_bf16_f32 v75, v38, v39\n"
" v_permlane16_swap_b32 v72, v74\n"
" s_nop 0\n"
" v_permlane16_swap_b32 v73, v75\n"
" buffer_store_dwordx4 v[72:75], v226, s[52:55], s76 offen offset:0 sc1\n"
" s_add_i32 s76, s94, 4096\n"
" buffer_store_dwordx4 v[40:43], v226, s[40:43], s94 offen offset:0 sc1\n"
" buffer_store_dwordx4 v[44:47], v226, s[40:43], s94 offen offset:1024 sc1\n"
" buffer_store_dwordx4 v[48:51], v226, s[40:43], s94 offen offset:2048 sc1\n"
" buffer_store_dwordx4 v[52:55], v226, s[40:43], s94 offen offset:3072 sc1\n"
" buffer_store_dwordx4 v[56:59], v226, s[40:43], s76 offen offset:0 sc1\n"
" s_mov_b32 s72, 0\n"
" s_mov_b32 s71, s68\n"
" v_xor_b32 v228, 0x80, v228\n"
" s_waitcnt vmcnt(10)\n"
" v_cmp_gt_i32 vcc, s77, v229\n"
" s_nop 0\n"
" s_cbranch_vccnz .Lr1_nx_%=\n"
" s_and_b32 s92, s77, 15\n"
" s_lshl_b32 s92, s92, 21\n"
" s_add_i32 s92, s92, s85\n"
" s_lshr_b32 s76, s92, 21\n"
" s_bitcmp1_b64 s[86:87], s76\n"
" s_cselect_b32 s76, 8192, 0\n"
" s_add_i32 s92, s92, s76\n"
" s_and_b32 s93, s77, s73\n"
" s_lshl_b32 s93, s93, 17\n"
" s_add_i32 s93, s93, s88\n"
" s_lshr_b32 s76, s93, 21\n"
" s_bitcmp1_b64 s[86:87], s76\n"
" s_cselect_b32 s76, 8192, 0\n"
" s_add_i32 s93, s93, s76\n"
" s_add_i32 s78, s92, 0\n"
" buffer_load_dwordx4 a[0:3], v224, s[36:39], s78 offen offset:0 sc1\n"
" s_add_i32 s79, s92, 32768\n"
" buffer_load_dwordx4 a[4:7], v224, s[36:39], s79 offen offset:0 sc1\n"
" buffer_load_dwordx4 a[8:11], v225, s[44:47], s93 offen offset:0 sc1\n"
" buffer_load_dwordx4 a[12:15], v224, s[36:39], s78 offen offset:512 sc1\n"
" buffer_load_dwordx4 a[16:19], v224, s[36:39], s79 offen offset:512 sc1\n"
" buffer_load_dwordx4 a[20:23], v225, s[44:47], s93 offen offset:512 sc1\n"
" buffer_load_dwordx4 a[24:27], v224, s[36:39], s78 offen offset:1024 sc1\n"
" buffer_load_dwordx4 a[28:31], v224, s[36:39], s79 offen offset:1024 sc1\n"
" buffer_load_dwordx4 a[32:35], v225, s[44:47], s93 offen offset:1024 sc1\n"
" buffer_load_dwordx4 a[36:39], v224, s[36:39], s78 offen offset:1536 sc1\n"
" buffer_load_dwordx4 a[40:43], v224, s[36:39], s79 offen offset:1536 sc1\n"
" buffer_load_dwordx4 a[44:47], v225, s[44:47], s93 offen offset:1536 sc1\n"
" buffer_load_dwordx4 a[48:51], v224, s[36:39], s78 offen offset:2048 sc1\n"
" buffer_load_dwordx4 a[52:55], v224, s[36:39], s79 offen offset:2048 sc1\n"
" buffer_load_dwordx4 a[56:59], v225, s[44:47], s93 offen offset:2048 sc1\n"
" buffer_load_dwordx4 a[60:63], v224, s[36:39], s78 offen offset:2560 sc1\n"
" buffer_load_dwordx4 a[64:67], v224, s[36:39], s79 offen offset:2560 sc1\n"
" buffer_load_dwordx4 a[68:71], v225, s[44:47], s93 offen offset:2560 sc1\n"
" buffer_load_dwordx4 a[72:75], v224, s[36:39], s78 offen offset:3072 sc1\n"
" buffer_load_dwordx4 a[76:79], v224, s[36:39], s79 offen offset:3072 sc1\n"
" buffer_load_dwordx4 a[80:83], v225, s[44:47], s93 offen offset:3072 sc1\n"
" buffer_load_dwordx4 a[84:87], v224, s[36:39], s78 offen offset:3584 sc1\n"
" buffer_load_dwordx4 a[88:91], v224, s[36:39], s79 offen offset:3584 sc1\n"
" buffer_load_dwordx4 a[92:95], v225, s[44:47], s93 offen offset:3584 sc1\n"
" s_add_i32 s78, s92, 4096\n"
" buffer_load_dwordx4 a[96:99], v224, s[36:39], s78 offen offset:0 sc1\n"
" s_add_i32 s79, s92, 36864\n"
" buffer_load_dwordx4 a[100:103], v224, s[36:39], s79 offen offset:0 sc1\n"
" s_add_i32 s93, s93, 4096\n"
" buffer_load_dwordx4 a[104:107], v225, s[44:47], s93 offen offset:0 sc1\n"
" buffer_load_dwordx4 a[108:111], v224, s[36:39], s78 offen offset:512 sc1\n"
" buffer_load_dwordx4 a[112:115], v224, s[36:39], s79 offen offset:512 sc1\n"
" buffer_load_dwordx4 a[116:119], v225, s[44:47], s93 offen offset:512 sc1\n"
" s_mov_b32 s72, 1\n"
" s_waitcnt vmcnt(30)\n"
" v_mov_b32 v66, s71\n"
" buffer_store_dword v66, v228, s[48:51], 0 offen sc1\n"
".Lr1_nx_%=:\n"
" s_mov_b32 s68, s77\n"
" s_cmp_lt_u32 s68, 64\n"
" s_cbranch_scc1 .Lr1_loop_%=\n"
" s_waitcnt vmcnt(0)\n"
" v_mov_b32 v66, s71\n"
" buffer_store_dword v66, v228, s[48:51], 0 offen sc1\n"
" s_waitcnt vmcnt(0)\n"
      RED_OPS);
  else if (KIND == 2) asm volatile(
" s_nop 4\n"
" s_mov_b32 s90, 0\n"
" s_mov_b32 s72, 0\n"
" s_mov_b32 s71, -1\n"
" s_mov_b32 s91, 0\n"
".Lr2_pol_%=:\n"
" buffer_load_dwordx2 v[220:221], v229, s[48:51], 0 offen sc1\n"
" s_waitcnt vmcnt(0)\n"
" v_or_b32 v222, 1, v220\n"
" v_mov_b32 v223, v221\n"
" v_cmp_ne_u64 vcc, s[16:17], v[222:223]\n"
" s_nop 0\n"
" s_cbranch_vccz .Lr2_polok_%=\n"
" s_add_i32 s91, s91, 1\n"
" s_or_b32 s91, s91, s90\n"
" s_cmp_ge_u32 s91, 65536\n"
" s_cselect_b32 s90, 65536, 0\n"
" s_cbranch_scc1 .Lr2_polok_%=\n"
" s_sleep 4\n"
" s_branch .Lr2_pol_%=\n"
".Lr2_polok_%=:\n"
" v_and_b32 v220, 1, v220\n"
" v_cmp_ne_u32_e64 s[86:87], 1, v220\n"
" s_nop 1\n"
" s_bitcmp1_b32 s86, 0\n"
" s_cselect_b32 s76, 8192, 0\n"
" s_add_u32 s48, s48, s76\n"
".Lr2_loop_%=:\n"
" s_cmp_lg_u32 s72, 0\n"
" s_cbranch_scc1 .Lr2_have_%=\n"
" s_mov_b32 s91, 0\n"
" buffer_load_dword v229, v227, s[48:51], 0 offen sc1\n"
" s_waitcnt vmcnt(0)\n"
" v_mov_b32 v66, s71\n"
" buffer_store_dword v66, v228, s[48:51], 0 offen sc1\n"
".Lr2_in_%=:\n"
" v_cmp_gt_i32 vcc, s68, v229\n"
" s_nop 0\n"
" s_cbranch_vccz .Lr2_inok_%=\n"
" s_add_i32 s91, s91, 1\n"
" s_or_b32 s91, s91, s90\n"
" s_cmp_ge_u32 s91, 65536\n"
" s_cselect_b32 s90, 65536, 0\n"
" s_cbranch_scc1 .Lr2_inok_%=\n"
" s_sleep 2\n"
" buffer_load_dword v229, v227, s[48:51], 0 offen sc1\n"
" s_waitcnt vmcnt(0)\n"
" s_branch .Lr2_in_%=\n"
".Lr2_inok_%=:\n"
" s_and_b32 s92, s68, 15\n"
" s_lshl_b32 s92, s92, 21\n"
" s_add_i32 s92, s92, s85\n"
" s_lshr_b32 s76, s92, 21\n"
" s_bitcmp1_b64 s[86:87], s76\n"
" s_cselect_b32 s76, 8192, 0\n"
" s_add_i32 s92, s92, s76\n"
" s_add_i32 s78, s92, 0\n"
" buffer_load_dwordx4 a[0:3], v224, s[36:39], s78 offen offset:0 sc1\n"
" s_add_i32 s79, s92, 32768\n"
" buffer_load_dwordx4 a[4:7], v224, s[36:39], s79 offen offset:0 sc1\n"
" s_add_i32 s80, s92, 65536\n"
" buffer_load_dwordx4 a[8:11], v224, s[36:39], s80 offen offset:0 sc1\n"
" s_add_i32 s81, s92, 98304\n"
" buffer_load_dwordx4 a[12:15], v224, s[36:39], s81 offen offset:0 sc1\n"
" s_add_i32 s82, s92, 131072\n"
" buffer_load_dwordx4 a[16:19], v224, s[36:39], s82 offen offset:0 sc1\n"
" buffer_load_dwordx4 a[20:23], v224, s[36:39], s78 offen offset:512 sc1\n"
" buffer_load_dwordx4 a[24:27], v224, s[36:39], s79 offen offset:512 sc1\n"
" buffer_load_dwordx4 a[28:31], v224, s[36:39], s80 offen offset:512 sc1\n"
" buffer_load_dwordx4 a[32:35], v224, s[36:39], s81 offen offset:512 sc1\n"
" buffer_load_dwordx4 a[36:39], v224, s[36:39], s82 offen offset:512 sc1\n"
" buffer_load_dwordx4 a[40:43], v224, s[36:39], s78 offen offset:1024 sc1\n"
" buffer_load_dwordx4 a[44:47], v224, s[36:39], s79 offen offset:1024 sc1\n"
" buffer_load_dwordx4 a[48:51], v224, s[36:39], s80 offen offset:1024 sc1\n"
" buffer_load_dwordx4 a[52:55], v224, s[36:39], s81 offen offset:1024 sc1\n"
" buffer_load_dwordx4 a[56:59], v224, s[36:39], s82 offen offset:1024 sc1\n"
" buffer_load_dwordx4 a[60:63], v224, s[36:39], s78 offen offset:1536 sc1\n"
" buffer_load_dwordx4 a[64:67], v224, s[36:39], s79 offen offset:1536 sc1\n"
" buffer_load_dwordx4 a[68:71], v224, s[36:39], s80 offen offset:1536 sc1\n"
" buffer_load_dwordx4 a[72:75], v224, s[36:39], s81 offen offset:1536 sc1\n"
" buffer_load_dwordx4 a[76:79], v224, s[36:39], s82 offen offset:1536 sc1\n"
" buffer_load_dwordx4 a[80:83], v224, s[36:39], s78 offen offset:2048 sc1\n"
" buffer_load_dwordx4 a[84:87], v224, s[36:39], s79 offen offset:2048 sc1\n"
" buffer_load_dwordx4 a[88:91], v224, s[36:39], s80 offen offset:2048 sc1\n"
" buffer_load_dwordx4 a[92:95], v224, s[36:39], s81 offen offset:2048 sc1\n"
" buffer_load_dwordx4 a[96:99], v224, s[36:39], s82 offen offset:2048 sc1\n"
" buffer_load_dwordx4 a[100:103], v224, s[36:39], s78 offen offset:2560 sc1\n"
" buffer_load_dwordx4 a[104:107], v224, s[36:39], s79 offen offset:2560 sc1\n"
" buffer_load_dwordx4 a[108:111], v224, s[36:39], s80 offen offset:2560 sc1\n"
" buffer_load_dwordx4 a[112:115], v224, s[36:39], s81 offen offset:2560 sc1\n"
" buffer_load_dwordx4 a[116:119], v224, s[36:39], s82 offen offset:2560 sc1\n"
" buffer_load_dwordx4 a[120:123], v224, s[36:39], s78 offen offset:3072 sc1\n"
" buffer_load_dwordx4 a[124:127], v224, s[36:39], s79 offen offset:3072 sc1\n"
" buffer_load_dwordx4 a[128:131], v224, s[36:39], s80 offen offset:3072 sc1\n"
" buffer_load_dwordx4 a[132:135], v224, s[36:39], s81 offen offset:3072 sc1\n"
" buffer_load_dwordx4 a[136:139], v224, s[36:39], s82 offen offset:3072 sc1\n"
" buffer_load_dwordx4 a[140:143], v224, s[36:39], s78 offen offset:3584 sc1\n"
" buffer_load_dwordx4 a[144:147], v224, s[36:39], s79 offen offset:3584 sc1\n"
" buffer_load_dwordx4 a[148:151], v224, s[36:39], s80 offen offset:3584 sc1\n"
" buffer_load_dwordx4 a[152:155], v224, s[36:39], s81 offen offset:3584 sc1\n"
" buffer_load_dwordx4 a[156:159], v224, s[36:39], s82 offen offset:3584 sc1\n"
" s_add_i32 s78, s92, 4096\n"
" buffer_load_dwordx4 a[160:163], v224, s[36:39], s78 offen offset:0 sc1\n"
" s_add_i32 s79, s92, 36864\n"
" buffer_load_dwordx4 a[164:167], v224, s[36:39], s79 offen offset:0 sc1\n"
" s_add_i32 s80, s92, 69632\n"
" buffer_load_dwordx4 a[168:171], v224, s[36:39], s80 offen offset:0 sc1\n"
" s_add_i32 s81, s92, 102400\n"
" buffer_load_dwordx4 a[172:175], v224, s[36:39], s81 offen offset:0 sc1\n"
" s_add_i32 s82, s92, 135168\n"
" buffer_load_dwordx4 a[176:179], v224, s[36:39], s82 offen offset:0 sc1\n"
" buffer_load_dwordx4 a[180:183], v224, s[36:39], s78 offen offset:512 sc1\n"
" buffer_load_dwordx4 a[184:187], v224, s[36:39], s79 offen offset:512 sc1\n"
" buffer_load_dwordx4 a[188:191], v224, s[36:39], s80 offen offset:512 sc1\n"
" buffer_load_dwordx4 a[192:195], v224, s[36:39], s81 offen offset:512 sc1\n"
" buffer_load_dwordx4 a[196:199], v224, s[36:39], s82 offen offset:512 sc1\n"
".Lr2_have_%=:\n"
" s_and_b32 s94, s68, s73\n"
" s_lshl_b32 s94, s94, 17\n"
" s_add_i32 s94, s94, s88\n"
" s_lshr_b32 s76, s94, 21\n"
" s_bitcmp1_b64 s[86:87], s76\n"
" s_cselect_b32 s76, 8192, 0\n"
" s_add_i32 s94, s94, s76\n"
" s_and_b32 s95, s68, s73\n"
" s_lshl_b32 s95, s95, 17\n"
" s_add_i32 s95, s95, s84\n"
" s_lshr_b32 s76, s95, 21\n"
" s_bitcmp1_b64 s[86:87], s76\n"
" s_cselect_b32 s76, 8192, 0\n"
" s_add_i32 s95, s95, s76\n"
" s_waitcnt vmcnt(49)\n"
" v_mfma_f32_16x16x32_bf16 v[0:3], v[232:235], a[0:3], 0\n"
" s_waitcnt vmcnt(48)\n"
" v_mfma_f32_16x16x32_bf16 v[0:3], v[232:235], a[4:7], v[0:3]\n"
" s_waitcnt vmcnt(47)\n"
" v_mfma_f32_16x16x32_bf16 v[0:3], v[232:235], a[8:11], v[0:3]\n"
" s_waitcnt vmcnt(46)\n"
" v_mfma_f32_16x16x32_bf16 v[0:3], v[232:235], a[12:15], v[0:3]\n"
" s_waitcnt vmcnt(45)\n"
" v_mfma_f32_16x16x32_bf16 v[0:3], v[232:235], a[16:19], v[0:3]\n"
" s_waitcnt vmcnt(44)\n"
" v_mfma_f32_16x16x32_bf16 v[4:7], v[232:235], a[20:23], 0\n"
" s_waitcnt vmcnt(43)\n"
" v_mfma_f32_16x16x32_bf16 v[4:7], v[232:235], a[24:27], v[4:7]\n"
" s_waitcnt vmcnt(42)\n"
" v_mfma_f32_16x16x32_bf16 v[4:7], v[232:235], a[28:31], v[4:7]\n"
" s_waitcnt vmcnt(41)\n"
" v_mfma_f32_16x16x32_bf16 v[4:7], v[232:235], a[32:35], v[4:7]\n"
" s_waitcnt vmcnt(40)\n"
" v_mfma_f32_16x16x32_bf16 v[4:7], v[232:235], a[36:39], v[4:7]\n"
" v_cvt_pk_bf16_f32 v40, v0, v1\n"
" v_cvt_pk_bf16_f32 v41, v2, v3\n"
" v_add_f32 v64, v0, v1\n"
" v_add_f32 v66, v2, v3\n"
" v_add_f32 v64, v64, v66\n"
" s_nop 1\n"
" v_add_f32_dpp v64, v64, v64 row_ror:4 row_mask:0xf bank_mask:0xf\n"
" s_nop 1\n"
" v_add_f32_dpp v64, v64, v64 row_ror:8 row_mask:0xf bank_mask:0xf\n"
" v_mov_b32 v66, v64\n"
" s_nop 1\n"
" v_permlane16_swap_b32 v64, v66\n"
" v_add_f32 v64, v64, v66\n"
" v_mov_b32 v66, v64\n"
" s_nop 1\n"
" v_permlane32_swap_b32 v64, v66\n"
" v_add_f32 v64, v64, v66\n"
" v_mul_f32 v64, 0xbc800000, v64\n"
" v_pk_add_f32 v[0:1], v[0:1], v[64:65] op_sel_hi:[1,0]\n"
" v_pk_add_f32 v[2:3], v[2:3], v[64:65] op_sel_hi:[1,0]\n"
" v_pk_add_f32 v[60:61], v[0:1], v[2:3]\n"
" v_pk_mul_f32 v[62:63], v[0:1], v[0:1]\n"
" v_pk_fma_f32 v[62:63], v[2:3], v[2:3], v[62:63]\n"
" s_waitcnt vmcnt(39)\n"
" v_mfma_f32_16x16x32_bf16 v[8:11], v[232:235], a[40:43], 0\n"
" s_waitcnt vmcnt(38)\n"
" v_mfma_f32_16x16x32_bf16 v[8:11], v[232:235], a[44:47], v[8:11]\n"
" s_waitcnt vmcnt(37)\n"
" v_mfma_f32_16x16x32_bf16 v[8:11], v[232:235], a[48:51], v[8:11]\n"
" s_waitcnt vmcnt(36)\n"
" v_mfma_f32_16x16x32_bf16 v[8:11], v[232:235], a[52:55], v[8:11]\n"
" s_waitcnt vmcnt(35)\n"
" v_mfma_f32_16x16x32_bf16 v[8:11], v[232:235], a[56:59], v[8:11]\n"
" v_cvt_pk_bf16_f32 v42, v4, v5\n"
" v_cvt_pk_bf16_f32 v43, v6, v7\n"
" v_pk_add_f32 v[4:5], v[4:5], v[64:65] op_sel_hi:[1,0]\n"
" v_pk_add_f32 v[6:7], v[6:7], v[64:65] op_sel_hi:[1,0]\n"
" v_pk_add_f32 v[60:61], v[60:61], v[4:5]\n"
" v_pk_fma_f32 v[62:63], v[4:5], v[4:5], v[62:63]\n"
" v_pk_add_f32 v[60:61], v[60:61], v[6:7]\n"
" v_pk_fma_f32 v[62:63], v[6:7], v[6:7], v[62:63]\n"
" v_permlane16_swap_b32 v40, v42\n"
" v_permlane16_swap_b32 v41, v43\n"
" s_waitcnt vmcnt(34)\n"
" v_mfma_f32_16x16x32_bf16 v[12:15], v[232:235], a[60:63], 0\n"
" s_waitcnt vmcnt(33)\n"
" v_mfma_f32_16x16x32_bf16 v[12:15], v[232:235], a[64:67], v[12:15]\n"
" s_waitcnt vmcnt(32)\n"
" v_mfma_f32_16x16x32_bf16 v[12:15], v[232:235], a[68:71], v[12:15]\n"
" s_waitcnt vmcnt(31)\n"
" v_mfma_f32_16x16x32_bf16 v[12:15], v[232:235], a[72:75], v[12:15]\n"
" s_waitcnt vmcnt(30)\n"
" v_mfma_f32_16x16x32_bf16 v[12:15], v[232:235], a[76:79], v[12:15]\n"
" v_cvt_pk_bf16_f32 v44, v8, v9\n"
" v_cvt_pk_bf16_f32 v45, v10, v11\n"
" v_pk_add_f32 v[8:9], v[8:9], v[64:65] op_sel_hi:[1,0]\n"
" v_pk_add_f32 v[10:11], v[10:11], v[64:65] op_sel_hi:[1,0]\n"
" v_pk_add_f32 v[60:61], v[60:61], v[8:9]\n"
" v_pk_fma_f32 v[62:63], v[8:9], v[8:9], v[62:63]\n"
" v_pk_add_f32 v[60:61], v[60:61], v[10:11]\n"
" v_pk_fma_f32 v[62:63], v[10:11], v[10:11], v[62:63]\n"
" s_waitcnt vmcnt(29)\n"
" v_mfma_f32_16x16x32_bf16 v[16:19], v[232:235], a[80:83], 0\n"
" s_waitcnt vmcnt(28)\n"
" v_mfma_f32_16x16x32_bf16 v[16:19], v[232:235], a[84:87], v[16:19]\n"
" s_waitcnt vmcnt(27)\n"
" v_mfma_f32_16x16x32_bf16 v[16:19], v[232:235], a[88:91], v[16:19]\n"
" s_waitcnt vmcnt(26)\n"
" v_mfma_f32_16x16x32_bf16 v[16:19], v[232:235], a[92:95], v[16:19]\n"
" s_waitcnt vmcnt(25)\n"
" v_mfma_f32_16x16x32_bf16 v[16:19], v[232:235], a[96:99], v[16:19]\n"
" v_cvt_pk_bf16_f32 v46, v12, v13\n"
" v_cvt_pk_bf16_f32 v47, v14, v15\n"
" v_pk_add_f32 v[12:13], v[12:13], v[64:65] op_sel_hi:[1,0]\n"
" v_pk_add_f32 v[14:15], v[14:15], v[64:65] op_sel_hi:[1,0]\n"
" v_pk_add_f32 v[60:61], v[60:61], v[12:13]\n"
" v_pk_fma_f32 v[62:63], v[12:13], v[12:13], v[62:63]\n"
" v_pk_add_f32 v[60:61], v[60:61], v[14:15]\n"
" v_pk_fma_f32 v[62:63], v[14:15], v[14:15], v[62:63]\n"
" v_permlane16_swap_b32 v44, v46\n"
" v_permlane16_swap_b32 v45, v47\n"
" s_waitcnt vmcnt(24)\n"
" v_mfma_f32_16x16x32_bf16 v[20:23], v[232:235], a[100:103], 0\n"
" s_waitcnt vmcnt(23)\n"
" v_mfma_f32_16x16x32_bf16 v[20:23], v[232:235], a[104:107], v[20:23]\n"
" s_waitcnt vmcnt(22)\n"
" v_mfma_f32_16x16x32_bf16 v[20:23], v[232:235], a[108:111], v[20:23]\n"
" s_waitcnt vmcnt(21)\n"
" v_mfma_f32_16x16x32_bf16 v[20:23], v[232:235], a[112:115], v[20:23]\n"
" s_waitcnt vmcnt(20)\n"
" v_mfma_f32_16x16x32_bf16 v[20:23], v[232:235], a[116:119], v[20:23]\n"
" v_cvt_pk_bf16_f32 v48, v16, v17\n"
" v_cvt_pk_bf16_f32 v49, v18, v19\n"
" v_pk_add_f32 v[16:17], v[16:17], v[64:65] op_sel_hi:[1,0]\n"
" v_pk_add_f32 v[18:19], v[18:19], v[64:65] op_sel_hi:[1,0]\n"
" v_pk_add_f32 v[60:61], v[60:61], v[16:17]\n"
" v_pk_fma_f32 v[62:63], v[16:17], v[16:17], v[62:63]\n"
" v_pk_add_f32 v[60:61], v[60:61], v[18:19]\n"
" v_pk_fma_f32 v[62:63], v[18:19], v[18:19], v[62:63]\n"
" s_waitcnt vmcnt(19)\n"
" v_mfma_f32_16x16x32_bf16 v[24:27], v[232:235], a[120:123], 0\n"
" s_waitcnt vmcnt(18)\n"
" v_mfma_f32_16x16x32_bf16 v[24:27], v[232:235], a[124:127], v[24:27]\n"
" s_waitcnt vmcnt(17)\n"
" v_mfma_f32_16x16x32_bf16 v[24:27], v[232:235], a[128:131], v[24:27]\n"
" s_waitcnt vmcnt(16)\n"
" v_mfma_f32_16x16x32_bf16 v[24:27], v[232:235], a[132:135], v[24:27]\n"
" s_waitcnt vmcnt(15)\n"
" v_mfma_f32_16x16x32_bf16 v[24:27], v[232:235], a[136:139], v[24:27]\n"
" v_cvt_pk_bf16_f32 v50, v20, v21\n"
" v_cvt_pk_bf16_f32 v51, v22, v23\n"
" v_pk_add_f32 v[20:21], v[20:21], v[64:65] op_sel_hi:[1,0]\n"
" v_pk_add_f32 v[22:23], v[22:23], v[64:65] op_sel_hi:[1,0]\n"
" v_pk_add_f32 v[60:61], v[60:61], v[20:21]\n"
" v_pk_fma_f32 v[62:63], v[20:21], v[20:21], v[62:63]\n"
" v_pk_add_f32 v[60:61], v[60:61], v[22:23]\n"
" v_pk_fma_f32 v[62:63], v[22:23], v[22:23], v[62:63]\n"
" v_permlane16_swap_b32 v48, v50\n"
" v_permlane16_swap_b32 v49, v51\n"
" s_waitcnt vmcnt(14)\n"
" v_mfma_f32_16x16x32_bf16 v[28:31], v[232:235], a[140:143], 0\n"
" s_waitcnt vmcnt(13)\n"
" v_mfma_f32_16x16x32_bf16 v[28:31], v[232:235], a[144:147], v[28:31]\n"
" s_waitcnt vmcnt(12)\n"
" v_mfma_f32_16x16x32_bf16 v[28:31], v[232:235], a[148:151], v[28:31]\n"
" s_waitcnt vmcnt(11)\n"
" v_mfma_f32_16x16x32_bf16 v[28:31], v[232:235], a[152:155], v[28:31]\n"
" s_waitcnt vmcnt(10)\n"
" v_mfma_f32_16x16x32_bf16 v[28:31], v[232:235], a[156:159], v[28:31]\n"
" v_cvt_pk_bf16_f32 v52, v24, v25\n"
" v_cvt_pk_bf16_f32 v53, v26, v27\n"
" v_pk_add_f32 v[24:25], v[24:25], v[64:65] op_sel_hi:[1,0]\n"
" v_pk_add_f32 v[26:27], v[26:27], v[64:65] op_sel_hi:[1,0]\n"
" v_pk_add_f32 v[60:61], v[60:61], v[24:25]\n"
" v_pk_fma_f32 v[62:63], v[24:25], v[24:25], v[62:63]\n"
" v_pk_add_f32 v[60:61], v[60:61], v[26:27]\n"
" v_pk_fma_f32 v[62:63], v[26:27], v[26:27], v[62:63]\n"
" s_waitcnt vmcnt(9)\n"
" v_mfma_f32_16x16x32_bf16 v[32:35], v[232:235], a[160:163], 0\n"
" s_waitcnt vmcnt(8)\n"
" v_mfma_f32_16x16x32_bf16 v[32:35], v[232:235], a[164:167], v[32:35]\n"
" s_waitcnt vmcnt(7)\n"
" v_mfma_f32_16x16x32_bf16 v[32:35], v[232:235], a[168:171], v[32:35]\n"
" s_waitcnt vmcnt(6)\n"
" v_mfma_f32_16x16x32_bf16 v[32:35], v[232:235], a[172:175], v[32:35]\n"
" s_waitcnt vmcnt(5)\n"
" v_mfma_f32_16x16x32_bf16 v[32:35], v[232:235], a[176:179], v[32:35]\n"
" v_cvt_pk_bf16_f32 v54, v28, v29\n"
" v_cvt_pk_bf16_f32 v55, v30, v31\n"
" v_pk_add_f32 v[28:29], v[28:29], v[64:65] op_sel_hi:[1,0]\n"
" v_pk_add_f32 v[30:31], v[30:31], v[64:65] op_sel_hi:[1,0]\n"
" v_pk_add_f32 v[60:61], v[60:61], v[28:29]\n"
" v_pk_fma_f32 v[62:63], v[28:29], v[28:29], v[62:63]\n"
" v_pk_add_f32 v[60:61], v[60:61], v[30:31]\n"
" v_pk_fma_f32 v[62:63], v[30:31], v[30:31], v[62:63]\n"
" v_permlane16_swap_b32 v52, v54\n"
" v_permlane16_swap_b32 v53, v55\n"
" s_waitcnt vmcnt(4)\n"
" v_mfma_f32_16x16x32_bf16 v[36:39], v[232:235], a[180:183], 0\n"
" s_waitcnt vmcnt(3)\n"
" v_mfma_f32_16x16x32_bf16 v[36:39], v[232:235], a[184:187], v[36:39]\n"
" s_waitcnt vmcnt(2)\n"
" v_mfma_f32_16x16x32_bf16 v[36:39], v[232:235], a[188:191], v[36:39]\n"
" s_waitcnt vmcnt(1)\n"
" v_mfma_f32_16x16x32_bf16 v[36:39], v[232:235], a[192:195], v[36:39]\n"
" s_waitcnt vmcnt(0)\n"
" v_mfma_f32_16x16x32_bf16 v[36:39], v[232:235], a[196:199], v[36:39]\n"
" v_cvt_pk_bf16_f32 v56, v32, v33\n"
" v_cvt_pk_bf16_f32 v57, v34, v35\n"
" v_pk_add_f32 v[32:33], v[32:33], v[64:65] op_sel_hi:[1,0]\n"
" v_pk_add_f32 v[34:35], v[34:35], v[64:65] op_sel_hi:[1,0]\n"
" v_pk_add_f32 v[60:61], v[60:61], v[32:33]\n"
" v_pk_fma_f32 v[62:63], v[32:33], v[32:33], v[62:63]\n"
" v_pk_add_f32 v[60:61], v[60:61], v[34:35]\n"
" v_pk_fma_f32 v[62:63], v[34:35], v[34:35], v[62:63]\n"
" s_add_i32 s77, s68, s70\n"
" buffer_load_dword v229, v227, s[48:51], 0 offen sc1\n"
" v_cvt_pk_bf16_f32 v58, v36, v37\n"
" v_cvt_pk_bf16_f32 v59, v38, v39\n"
" v_pk_add_f32 v[36:37], v[36:37], v[64:65] op_sel_hi:[1,0]\n"
" v_pk_add_f32 v[38:39], v[38:39], v[64:65] op_sel_hi:[1,0]\n"
" v_pk_add_f32 v[60:61], v[60:61], v[36:37]\n"
" v_pk_fma_f32 v[62:63], v[36:37], v[36:37], v[62:63]\n"
" v_pk_add_f32 v[60:61], v[60:61], v[38:39]\n"
" v_pk_fma_f32 v[62:63], v[38:39], v[38:39], v[62:63]\n"
" v_permlane16_swap_b32 v56, v58\n"
" v_permlane16_swap_b32 v57, v59\n"
" v_add_f32 v60, v60, v61\n"
" v_add_f32 v62, v62, v63\n"
" s_nop 0\n"
" v_add_f32_dpp v60, v60, v60 row_ror:4 row_mask:0xf bank_mask:0xf\n"
" v_add_f32_dpp v62, v62, v62 row_ror:4 row_mask:0xf bank_mask:0xf\n"
" s_nop 0\n"
" v_add_f32_dpp v60, v60, v60 row_ror:8 row_mask:0xf bank_mask:0xf\n"
" v_add_f32_dpp v62, v62, v62 row_ror:8 row_mask:0xf bank_mask:0xf\n"
" v_mov_b32 v66, v60\n"
" v_mov_b32 v67, v62\n"
" s_nop 0\n"
" v_permlane16_swap_b32 v60, v66\n"
" v_permlane16_swap_b32 v62, v67\n"
" v_add_f32 v60, v60, v66\n"
" v_add_f32 v62, v62, v67\n"
" v_mov_b32 v66, v60\n"
" v_mov_b32 v67, v62\n"
" s_nop 0\n"
" v_permlane32_swap_b32 v60, v66\n"
" v_permlane32_swap_b32 v62, v67\n"
" v_add_f32 v60, v60, v66\n"
" v_add_f32 v62, v62, v67\n"
" v_mul_f32 v68, 0x3acccccd, v60\n"
" v_mul_f32 v66, v68, v68\n"
" v_fma_f32 v66, v62, s98, -v66\n"
" v_max_f32 v66, 0, v66\n"
" v_add_f32 v66, 0x3727c5ac, v66\n"
" v_rsq_f32 v70, v66\n"
" s_add_i32 s76, s95, 4096\n"
" v_mul_f32 v68, v68, v70\n"
" v_pk_fma_f32 v[0:1], v[0:1], v[70:71], v[68:69] op_sel_hi:[1,0,0] neg_lo:[0,0,1] neg_hi:[0,0,1]\n"
" v_pk_fma_f32 v[2:3], v[2:3], v[70:71], v[68:69] op_sel_hi:[1,0,0] neg_lo:[0,0,1] neg_hi:[0,0,1]\n"
" v_pk_fma_f32 v[4:5], v[4:5], v[70:71], v[68:69] op_sel_hi:[1,0,0] neg_lo:[0,0,1] neg_hi:[0,0,1]\n"
" v_pk_fma_f32 v[6:7], v[6:7], v[70:71], v[68:69] op_sel_hi:[1,0,0] neg_lo:[0,0,1] neg_hi:[0,0,1]\n"
" v_cvt_pk_bf16_f32 v72, v0, v1\n"
" v_cvt_pk_bf16_f32 v74, v4, v5\n"
" v_cvt_pk_bf16_f32 v73, v2, v3\n"
" v_cvt_pk_bf16_f32 v75, v6, v7\n"
" v_permlane16_swap_b32 v72, v74\n"
" s_nop 0\n"
" v_permlane16_swap_b32 v73, v75\n"
" buffer_store_dwordx4 v[72:75], v226, s[52:55], s95 offen offset:0 sc1\n"
" v_pk_fma_f32 v[8:9], v[8:9], v[70:71], v[68:69] op_sel_hi:[1,0,0] neg_lo:[0,0,1] neg_hi:[0,0,1]\n"
" v_pk_fma_f32 v[10:11], v[10:11], v[70:71], v[68:69] op_sel_hi:[1,0,0] neg_lo:[0,0,1] neg_hi:[0,0,1]\n"
" v_pk_fma_f32 v[12:13], v[12:13], v[70:71], v[68:69] op_sel_hi:[1,0,0] neg_lo:[0,0,1] neg_hi:[0,0,1]\n"
" v_pk_fma_f32 v[14:15], v[14:15], v[70:71], v[68:69] op_sel_hi:[1,0,0] neg_lo:[0,0,1] neg_hi:[0,0,1]\n"
" v_cvt_pk_bf16_f32 v76, v8, v9\n"
" v_cvt_pk_bf16_f32 v78, v12, v13\n"
" v_cvt_pk_bf16_f32 v77, v10, v11\n"
" v_cvt_pk_bf16_f32 v79, v14, v15\n"
" v_permlane16_swap_b32 v76, v78\n"
" s_nop 0\n"
" v_permlane16_swap_b32 v77, v79\n"
" buffer_store_dwordx4 v[76:79], v226, s[52:55], s95 offen offset:1024 sc1\n"
" v_pk_fma_f32 v[16:17], v[16:17], v[70:71], v[68:69] op_sel_hi:[1,0,0] neg_lo:[0,0,1] neg_hi:[0,0,1]\n"
" v_pk_fma_f32 v[18:19], v[18:19], v[70:71], v[68:69] op_sel_hi:[1,0,0] neg_lo:[0,0,1] neg_hi:[0,0,1]\n"
" v_pk_fma_f32 v[20:21], v[20:21], v[70:71], v[68:69] op_sel_hi:[1,0,0] neg_lo:[0,0,1] neg_hi:[0,0,1]\n"
" v_pk_fma_f32 v[22:23], v[22:23], v[70:71], v[68:69] op_sel_hi:[1,0,0] neg_lo:[0,0,1] neg_hi:[0,0,1]\n"
" v_cvt_pk_bf16_f32 v72, v16, v17\n"
" v_cvt_pk_bf16_f32 v74, v20, v21\n"
" v_cvt_pk_bf16_f32 v73, v18, v19\n"
" v_cvt_pk_bf16_f32 v75, v22, v23\n"
" v_permlane16_swap_b32 v72, v74\n"
" s_nop 0\n"
" v_permlane16_swap_b32 v73, v75\n"
" buffer_store_dwordx4 v[72:75], v226, s[52:55], s95 offen offset:2048 sc1\n"
" v_pk_fma_f32 v[24:25], v[24:25], v[70:71], v[68:69] op_sel_hi:[1,0,0] neg_lo:[0,0,1] neg_hi:[0,0,1]\n"
" v_pk_fma_f32 v[26:27], v[26:27], v[70:71], v[68:69] op_sel_hi:[1,0,0] neg_lo:[0,0,1] neg_hi:[0,0,1]\n"
" v_pk_fma_f32 v[28:29], v[28:29], v[70:71], v[68:69] op_sel_hi:[1,0,0] neg_lo:[0,0,1] neg_hi:[0,0,1]\n"
" v_pk_fma_f32 v[30:31], v[30:31], v[70:71], v[68:69] op_sel_hi:[1,0,0] neg_lo:[0,0,1] neg_hi:[0,0,1]\n"
" v_cvt_pk_bf16_f32 v76, v24, v25\n"
" v_cvt_pk_bf16_f32 v78, v28, v29\n"
" v_cvt_pk_bf16_f32 v77, v26, v27\n"
" v_cvt_pk_bf16_f32 v79, v30, v31\n"
" v_permlane16_swap_b32 v76, v78\n"
" s_nop 0\n"
" v_permlane16_swap_b32 v77, v79\n"
" buffer_store_dwordx4 v[76:79], v226, s[52:55], s95 offen offset:3072 sc1\n"
" v_pk_fma_f32 v[32:33], v[32:33], v[70:71], v[68:69] op_sel_hi:[1,0,0] neg_lo:[0,0,1] neg_hi:[0,0,1]\n"
" v_pk_fma_f32 v[34:35], v[34:35], v[70:71], v[68:69] op_sel_hi:[1,0,0] neg_lo:[0,0,1] neg_hi:[0,0,1]\n"
" v_pk_fma_f32 v[36:37], v[36:37], v[70:71], v[68:69] op_sel_hi:[1,0,0] neg_lo:[0,0,1] neg_hi:[0,0,1]\n"
" v_pk_fma_f32 v[38:39], v[38:39], v[70:71], v[68:69] op_sel_hi:[1,0,0] neg_lo:[0,0,1] neg_hi:[0,0,1]\n"
" v_cvt_pk_bf16_f32 v72, v32, v33\n"
" v_cvt_pk_bf16_f32 v74, v36, v37\n"
" v_cvt_pk_bf16_f32 v73, v34, v35\n"
" v_cvt_pk_bf16_f32 v75, v38, v39\n"
" v_permlane16_swap_b32 v72, v74\n"
" s_nop 0\n"
" v_permlane16_swap_b32 v73, v75\n"
" buffer_store_dwordx4 v[72:75], v226, s[52:55], s76 offen offset:0 sc1\n"
" s_add_i32 s76, s94, 4096\n"
" buffer_store_dwordx4 v[40:43], v226, s[40:43], s94 offen offset:0 sc1\n"
" buffer_store_dwordx4 v[44:47], v226, s[40:43], s94 offen offset:1024 sc1\n"
" buffer_store_dwordx4 v[48:51], v226, s[40:43], s94 offen offset:2048 sc1\n"
" buffer_store_dwordx4 v[52:55], v226, s[40:43], s94 offen offset:3072 sc1\n"
" buffer_store_dwordx4 v[56:59], v226, s[40:43], s76 offen offset:0 sc1\n"
" s_mov_b32 s72, 0\n"
" s_mov_b32 s71, s68\n"
" v_xor_b32 v228, 0x80, v228\n"
" s_waitcnt vmcnt(10)\n"
" v_cmp_gt_i32 vcc, s77, v229\n"
" s_nop 0\n"
" s_cbranch_vccnz .Lr2_nx_%=\n"
" s_and_b32 s92, s77, 15\n"
" s_lshl_b32 s92, s92, 21\n"
" s_add_i32 s92, s92, s85\n"
" s_lshr_b32 s76, s92, 21\n"
" s_bitcmp1_b64 s[86:87], s76\n"
" s_cselect_b32 s76, 8192, 0\n"
" s_add_i32 s92, s92, s76\n"
" s_add_i32 s78, s92, 0\n"
" buffer_load_dwordx4 a[0:3], v224, s[36:39], s78 offen offset:0 sc1\n"
" s_add_i32 s79, s92, 32768\n"
" buffer_load_dwordx4 a[4:7], v224, s[36:39], s79 offen offset:0 sc1\n"
" s_add_i32 s80, s92, 65536\n"
" buffer_load_dwordx4 a[8:11], v224, s[36:39], s80 offen offset:0 sc1\n"
" s_add_i32 s81, s92, 98304\n"
" buffer_load_dwordx4 a[12:15], v224, s[36:39], s81 offen offset:0 sc1\n"
" s_add_i32 s82, s92, 131072\n"
" buffer_load_dwordx4 a[16:19], v224, s[36:39], s82 offen offset:0 sc1\n"
" buffer_load_dwordx4 a[20:23], v224, s[36:39], s78 offen offset:512 sc1\n"
" buffer_load_dwordx4 a[24:27], v224, s[36:39], s79 offen offset:512 sc1\n"
" buffer_load_dwordx4 a[28:31], v224, s[36:39], s80 offen offset:512 sc1\n"
" buffer_load_dwordx4 a[32:35], v224, s[36:39], s81 offen offset:512 sc1\n"
" buffer_load_dwordx4 a[36:39], v224, s[36:39], s82 offen offset:512 sc1\n"
" buffer_load_dwordx4 a[40:43], v224, s[36:39], s78 offen offset:1024 sc1\n"
" buffer_load_dwordx4 a[44:47], v224, s[36:39], s79 offen offset:1024 sc1\n"
" buffer_load_dwordx4 a[48:51], v224, s[36:39], s80 offen offset:1024 sc1\n"
" buffer_load_dwordx4 a[52:55], v224, s[36:39], s81 offen offset:1024 sc1\n"
" buffer_load_dwordx4 a[56:59], v224, s[36:39], s82 offen offset:1024 sc1\n"
" buffer_load_dwordx4 a[60:63], v224, s[36:39], s78 offen offset:1536 sc1\n"
" buffer_load_dwordx4 a[64:67], v224, s[36:39], s79 offen offset:1536 sc1\n"
" buffer_load_dwordx4 a[68:71], v224, s[36:39], s80 offen offset:1536 sc1\n"
" buffer_load_dwordx4 a[72:75], v224, s[36:39], s81 offen offset:1536 sc1\n"
" buffer_load_dwordx4 a[76:79], v224, s[36:39], s82 offen offset:1536 sc1\n"
" buffer_load_dwordx4 a[80:83], v224, s[36:39], s78 offen offset:2048 sc1\n"
" buffer_load_dwordx4 a[84:87], v224, s[36:39], s79 offen offset:2048 sc1\n"
" buffer_load_dwordx4 a[88:91], v224, s[36:39], s80 offen offset:2048 sc1\n"
" buffer_load_dwordx4 a[92:95], v224, s[36:39], s81 offen offset:2048 sc1\n"
" buffer_load_dwordx4 a[96:99], v224, s[36:39], s82 offen offset:2048 sc1\n"
" buffer_load_dwordx4 a[100:103], v224, s[36:39], s78 offen offset:2560 sc1\n"
" buffer_load_dwordx4 a[104:107], v224, s[36:39], s79 offen offset:2560 sc1\n"
" buffer_load_dwordx4 a[108:111], v224, s[36:39], s80 offen offset:2560 sc1\n"
" buffer_load_dwordx4 a[112:115], v224, s[36:39], s81 offen offset:2560 sc1\n"
" buffer_load_dwordx4 a[116:119], v224, s[36:39], s82 offen offset:2560 sc1\n"
" buffer_load_dwordx4 a[120:123], v224, s[36:39], s78 offen offset:3072 sc1\n"
" buffer_load_dwordx4 a[124:127], v224, s[36:39], s79 offen offset:3072 sc1\n"
" buffer_load_dwordx4 a[128:131], v224, s[36:39], s80 offen offset:3072 sc1\n"
" buffer_load_dwordx4 a[132:135], v224, s[36:39], s81 offen offset:3072 sc1\n"
" buffer_load_dwordx4 a[136:139], v224, s[36:39], s82 offen offset:3072 sc1\n"
" buffer_load_dwordx4 a[140:143], v224, s[36:39], s78 offen offset:3584 sc1\n"
" buffer_load_dwordx4 a[144:147], v224, s[36:39], s79 offen offset:3584 sc1\n"
" buffer_load_dwordx4 a[148:151], v224, s[36:39], s80 offen offset:3584 sc1\n"
" buffer_load_dwordx4 a[152:155], v224, s[36:39], s81 offen offset:3584 sc1\n"
" buffer_load_dwordx4 a[156:159], v224, s[36:39], s82 offen offset:3584 sc1\n"
" s_add_i32 s78, s92, 4096\n"
" buffer_load_dwordx4 a[160:163], v224, s[36:39], s78 offen offset:0 sc1\n"
" s_add_i32 s79, s92, 36864\n"
" buffer_load_dwordx4 a[164:167], v224, s[36:39], s79 offen offset:0 sc1\n"
" s_add_i32 s80, s92, 69632\n"
" buffer_load_dwordx4 a[168:171], v224, s[36:39], s80 offen offset:0 sc1\n"
" s_add_i32 s81, s92, 102400\n"
" buffer_load_dwordx4 a[172:175], v224, s[36:39], s81 offen offset:0 sc1\n"
" s_add_i32 s82, s92, 135168\n"
" buffer_load_dwordx4 a[176:179], v224, s[36:39], s82 offen offset:0 sc1\n"
" buffer_load_dwordx4 a[180:183], v224, s[36:39], s78 offen offset:512 sc1\n"
" buffer_load_dwordx4 a[184:187], v224, s[36:39], s79 offen offset:512 sc1\n"
" buffer_load_dwordx4 a[188:191], v224, s[36:39], s80 offen offset:512 sc1\n"
" buffer_load_dwordx4 a[192:195], v224, s[36:39], s81 offen offset:512 sc1\n"
" buffer_load_dwordx4 a[196:199], v224, s[36:39], s82 offen offset:512 sc1\n"
" s_mov_b32 s72, 1\n"
" s_waitcnt vmcnt(50)\n"
" v_mov_b32 v66, s71\n"
" buffer_store_dword v66, v228, s[48:51], 0 offen sc1\n"
".Lr2_nx_%=:\n"
" s_mov_b32 s68, s77\n"
" s_cmp_lt_u32 s68, 64\n"
" s_cbranch_scc1 .Lr2_loop_%=\n"
" s_waitcnt vmcnt(0)\n"
" v_mov_b32 v66, s71\n"
" buffer_store_dword v66, v228, s[48:51], 0 offen sc1\n"
" s_waitcnt vmcnt(0)\n"
      RED_OPS);
  else asm volatile(
" s_nop 4\n"
" s_mov_b32 s90, 0\n"
" s_mov_b32 s72, 0\n"
" s_mov_b32 s71, -1\n"
" s_mov_b32 s91, 0\n"
".Lr3_pol_%=:\n"
" buffer_load_dwordx2 v[220:221], v229, s[48:51], 0 offen sc1\n"
" s_waitcnt vmcnt(0)\n"
" v_or_b32 v222, 1, v220\n"
" v_mov_b32 v223, v221\n"
" v_cmp_ne_u64 vcc, s[16:17], v[222:223]\n"
" s_nop 0\n"
" s_cbranch_vccz .Lr3_polok_%=\n"
" s_add_i32 s91, s91, 1\n"
" s_or_b32 s91, s91, s90\n"
" s_cmp_ge_u32 s91, 65536\n"
" s_cselect_b32 s90, 65536, 0\n"
" s_cbranch_scc1 .Lr3_polok_%=\n"
" s_sleep 4\n"
" s_branch .Lr3_pol_%=\n"
".Lr3_polok_%=:\n"
" v_and_b32 v220, 1, v220\n"
" v_cmp_ne_u32_e64 s[86:87], 1, v220\n"
" s_nop 1\n"
" s_bitcmp1_b32 s86, 0\n"
" s_cselect_b32 s76, 8192, 0\n"
" s_add_u32 s48, s48, s76\n"
".Lr3_loop_%=:\n"
" s_cmp_lg_u32 s72, 0\n"
" s_cbranch_scc1 .Lr3_have_%=\n"
" s_mov_b32 s91, 0\n"
" buffer_load_dword v229, v227, s[48:51], 0 offen sc1\n"
" s_waitcnt vmcnt(0)\n"
".Lr3_in_%=:\n"
" v_cmp_gt_i32 vcc, s68, v229\n"
" s_nop 0\n"
" s_cbranch_vccz .Lr3_inok_%=\n"
" s_add_i32 s91, s91, 1\n"
" s_or_b32 s91, s91, s90\n"
" s_cmp_ge_u32 s91, 65536\n"
" s_cselect_b32 s90, 65536, 0\n"
" s_cbranch_scc1 .Lr3_inok_%=\n"
" s_sleep 2\n"
" buffer_load_dword v229, v227, s[48:51], 0 offen sc1\n"
" s_waitcnt vmcnt(0)\n"
" s_branch .Lr3_in_%=\n"
".Lr3_inok_%=:\n"
" s_and_b32 s92, s68, 15\n"
" s_lshl_b32 s92, s92, 21\n"
" s_add_i32 s92, s92, s85\n"
" s_lshr_b32 s76, s92, 21\n"
" s_bitcmp1_b64 s[86:87], s76\n"
" s_cselect_b32 s76, 8192, 0\n"
" s_add_i32 s92, s92, s76\n"
" s_add_i32 s78, s92, 0\n"
" buffer_load_dwordx4 a[0:3], v224, s[36:39], s78 offen offset:0 sc1\n"
" s_add_i32 s79, s92, 32768\n"
" buffer_load_dwordx4 a[4:7], v224, s[36:39], s79 offen offset:0 sc1\n"
" s_add_i32 s80, s92, 65536\n"
" buffer_load_dwordx4 a[8:11], v224, s[36:39], s80 offen offset:0 sc1\n"
" s_add_i32 s81, s92, 98304\n"
" buffer_load_dwordx4 a[12:15], v224, s[36:39], s81 offen offset:0 sc1\n"
" s_add_i32 s82, s92, 131072\n"
" buffer_load_dwordx4 a[16:19], v224, s[36:39], s82 offen offset:0 sc1\n"
" buffer_load_dwordx4 a[20:23], v224, s[36:39], s78 offen offset:512 sc1\n"
" buffer_load_dwordx4 a[24:27], v224, s[36:39], s79 offen offset:512 sc1\n"
" buffer_load_dwordx4 a[28:31], v224, s[36:39], s80 offen offset:512 sc1\n"
" buffer_load_dwordx4 a[32:35], v224, s[36:39], s81 offen offset:512 sc1\n"
" buffer_load_dwordx4 a[36:39], v224, s[36:39], s82 offen offset:512 sc1\n"
" buffer_load_dwordx4 a[40:43], v224, s[36:39], s78 offen offset:1024 sc1\n"
" buffer_load_dwordx4 a[44:47], v224, s[36:39], s79 offen offset:1024 sc1\n"
" buffer_load_dwordx4 a[48:51], v224, s[36:39], s80 offen offset:1024 sc1\n"
" buffer_load_dwordx4 a[52:55], v224, s[36:39], s81 offen offset:1024 sc1\n"
" buffer_load_dwordx4 a[56:59], v224, s[36:39], s82 offen offset:1024 sc1\n"
" buffer_load_dwordx4 a[60:63], v224, s[36:39], s78 offen offset:1536 sc1\n"
" buffer_load_dwordx4 a[64:67], v224, s[36:39], s79 offen offset:1536 sc1\n"
" buffer_load_dwordx4 a[68:71], v224, s[36:39], s80 offen offset:1536 sc1\n"
" buffer_load_dwordx4 a[72:75], v224, s[36:39], s81 offen offset:1536 sc1\n"
" buffer_load_dwordx4 a[76:79], v224, s[36:39], s82 offen offset:1536 sc1\n"
" buffer_load_dwordx4 a[80:83], v224, s[36:39], s78 offen offset:2048 sc1\n"
" buffer_load_dwordx4 a[84:87], v224, s[36:39], s79 offen offset:2048 sc1\n"
" buffer_load_dwordx4 a[88:91], v224, s[36:39], s80 offen offset:2048 sc1\n"
" buffer_load_dwordx4 a[92:95], v224, s[36:39], s81 offen offset:2048 sc1\n"
" buffer_load_dwordx4 a[96:99], v224, s[36:39], s82 offen offset:2048 sc1\n"
" buffer_load_dwordx4 a[100:103], v224, s[36:39], s78 offen offset:2560 sc1\n"
" buffer_load_dwordx4 a[104:107], v224, s[36:39], s79 offen offset:2560 sc1\n"
" buffer_load_dwordx4 a[108:111], v224, s[36:39], s80 offen offset:2560 sc1\n"
" buffer_load_dwordx4 a[112:115], v224, s[36:39], s81 offen offset:2560 sc1\n"
" buffer_load_dwordx4 a[116:119], v224, s[36:39], s82 offen offset:2560 sc1\n"
" buffer_load_dwordx4 a[120:123], v224, s[36:39], s78 offen offset:3072 sc1\n"
" buffer_load_dwordx4 a[124:127], v224, s[36:39], s79 offen offset:3072 sc1\n"
" buffer_load_dwordx4 a[128:131], v224, s[36:39], s80 offen offset:3072 sc1\n"
" buffer_load_dwordx4 a[132:135], v224, s[36:39], s81 offen offset:3072 sc1\n"
" buffer_load_dwordx4 a[136:139], v224, s[36:39], s82 offen offset:3072 sc1\n"
" buffer_load_dwordx4 a[140:143], v224, s[36:39], s78 offen offset:3584 sc1\n"
" buffer_load_dwordx4 a[144:147], v224, s[36:39], s79 offen offset:3584 sc1\n"
" buffer_load_dwordx4 a[148:151], v224, s[36:39], s80 offen offset:3584 sc1\n"
" buffer_load_dwordx4 a[152:155], v224, s[36:39], s81 offen offset:3584 sc1\n"
" buffer_load_dwordx4 a[156:159], v224, s[36:39], s82 offen offset:3584 sc1\n"
" s_add_i32 s78, s92, 4096\n"
" buffer_load_dwordx4 a[160:163], v224, s[36:39], s78 offen offset:0 sc1\n"
" s_add_i32 s79, s92, 36864\n"
" buffer_load_dwordx4 a[164:167], v224, s[36:39], s79 offen offset:0 sc1\n"
" s_add_i32 s80, s92, 69632\n"
" buffer_load_dwordx4 a[168:171], v224, s[36:39], s80 offen offset:0 sc1\n"
" s_add_i32 s81, s92, 102400\n"
" buffer_load_dwordx4 a[172:175], v224, s[36:39], s81 offen offset:0 sc1\n"
" s_add_i32 s82, s92, 135168\n"
" buffer_load_dwordx4 a[176:179], v224, s[36:39], s82 offen offset:0 sc1\n"
" buffer_load_dwordx4 a[180:183], v224, s[36:39], s78 offen offset:512 sc1\n"
" buffer_load_dwordx4 a[184:187], v224, s[36:39], s79 offen offset:512 sc1\n"
" buffer_load_dwordx4 a[188:191], v224, s[36:39], s80 offen offset:512 sc1\n"
" buffer_load_dwordx4 a[192:195], v224, s[36:39], s81 offen offset:512 sc1\n"
" buffer_load_dwordx4 a[196:199], v224, s[36:39], s82 offen offset:512 sc1\n"
".Lr3_have_%=:\n"
" s_mul_i32 s94, s68, 40960\n"
" s_waitcnt vmcnt(49)\n"
" v_mfma_f32_16x16x32_bf16 v[0:3], v[232:235], a[0:3], 0\n"
" s_waitcnt vmcnt(48)\n"
" v_mfma_f32_16x16x32_bf16 v[0:3], v[232:235], a[4:7], v[0:3]\n"
" s_waitcnt vmcnt(47)\n"
" v_mfma_f32_16x16x32_bf16 v[0:3], v[232:235], a[8:11], v[0:3]\n"
" s_waitcnt vmcnt(46)\n"
" v_mfma_f32_16x16x32_bf16 v[0:3], v[232:235], a[12:15], v[0:3]\n"
" s_waitcnt vmcnt(45)\n"
" v_mfma_f32_16x16x32_bf16 v[0:3], v[232:235], a[16:19], v[0:3]\n"
" s_waitcnt vmcnt(44)\n"
" v_mfma_f32_16x16x32_bf16 v[4:7], v[232:235], a[20:23], 0\n"
" s_waitcnt vmcnt(43)\n"
" v_mfma_f32_16x16x32_bf16 v[4:7], v[232:235], a[24:27], v[4:7]\n"
" s_waitcnt vmcnt(42)\n"
" v_mfma_f32_16x16x32_bf16 v[4:7], v[232:235], a[28:31], v[4:7]\n"
" s_waitcnt vmcnt(41)\n"
" v_mfma_f32_16x16x32_bf16 v[4:7], v[232:235], a[32:35], v[4:7]\n"
" s_waitcnt vmcnt(40)\n"
" v_mfma_f32_16x16x32_bf16 v[4:7], v[232:235], a[36:39], v[4:7]\n"
" v_cvt_pk_bf16_f32 v40, v0, v1\n"
" v_cvt_pk_bf16_f32 v41, v2, v3\n"
" s_waitcnt vmcnt(39)\n"
" v_mfma_f32_16x16x32_bf16 v[8:11], v[232:235], a[40:43], 0\n"
" s_waitcnt vmcnt(38)\n"
" v_mfma_f32_16x16x32_bf16 v[8:11], v[232:235], a[44:47], v[8:11]\n"
" s_waitcnt vmcnt(37)\n"
" v_mfma_f32_16x16x32_bf16 v[8:11], v[232:235], a[48:51], v[8:11]\n"
" s_waitcnt vmcnt(36)\n"
" v_mfma_f32_16x16x32_bf16 v[8:11], v[232:235], a[52:55], v[8:11]\n"
" s_waitcnt vmcnt(35)\n"
" v_mfma_f32_16x16x32_bf16 v[8:11], v[232:235], a[56:59], v[8:11]\n"
" v_cvt_pk_bf16_f32 v42, v4, v5\n"
" v_cvt_pk_bf16_f32 v43, v6, v7\n"
" s_waitcnt vmcnt(34)\n"
" v_mfma_f32_16x16x32_bf16 v[12:15], v[232:235], a[60:63], 0\n"
" s_waitcnt vmcnt(33)\n"
" v_mfma_f32_16x16x32_bf16 v[12:15], v[232:235], a[64:67], v[12:15]\n"
" s_waitcnt vmcnt(32)\n"
" v_mfma_f32_16x16x32_bf16 v[12:15], v[232:235], a[68:71], v[12:15]\n"
" s_waitcnt vmcnt(31)\n"
" v_mfma_f32_16x16x32_bf16 v[12:15], v[232:235], a[72:75], v[12:15]\n"
" s_waitcnt vmcnt(30)\n"
" v_mfma_f32_16x16x32_bf16 v[12:15], v[232:235], a[76:79], v[12:15]\n"
" v_cvt_pk_bf16_f32 v44, v8, v9\n"
" v_cvt_pk_bf16_f32 v45, v10, v11\n"
" s_waitcnt vmcnt(29)\n"
" v_mfma_f32_16x16x32_bf16 v[16:19], v[232:235], a[80:83], 0\n"
" s_waitcnt vmcnt(28)\n"
" v_mfma_f32_16x16x32_bf16 v[16:19], v[232:235], a[84:87], v[16:19]\n"
" s_waitcnt vmcnt(27)\n"
" v_mfma_f32_16x16x32_bf16 v[16:19], v[232:235], a[88:91], v[16:19]\n"
" s_waitcnt vmcnt(26)\n"
" v_mfma_f32_16x16x32_bf16 v[16:19], v[232:235], a[92:95], v[16:19]\n"
" s_waitcnt vmcnt(25)\n"
" v_mfma_f32_16x16x32_bf16 v[16:19], v[232:235], a[96:99], v[16:19]\n"
" v_cvt_pk_bf16_f32 v46, v12, v13\n"
" v_cvt_pk_bf16_f32 v47, v14, v15\n"
" s_waitcnt vmcnt(24)\n"
" v_mfma_f32_16x16x32_bf16 v[20:23], v[232:235], a[100:103], 0\n"
" s_waitcnt vmcnt(23)\n"
" v_mfma_f32_16x16x32_bf16 v[20:23], v[232:235], a[104:107], v[20:23]\n"
" s_waitcnt vmcnt(22)\n"
" v_mfma_f32_16x16x32_bf16 v[20:23], v[232:235], a[108:111], v[20:23]\n"
" s_waitcnt vmcnt(21)\n"
" v_mfma_f32_16x16x32_bf16 v[20:23], v[232:235], a[112:115], v[20:23]\n"
" s_waitcnt vmcnt(20)\n"
" v_mfma_f32_16x16x32_bf16 v[20:23], v[232:235], a[116:119], v[20:23]\n"
" v_cvt_pk_bf16_f32 v48, v16, v17\n"
" v_cvt_pk_bf16_f32 v49, v18, v19\n"
" s_waitcnt vmcnt(19)\n"
" v_mfma_f32_16x16x32_bf16 v[24:27], v[232:235], a[120:123], 0\n"
" s_waitcnt vmcnt(18)\n"
" v_mfma_f32_16x16x32_bf16 v[24:27], v[232:235], a[124:127], v[24:27]\n"
" s_waitcnt vmcnt(17)\n"
" v_mfma_f32_16x16x32_bf16 v[24:27], v[232:235], a[128:131], v[24:27]\n"
" s_waitcnt vmcnt(16)\n"
" v_mfma_f32_16x16x32_bf16 v[24:27], v[232:235], a[132:135], v[24:27]\n"
" s_waitcnt vmcnt(15)\n"
" v_mfma_f32_16x16x32_bf16 v[24:27], v[232:235], a[136:139], v[24:27]\n"
" v_cvt_pk_bf16_f32 v50, v20, v21\n"
" v_cvt_pk_bf16_f32 v51, v22, v23\n"
" s_waitcnt vmcnt(14)\n"
" v_mfma_f32_16x16x32_bf16 v[28:31], v[232:235], a[140:143], 0\n"
" s_waitcnt vmcnt(13)\n"
" v_mfma_f32_16x16x32_bf16 v[28:31], v[232:235], a[144:147], v[28:31]\n"
" s_waitcnt vmcnt(12)\n"
" v_mfma_f32_16x16x32_bf16 v[28:31], v[232:235], a[148:151], v[28:31]\n"
" s_waitcnt vmcnt(11)\n"
" v_mfma_f32_16x16x32_bf16 v[28:31], v[232:235], a[152:155], v[28:31]\n"
" s_waitcnt vmcnt(10)\n"
" v_mfma_f32_16x16x32_bf16 v[28:31], v[232:235], a[156:159], v[28:31]\n"
" v_cvt_pk_bf16_f32 v52, v24, v25\n"
" v_cvt_pk_bf16_f32 v53, v26, v27\n"
" s_waitcnt vmcnt(9)\n"
" v_mfma_f32_16x16x32_bf16 v[32:35], v[232:235], a[160:163], 0\n"
" s_waitcnt vmcnt(8)\n"
" v_mfma_f32_16x16x32_bf16 v[32:35], v[232:235], a[164:167], v[32:35]\n"
" s_waitcnt vmcnt(7)\n"
" v_mfma_f32_16x16x32_bf16 v[32:35], v[232:235], a[168:171], v[32:35]\n"
" s_waitcnt vmcnt(6)\n"
" v_mfma_f32_16x16x32_bf16 v[32:35], v[232:235], a[172:175], v[32:35]\n"
" s_waitcnt vmcnt(5)\n"
" v_mfma_f32_16x16x32_bf16 v[32:35], v[232:235], a[176:179], v[32:35]\n"
" v_cvt_pk_bf16_f32 v54, v28, v29\n"
" v_cvt_pk_bf16_f32 v55, v30, v31\n"
" s_waitcnt vmcnt(4)\n"
" v_mfma_f32_16x16x32_bf16 v[36:39], v[232:235], a[180:183], 0\n"
" s_waitcnt vmcnt(3)\n"
" v_mfma_f32_16x16x32_bf16 v[36:39], v[232:235], a[184:187], v[36:39]\n"
" s_waitcnt vmcnt(2)\n"
" v_mfma_f32_16x16x32_bf16 v[36:39], v[232:235], a[188:191], v[36:39]\n"
" s_waitcnt vmcnt(1)\n"
" v_mfma_f32_16x16x32_bf16 v[36:39], v[232:235], a[192:195], v[36:39]\n"
" s_waitcnt vmcnt(0)\n"
" v_mfma_f32_16x16x32_bf16 v[36:39], v[232:235], a[196:199], v[36:39]\n"
" v_cvt_pk_bf16_f32 v56, v32, v33\n"
" v_cvt_pk_bf16_f32 v57, v34, v35\n"
" s_add_i32 s77, s68, s70\n"
" buffer_load_dword v229, v227, s[48:51], 0 offen sc1\n"
" s_nop 3\n"
" v_cvt_pk_bf16_f32 v58, v36, v37\n"
" v_cvt_pk_bf16_f32 v59, v38, v39\n"
" s_add_i32 s76, s68, 16\n"
" v_mov_b32 v66, s76\n"
" buffer_store_dword v66, v228, s[48:51], 0 offen sc1\n"
" buffer_store_dwordx2 v[40:41], v226, s[40:43], s94 offen offset:0\n"
" buffer_store_dwordx2 v[42:43], v226, s[40:43], s94 offen offset:128\n"
" buffer_store_dwordx2 v[44:45], v226, s[40:43], s94 offen offset:256\n"
" buffer_store_dwordx2 v[46:47], v226, s[40:43], s94 offen offset:384\n"
" buffer_store_dwordx2 v[48:49], v226, s[40:43], s94 offen offset:512\n"
" buffer_store_dwordx2 v[50:51], v226, s[40:43], s94 offen offset:640\n"
" buffer_store_dwordx2 v[52:53], v226, s[40:43], s94 offen offset:768\n"
" buffer_store_dwordx2 v[54:55], v226, s[40:43], s94 offen offset:896\n"
" buffer_store_dwordx2 v[56:57], v226, s[40:43], s94 offen offset:1024\n"
" buffer_store_dwordx2 v[58:59], v226, s[40:43], s94 offen offset:1152\n"
" s_mov_b32 s72, 0\n"
" s_mov_b32 s71, s68\n"
" s_waitcnt vmcnt(11)\n"
" v_cmp_gt_i32 vcc, s77, v229\n"
" s_nop 0\n"
" s_cbranch_vccnz .Lr3_nx_%=\n"
" s_and_b32 s92, s77, 15\n"
" s_lshl_b32 s92, s92, 21\n"
" s_add_i32 s92, s92, s85\n"
" s_lshr_b32 s76, s92, 21\n"
" s_bitcmp1_b64 s[86:87], s76\n"
" s_cselect_b32 s76, 8192, 0\n"
" s_add_i32 s92, s92, s76\n"
" s_add_i32 s78, s92, 0\n"
" buffer_load_dwordx4 a[0:3], v224, s[36:39], s78 offen offset:0 sc1\n"
" s_add_i32 s79, s92, 32768\n"
" buffer_load_dwordx4 a[4:7], v224, s[36:39], s79 offen offset:0 sc1\n"
" s_add_i32 s80, s92, 65536\n"
" buffer_load_dwordx4 a[8:11], v224, s[36:39], s80 offen offset:0 sc1\n"
" s_add_i32 s81, s92, 98304\n"
" buffer_load_dwordx4 a[12:15], v224, s[36:39], s81 offen offset:0 sc1\n"
" s_add_i32 s82, s92, 131072\n"
" buffer_load_dwordx4 a[16:19], v224, s[36:39], s82 offen offset:0 sc1\n"
" buffer_load_dwordx4 a[20:23], v224, s[36:39], s78 offen offset:512 sc1\n"
" buffer_load_dwordx4 a[24:27], v224, s[36:39], s79 offen offset:512 sc1\n"
" buffer_load_dwordx4 a[28:31], v224, s[36:39], s80 offen offset:512 sc1\n"
" buffer_load_dwordx4 a[32:35], v224, s[36:39], s81 offen offset:512 sc1\n"
" buffer_load_dwordx4 a[36:39], v224, s[36:39], s82 offen offset:512 sc1\n"
" buffer_load_dwordx4 a[40:43], v224, s[36:39], s78 offen offset:1024 sc1\n"
" buffer_load_dwordx4 a[44:47], v224, s[36:39], s79 offen offset:1024 sc1\n"
" buffer_load_dwordx4 a[48:51], v224, s[36:39], s80 offen offset:1024 sc1\n"
" buffer_load_dwordx4 a[52:55], v224, s[36:39], s81 offen offset:1024 sc1\n"
" buffer_load_dwordx4 a[56:59], v224, s[36:39], s82 offen offset:1024 sc1\n"
" buffer_load_dwordx4 a[60:63], v224, s[36:39], s78 offen offset:1536 sc1\n"
" buffer_load_dwordx4 a[64:67], v224, s[36:39], s79 offen offset:1536 sc1\n"
" buffer_load_dwordx4 a[68:71], v224, s[36:39], s80 offen offset:1536 sc1\n"
" buffer_load_dwordx4 a[72:75], v224, s[36:39], s81 offen offset:1536 sc1\n"
" buffer_load_dwordx4 a[76:79], v224, s[36:39], s82 offen offset:1536 sc1\n"
" buffer_load_dwordx4 a[80:83], v224, s[36:39], s78 offen offset:2048 sc1\n"
" buffer_load_dwordx4 a[84:87], v224, s[36:39], s79 offen offset:2048 sc1\n"
" buffer_load_dwordx4 a[88:91], v224, s[36:39], s80 offen offset:2048 sc1\n"
" buffer_load_dwordx4 a[92:95], v224, s[36:39], s81 offen offset:2048 sc1\n"
" buffer_load_dwordx4 a[96:99], v224, s[36:39], s82 offen offset:2048 sc1\n"
" buffer_load_dwordx4 a[100:103], v224, s[36:39], s78 offen offset:2560 sc1\n"
" buffer_load_dwordx4 a[104:107], v224, s[36:39], s79 offen offset:2560 sc1\n"
" buffer_load_dwordx4 a[108:111], v224, s[36:39], s80 offen offset:2560 sc1\n"
" buffer_load_dwordx4 a[112:115], v224, s[36:39], s81 offen offset:2560 sc1\n"
" buffer_load_dwordx4 a[116:119], v224, s[36:39], s82 offen offset:2560 sc1\n"
" buffer_load_dwordx4 a[120:123], v224, s[36:39], s78 offen offset:3072 sc1\n"
" buffer_load_dwordx4 a[124:127], v224, s[36:39], s79 offen offset:3072 sc1\n"
" buffer_load_dwordx4 a[128:131], v224, s[36:39], s80 offen offset:3072 sc1\n"
" buffer_load_dwordx4 a[132:135], v224, s[36:39], s81 offen offset:3072 sc1\n"
" buffer_load_dwordx4 a[136:139], v224, s[36:39], s82 offen offset:3072 sc1\n"
" buffer_load_dwordx4 a[140:143], v224, s[36:39], s78 offen offset:3584 sc1\n"
" buffer_load_dwordx4 a[144:147], v224, s[36:39], s79 offen offset:3584 sc1\n"
" buffer_load_dwordx4 a[148:151], v224, s[36:39], s80 offen offset:3584 sc1\n"
" buffer_load_dwordx4 a[152:155], v224, s[36:39], s81 offen offset:3584 sc1\n"
" buffer_load_dwordx4 a[156:159], v224, s[36:39], s82 offen offset:3584 sc1\n"
" s_add_i32 s78, s92, 4096\n"
" buffer_load_dwordx4 a[160:163], v224, s[36:39], s78 offen offset:0 sc1\n"
" s_add_i32 s79, s92, 36864\n"
" buffer_load_dwordx4 a[164:167], v224, s[36:39], s79 offen offset:0 sc1\n"
" s_add_i32 s80, s92, 69632\n"
" buffer_load_dwordx4 a[168:171], v224, s[36:39], s80 offen offset:0 sc1\n"
" s_add_i32 s81, s92, 102400\n"
" buffer_load_dwordx4 a[172:175], v224, s[36:39], s81 offen offset:0 sc1\n"
" s_add_i32 s82, s92, 135168\n"
" buffer_load_dwordx4 a[176:179], v224, s[36:39], s82 offen offset:0 sc1\n"
" buffer_load_dwordx4 a[180:183], v224, s[36:39], s78 offen offset:512 sc1\n"
" buffer_load_dwordx4 a[184:187], v224, s[36:39], s79 offen offset:512 sc1\n"
" buffer_load_dwordx4 a[188:191], v224, s[36:39], s80 offen offset:512 sc1\n"
" buffer_load_dwordx4 a[192:195], v224, s[36:39], s81 offen offset:512 sc1\n"
" buffer_load_dwordx4 a[196:199], v224, s[36:39], s82 offen offset:512 sc1\n"
" s_mov_b32 s72, 1\n"
".Lr3_nx_%=:\n"
" s_mov_b32 s68, s77\n"
" s_cmp_lt_u32 s68, 64\n"
" s_cbranch_scc1 .Lr3_loop_%=\n"
" v_mov_b32 v64, s16\n"
" v_mov_b32 v65, s17\n"
" v_mov_b32 v66, 64\n"
" buffer_store_dwordx2 v[64:65], v66, s[52:55], 0 offen sc1\n"
" s_waitcnt vmcnt(0)\n"
      RED_OPS);
  __builtin_amdgcn_endpgm();
}
__device__ __forceinline__ void stream_job(const Params& p, int w, int c) {
  {
    const int l16 = lane_id() * 16;
#pragma unroll
    for (int k = 0; k < 4; ++k) {
      const rsrc_t r = mkrsrc(k == 0 ? p.Wk : k == 1 ? p.Wv : k == 2 ? p.Wq : p.Wo, 640 * 8192);
#pragma unroll
      for (int i = 0; i < 20; ++i)
        asm volatile("s_mov_b32 m0, %0\n\ts_nop 0\n\tbuffer_load_dwordx4 %1, %2, %3 offen lds" :: "s"(w * 1024 + (i & 7) * 4096), "v"(l16), "s"(r), "s"(c * 81920 + (i * 4 + w) * 1024) : "m0", "memory");
      if (k & 1) asm volatile("s_waitcnt vmcnt(20)" ::: "memory");
    }
#pragma unroll
    for (int k = 0; k < 2; ++k) {
      const rsrc_t r = mkrsrc(k == 0 ? p.w2 : p.w1, 640 * 3456);
#pragma unroll
      for (int i = 0; i < 9; ++i)
        asm volatile("s_mov_b32 m0, %0\n\ts_nop 0\n\tbuffer_load_dwordx4 %1, %2, %3 offen lds" :: "s"(w * 1024 + (i & 7) * 4096), "v"(l16), "s"(r), "s"(c * 36864 + (i * 4 + w) * 1024) : "m0", "memory");
    }
    const rsrc_t r = mkrsrc(p.x, 2048 * 1280);
#pragma unroll
    for (int i = 0; i < 10; ++i)
      asm volatile("s_mov_b32 m0, %0\n\ts_nop 0\n\tbuffer_load_dwordx4 %1, %2, %3 offen lds" :: "s"(w * 1024 + (i & 7) * 4096), "v"(l16), "s"(r), "s"(c * 40960 + (i * 4 + w) * 1024) : "m0", "memory");
  }
  VM0();
  __syncthreads();
}
__device__ __forceinline__ unsigned probe_page(const Params& p, int page) {
  const rsrc_t r = mkrsrc(p.ws + (size_t)page * (2 << 20) + (1 << 20) + 49152 + 7168);
  unsigned best[2] = {~0u, ~0u};
  for (int rep = 0; rep < 6; ++rep) {
    const int q = rep & 1;
    st4(0u, r, 0, q * 8192);
    unsigned long long t0, t1;
    asm volatile("s_waitcnt vmcnt(0)\n\ts_memtime %0\n\ts_waitcnt lgkmcnt(0)" : "=s"(t0) :: "memory");
    unsigned v = ld4(r, 0, q * 8192);
    asm volatile("s_waitcnt vmcnt(0)\n\ts_memtime %0\n\ts_waitcnt lgkmcnt(0)" : "=s"(t1), "+v"(v) :: "memory");
    const unsigned d = (unsigned)(t1 - t0);
    if (q) best[1] = min(best[1], d); else best[0] = min(best[0], d);
  }
  return best[1] < best[0] ? 0u : 1u;
}
__device__ __forceinline__ void init_job(const Params& p, int w, int c) {
  const int lane = lane_id(), tid = w * 64 + lane;
  const rsrc_t rW = mkrsrc(p.ws);
  const v4u sent4 = {SENT, SENT, SENT, SENT};
  static_assert(OFF_MI + 4 * 65536 <= OFF_PROG + 64 * 11264 && OFF_PROG + 64 * 11264 <= OFF_CLAIM, "clear");
#pragma unroll
  for (int i = 0; i < 3; ++i) st16(sent4, rW, (i < 2 || tid < 192) ? (int)OFF_PROG + c * 11264 + i * 4096 + tid * 16 : OOB, 0);
#pragma unroll
  for (int i = 0; i < 10; ++i) st16(sent4, rW, (int)OFF_PR + c * 131072 + w * 16384 + (i / 5) * 8192 + 1536 + (i % 5) * 1024 + lane * 16, 0);
  unsigned pol = 0;
  if (w == 0 && c < NPAGE) pol = probe_page(p, c);
  VM0();
  __syncthreads();
  if (w == 0) st8(v2u{((unsigned)p.tag & ~1u) | pol, (unsigned)(p.tag >> 32)}, rW, lane == 0 ? (int)OFF_RDY + c * 128 : OOB, 0);
}

__device__ __forceinline__ Params args(unsigned long long tag, int role) {
  const Params __attribute__((address_space(4)))* k = (const Params __attribute__((address_space(4)))*)__builtin_amdgcn_kernarg_segment_ptr();
  asm volatile("" : "+s"(k));
  Params q;
  q.x = k->x; q.mem0 = k->mem0; q.Wq = k->Wq; q.Wk = k->Wk; q.Wv = k->Wv; q.Wo = k->Wo; q.w1 = k->w1; q.w2 = k->w2; q.out = k->out; q.ws = k->ws; q.dbg = k->dbg;
  q.tag = tag; q.mask = 0; q.role = role;
  return q;
}
#define PA args(p.tag, b)

__global__ void __launch_bounds__(256, 1) mega(Params p) {
  const int w = __builtin_amdgcn_readfirstlane(threadIdx.x >> 6);
  {
    unsigned long long pc;
    asm volatile("s_getpc_b64 %0" : "=s"(pc));
    const rsrc_t rc = mkrsrc((const void*)(pc & ~255ull), CODE_BYTES);
    asm volatile("s_mov_b32 m0, %0\n\ts_nop 0\n\tbuffer_load_dwordx4 %1, %2, %3 offen lds" :: "s"(4096 + w * 1024), "v"((int)threadIdx.x * 16), "s"(rc), "s"((int)(blockIdx.x >> 3) * 4096) : "m0", "memory");
  }
  extern __shared__ __attribute__((aligned(16))) int s_dyn[];
  if (threadIdx.x == 0) {
    const unsigned long long tag = (__hip_atomic_load((GAS unsigned long long*)(p.ws + OFF_CTL + 64), __ATOMIC_RELAXED, __HIP_MEMORY_SCOPE_AGENT) + 0x9E3779B97F4A7C16ull) | 1ull;
    int slot = (__builtin_amdgcn_s_getreg(20 | (3 << 11)) & 7) * 32 + ((blockIdx.x >> 3) & 31), n = 0;
    while (n < 256 && __hip_atomic_exchange((GAS unsigned long long*)(p.ws + OFF_CLAIM + slot * 64), tag, __ATOMIC_RELAXED, __HIP_MEMORY_SCOPE_AGENT) == tag) { slot = (slot + 1) & 255; ++n; }
    s_dyn[0] = n < 256 ? slot : -1; s_dyn[1] = (int)tag; s_dyn[2] = (int)(tag >> 32);
  }
  __syncthreads();
  int b = __builtin_amdgcn_readfirstlane(s_dyn[0]);
  p.tag = (unsigned long long)(unsigned)uni(s_dyn[1]) | ((unsigned long long)(unsigned)uni(s_dyn[2]) << 32);
  VM0();
  __syncthreads();
  if (b < 0) return;
  const int xc = (b >> 5) & 3;
  {
    const int x = b >> 5, t = b & 31;
    b = t < 16 ? (x < 4 ? x * 16 + t : 192 + t * 4 + (x - 4)) : 64 + ((x & 3) * 8 + ((t - 16) >> 1)) * 4 + (x >> 2) * 2 + (t & 1);
  }
  p.role = b;
  if (b >= 192) init_job(PA, w, b - 192);
  if (b < 64) { chain_role(PA, w, b); }
  else if (b < 192) {
    if (b & 2) stream_job(PA, w, ((b - 64) >> 2) * 2 + (b & 1));
    head_role(PA, w, b - 64, xc);
  }
  else if (b < 224) { wo_role(PA, w, b - 192); }
  else if (b < 224 + 2 * FW) { __builtin_amdgcn_s_sleep(DLY); ffn_role(PA, w, b - 224, xc); }
  else {
    if (b < 250 && w >= 2) return;
    const int k = (b - 250) * 4 + w, quad = b < 250 ? b - 242 : k / 3, kind = b < 250 ? w : 2 + k % 3;
    if (kind == 4) ln0_wave(PA, quad);
    else if (kind == 3) {
      red_role<3>(PA, w, 1, quad, 0, 1, xc);
    }
    else if (kind == 1) red_role<2>(PA, w, 0, quad, 0, 1, xc);
    else red_role<1>(PA, w, kind >> 1, quad, 0, 1, xc);
  }
}

static void launch_all(void* const* d_in, void* d_out, void* d_ws, void* dbg, int do_memset, long long mask, hipStream_t stream) {
  Params p;
  p.x = (const u8*)d_in[0]; p.mem0 = (const u8*)d_in[1]; p.Wq = (const u8*)d_in[2]; p.Wk = (const u8*)d_in[3]; p.Wv = (const u8*)d_in[4];
  p.Wo = (const u8*)d_in[5]; p.w1 = (const u8*)d_in[8]; p.w2 = (const u8*)d_in[9]; p.out = (u8*)d_out; p.ws = (u8*)(((size_t)d_ws + (2 << 20) - 1) & ~(size_t)((2 << 20) - 1)); p.dbg = (unsigned long long*)dbg; p.mask = (int)mask; p.role = 0; p.tag = 0;
  (void)hipFuncSetAttribute((const void*)mega, hipFuncAttributeMaxDynamicSharedMemorySize, LDS_BYTES);
  hipLaunchKernelGGL(mega, dim3(256), dim3(256), LDS_BYTES, stream, p);
}
extern "C" void kernel_launch(void* const* d_in, const int* in_sizes, int n_in, void* d_out, int out_size, void* d_ws, size_t ws_size, hipStream_t stream) {
  launch_all(d_in, d_out, d_ws, d_ws, 1, 63, stream);
}
```
